```python
import torch

def _softmax(x, axis=-1):
    return torch.softmax(x, dim=axis)
def _gelu(x):
    return torch.nn.functional.gelu(x, approximate="tanh")
def _silu(x):
    return torch.nn.functional.silu(x)
def _sigmoid(x):
    return torch.sigmoid(x)
def _layernorm(x, axis=-1, eps=1e-5):
    m = x.mean(dim=axis, keepdim=True)
    v = x.var(dim=axis, keepdim=True, unbiased=False)
    return (x - m) / torch.sqrt(v + eps)
def _scatter_add(tgt, idx, src, axis=0):
    return tgt.clone().index_add_(axis, idx, src)
def _segment_sum(src, seg, n):
    out = torch.zeros((n,) + src.shape[1:], dtype=src.dtype)
    return out.index_add_(0, seg, src)
def _pad(x, pw):
    flat = tuple(v for pair in reversed(pw) for v in pair)
    return torch.nn.functional.pad(x, flat)

kernel_name = 'recurrent_scan__sliding_block_cached__mixer_token_mlp__prompt'

def setup_inputs():

    _w = (lambda a: (lambda t: t.to(torch.bfloat16) if t.is_floating_point() else t)(torch.as_tensor(a)))
    def _A_setup():
        B, Sq, D = 1, 2048, 576
        Hq, Hkv, Sk, Dh = 32, 16, 2048, 128
        assert Hq % Hkv == 0, (Hq, Hkv)
        return {
            "x": _w((lambda *s: torch.randn(*s, dtype=torch.bfloat16) * 0.05)(B, Sq, D)),
            "k_cache": _w((lambda *s: torch.randn(*s, dtype=torch.bfloat16) * 3.2)(B, Hkv, Sk, Dh)),
            "v_cache": _w((lambda *s: torch.randn(*s, dtype=torch.bfloat16) * 3.2)(B, Hkv, Sk, Dh)),
            "wq": _w((lambda *s: torch.randn(*s, dtype=torch.bfloat16) * 3.2)(D, Hq * Dh) * (D ** -0.5)),
            "wo": _w((lambda *s: torch.randn(*s, dtype=torch.bfloat16) * 3.2)(Hq * Dh, D) * ((Hq * Dh) ** -0.5)),
        }
    _A_in = _A_setup()

    def _B_setup():
        B, S, D, F = 1, 2048, 576, 4608
        return {
            "x": _w((lambda *s: torch.randn(*s, dtype=torch.bfloat16) * 0.05)(B, S, D)),
            "w1": _w((lambda *s: torch.randn(*s, dtype=torch.bfloat16) * 3.2)(S, F) * (S ** -0.5)),
            "b1": _w((lambda *s: torch.randn(*s, dtype=torch.bfloat16) * 3.2)(F) * 0.02),
            "w2": _w((lambda *s: torch.randn(*s, dtype=torch.bfloat16) * 0.4)(F, S) * (F ** -0.5)),
            "b2": _w((lambda *s: torch.randn(*s, dtype=torch.bfloat16) * 0.4)(S) * 0.02),
        }
    _B_in = _B_setup()

    return {
        "A_x": _A_in["x"],
        "A_k_cache": _A_in["k_cache"],
        "A_v_cache": _A_in["v_cache"],
        "A_wq": _A_in["wq"],
        "A_wo": _A_in["wo"],
        "B_x": _B_in["x"],
        "B_w1": _B_in["w1"],
        "B_b1": _B_in["b1"],
        "B_w2": _B_in["w2"],
        "B_b2": _B_in["b2"],
    }

def reference(A_x, A_k_cache, A_v_cache, A_wq, A_wo, B_x, B_w1, B_b1, B_w2, B_b2):
    def _A_ref(x, k_cache, v_cache, wq, wo):
        B, Sq, D = x.shape
        Hq, Dh = 32, 128
        Hkv, Sk = k_cache.shape[1], k_cache.shape[2]
        W = min(4096, max(256, Sk // 4))
        rep = Hq // Hkv
        q = (lambda x, *d: x.permute(*d))((x @ wq).reshape(B, Sq, Hq, Dh), 0, 2, 1, 3)
        k = (lambda x, n, axis: torch.repeat_interleave(x, n, dim=axis))(k_cache[:, :, Sk - W:, :], rep, axis=1)
        v = (lambda x, n, axis: torch.repeat_interleave(x, n, dim=axis))(v_cache[:, :, Sk - W:, :], rep, axis=1)
        scores = torch.einsum("bhqd,bhkd->bhqk", q, k) * (Dh ** -0.5)
        p = _softmax(scores, axis=-1)
        o = torch.einsum("bhqk,bhkd->bhqd", p, v)
        return (lambda x, *d: x.permute(*d))(o, 0, 2, 1, 3).reshape(B, Sq, Hq * Dh) @ wo

    def _B_ref(x, w1, b1, w2, b2):
        B, S, D = x.shape
        h = _layernorm(x, axis=-1)
        u = torch.einsum("bsd,sf->bdf", h, w1) + b1
        u = _gelu(u)
        y = torch.einsum("bdf,fs->bds", u, w2) + b2
        return (lambda x, *d: x.permute(*d))(y, 0, 2, 1)

    x = A_x
    xn = _layernorm(x, axis=-1)
    s = _A_ref(xn, A_k_cache, A_v_cache, A_wq, A_wo)
    g = _silu(_B_ref(xn, B_w1, B_b1, B_w2, B_b2))
    return x + s * g
```

```cpp
#include <hip/hip_runtime.h>
#include <hip/hip_bf16.h>
typedef unsigned short u16;
__device__ __forceinline__ float bf2f(u16 v){ return __uint_as_float(((unsigned)v)<<16); }
__device__ __forceinline__ u16 f2bf(float f){ unsigned u=__float_as_uint(f); u += 0x7fffu + ((u>>16)&1u); return (u16)(u>>16); }
__device__ __forceinline__ float rbf(float f){ return bf2f(f2bf(f)); }
constexpr int S=2048, D=576, HQ=32, HKV=16, SK=2048, DH=128, W=512, F=4608;

__global__ void n_ln(const u16* x, u16* y){
  int r=blockIdx.x, t=threadIdx.x; float v[9]; float s=0;
  for(int i=0;i<9;i++){ v[i]=bf2f(x[r*D+t+64*i]); s+=v[i]; }
  for(int o=32;o;o>>=1) s+=__shfl_xor(s,o);
  float mean=s/D; float q=0;
  for(int i=0;i<9;i++){ float d=v[i]-mean; q+=d*d; }
  for(int o=32;o;o>>=1) q+=__shfl_xor(q,o);
  float m=rbf(mean), var=rbf(q/D); float den=rbf(sqrtf(rbf(var+1e-5f)));
  for(int i=0;i<9;i++) y[r*D+t+64*i]=f2bf(rbf(v[i]-m)/den);
}
__global__ void n_gemm(const u16* A,long sam,long sak,const u16* B,long sbk,long sbn,u16* C,long scm,long scn,int M,int N,int K,const u16* bias,int bias_on_n,int act){
  __shared__ float As[16][17], Bs[16][17];
  int tx=threadIdx.x, ty=threadIdx.y; int m=blockIdx.y*16+ty, n=blockIdx.x*16+tx; float acc=0;
  for(int k0=0;k0<K;k0+=16){
    As[ty][tx]=bf2f(A[(long)m*sam+(long)(k0+tx)*sak]);
    Bs[ty][tx]=bf2f(B[(long)(k0+ty)*sbk+(long)n*sbn]);
    __syncthreads();
    for(int k=0;k<16;k++) acc+=As[ty][k]*Bs[k][tx];
    __syncthreads();
  }
  float v=rbf(acc);
  if(bias){ v=rbf(v+bf2f(bias[n])); }
  if(act==1){ float t=tanhf(0.7978845608028654f*(v+0.044715f*v*v*v)); v=0.5f*v*(1.f+t); }
  if(act==2){ v=v/(1.f+expf(-v)); }
  C[(long)m*scm+(long)n*scn]=f2bf(v);
}
__global__ void n_attn(const u16* q,const u16* kc,const u16* vc,u16* o){
  int r=blockIdx.x,h=blockIdx.y,t=threadIdx.x; int kv=h/2;
  __shared__ float qs[DH], p[W], red[128];
  qs[t]=bf2f(q[(long)r*HQ*DH+h*DH+t]); __syncthreads();
  const u16* K=kc+((long)kv*SK+(SK-W))*DH; const u16* V=vc+((long)kv*SK+(SK-W))*DH;
  float mx=-1e30f;
  for(int j=t;j<W;j+=128){ float s=0; for(int d=0;d<DH;d++) s+=qs[d]*bf2f(K[j*DH+d]); s=rbf(rbf(s)*0.08838834764831845f); p[j]=s; mx=fmaxf(mx,s);}
  red[t]=mx; __syncthreads(); for(int o2=64;o2;o2>>=1){ if(t<o2) red[t]=fmaxf(red[t],red[t+o2]); __syncthreads(); } mx=red[0]; __syncthreads();
  float sm=0; for(int j=t;j<W;j+=128){ float e=expf(p[j]-mx); p[j]=e; sm+=e; }
  red[t]=sm; __syncthreads(); for(int o2=64;o2;o2>>=1){ if(t<o2) red[t]+=red[t+o2]; __syncthreads(); } sm=red[0]; __syncthreads();
  float acc=0; for(int j=0;j<W;j++) acc+=rbf(p[j]/sm)*bf2f(V[j*DH+t]);
  o[(long)r*HQ*DH+h*DH+t]=f2bf(acc);
}
__global__ void n_final(const u16* x,const u16* s,const u16* g,u16* out,int n){ int i=blockIdx.x*256+threadIdx.x; if(i<n) out[i]=f2bf(bf2f(x[i])+rbf(bf2f(s[i])*bf2f(g[i]))); }

extern "C" void kernel_launch(void* const* d_in, const int* in_sizes, int n_in, void* d_out, int out_size, void* d_ws, size_t ws_size, hipStream_t stream) {
  const u16* x=(const u16*)d_in[0]; const u16* kc=(const u16*)d_in[1]; const u16* vc=(const u16*)d_in[2];
  const u16* wq=(const u16*)d_in[3]; const u16* wo=(const u16*)d_in[4];
  const u16* w1=(const u16*)d_in[6]; const u16* b1=(const u16*)d_in[7]; const u16* w2=(const u16*)d_in[8]; const u16* b2=(const u16*)d_in[9];
  u16* ws=(u16*)d_ws; u16* xn=ws; u16* h=xn+S*D; u16* q=h+S*D; u16* o=q+(long)S*4096; u16* u=o+(long)S*4096; u16* s=u+(long)D*F; u16* g=s+S*D;
  n_ln<<<S,64,0,stream>>>(x,xn); n_ln<<<S,64,0,stream>>>(xn,h);
  dim3 b(16,16);
  n_gemm<<<dim3(4096/16,S/16),b,0,stream>>>(xn,D,1,wq,4096,1,q,4096,1,S,4096,D,nullptr,0,0);
  n_attn<<<dim3(S,HQ),128,0,stream>>>(q,kc,vc,o);
  n_gemm<<<dim3(D/16,S/16),b,0,stream>>>(o,4096,1,wo,D,1,s,D,1,S,D,4096,nullptr,0,0);
  n_gemm<<<dim3(F/16,D/16),b,0,stream>>>(h,1,D,w1,F,1,u,F,1,D,F,S,b1,1,1);
  n_gemm<<<dim3(S/16,D/16),b,0,stream>>>(u,F,1,w2,S,1,g,1,D,D,S,F,b2,1,2);
  n_final<<<(S*D+255)/256,256,0,stream>>>(x,s,g,(u16*)d_out,S*D);
}
```

```cpp
#include <hip/hip_runtime.h>
#include <hip/hip_bf16.h>
typedef unsigned short u16;
constexpr size_t WS_H = 360448u;
constexpr size_t WS_WOF = 2719744u;
constexpr size_t WS_U = 7438336u;
constexpr size_t WS_O = 12746752u;
constexpr size_t WS_PY = 29523968u;
constexpr size_t WS_PS = 38961152u;
constexpr size_t WS_DBG = 54706176u;
constexpr size_t WS_END = 55230464u;
__global__ __launch_bounds__(256,1) void fused_main(const void* x, const void* k, const void* v, const void* wq, const void* wo, const void* w1, const void* b1,
                                                     const void* w2, const void* b2, void* out, void* ws){
  __shared__ __attribute__((aligned(16))) char lds[163840];
  const void* ka=(const void*)__builtin_amdgcn_kernarg_segment_ptr();
  unsigned tid=threadIdx.x, bid=blockIdx.x, ldsb=(unsigned)(size_t)lds;
  asm volatile(
"L_entry:\n"
"s_load_dwordx2 s[56:57], %0, 80\n"
"s_load_dwordx2 s[32:33], %0, 0\n"
"s_load_dwordx2 s[36:37], %0, 24\n"
"s_load_dwordx2 s[40:41], %0, 8\n"
"s_load_dwordx2 s[44:45], %0, 16\n"
"s_load_dwordx2 s[48:49], %0, 40\n"
"s_load_dwordx2 s[52:53], %0, 56\n"
"s_load_dwordx2 s[60:61], %0, 72\n"
"s_load_dwordx2 s[64:65], %0, 48\n"
"s_load_dwordx2 s[68:69], %0, 64\n"
"s_load_dwordx2 s[72:73], %0, 32\n"
"v_readfirstlane_b32 s76, %2\n"
"s_mov_b32 s77, %1\n"
"v_mbcnt_lo_u32_b32 v224, -1, 0\n"
"v_mbcnt_hi_u32_b32 v224, -1, v224\n"
"s_lshr_b32 s76, s76, 6\n"
"s_getpc_b64 s[22:23]\n"
"L_pc:\n"
"s_sub_u32 s22, s22, L_pc-L_entry\n"
"s_subb_u32 s23, s23, 0\n"
"s_lshr_b32 s84, s77, 3\n"
"s_lshl_b32 s84, s84, 2\n"
"s_add_u32 s84, s84, s76\n"
"s_lshl_b32 s84, s84, 7\n"
"v_and_b32 v254, 3, v224\n"
"v_lshl_add_u32 v254, v254, 14, s84\n"
"v_min_u32 v254, L_end-L_entry-4, v254\n"
"global_load_dword v250, v254, s[22:23]\n"
"s_mov_b32 s58, 0x34ac000\n"
"s_mov_b32 s59, 0x20000\n"
"s_mov_b32 s34, 0x240000\n"
"s_mov_b32 s35, 0x20000\n"
"s_mov_b32 s38, 0x480000\n"
"s_mov_b32 s39, 0x20000\n"
"s_and_b32 s78, s77, 3\n"
"s_bfe_u32 s79, s77, 0x10002\n"
"s_lshr_b32 s80, s77, 3\n"
"s_and_b32 s81, s80, 7\n"
"s_lshl_b32 s19, s78, 3\n"
"s_add_u32 s19, s19, s81\n"
"s_lshr_b32 s81, s80, 3\n"
"s_lshl_b32 s20, s79, 2\n"
"s_add_u32 s20, s20, s81\n"
"s_and_b32 s81, s76, 1\n"
"s_lshl_b32 s81, s81, 12\n"
"s_lshr_b32 s82, s76, 1\n"
"s_lshl_b32 s82, s82, 10\n"
"s_add_u32 s85, s81, s82\n"
"s_lshl_b32 s89, s76, 12\n"
"v_lshrrev_b32 v251, 2, v224\n"
"v_mul_u32_u24 v251, 0x2000, v251\n"
"v_and_b32 v252, 3, v224\n"
"v_lshl_add_u32 v251, v252, 4, v251\n"
"s_lshr_b32 s82, s76, 1\n"
"s_mul_i32 s82, s82, 0x20000\n"
"s_and_b32 s83, s76, 1\n"
"s_lshl_b32 s83, s83, 7\n"
"s_add_u32 s82, s82, s83\n"
"v_add_u32 v229, s82, v251\n"
"s_lshl_b32 s81, s20, 8\n"
"s_lshl_b32 s82, s76, 6\n"
"s_add_u32 s10, s81, s82\n"
"s_mul_i32 s81, s10, 0x480\n"
"v_lshrrev_b32 v251, 2, v224\n"
"v_mul_u32_u24 v251, 0x480, v251\n"
"v_add_u32 v251, s81, v251\n"
"v_lshrrev_b32 v252, 5, v224\n"
"v_lshlrev_b32 v252, 1, v252\n"
"v_and_b32 v253, 3, v224\n"
"v_xor_b32 v252, v252, v253\n"
"v_lshl_add_u32 v225, v252, 4, v251\n"
"v_add_u32 v226, 0x4800, v225\n"
"v_add_u32 v227, 0x9000, v225\n"
"v_add_u32 v228, 0xd800, v225\n"
"s_mul_i32 s93, s78, 1\n"
"s_mul_i32 s81, s79, 4\n"
"s_add_u32 s93, s93, s81\n"
"s_lshl_b32 s81, s19, 8\n"
"s_waitcnt lgkmcnt(0)\n"
"s_add_u32 s36, s36, s81\n"
"s_addc_u32 s37, s37, 0\n"
"s_sub_u32 s38, s38, s81\n"
"s_lshl_b32 s90, s93, 19\n"
"s_add_u32 m0, s85, 0\n"
"v_mov_b32 v251, 0\n"
"buffer_load_dwordx4 v229, s[36:39], s90 offen lds\n"
"buffer_load_dword v140, v251, s[56:59], 0 offen sc1\n"
"s_lshr_b32 s81, s77, 2\n"
"s_addk_i32 m0, 1984\n"
"s_lshl_b32 s81, s81, 5\n"
"buffer_load_dwordx4 v229, s[36:39], s90 offen offset:64 lds\n"
"s_lshr_b32 s82, s76, 1\n"
"s_lshl_b32 s82, s82, 4\n"
"s_add_u32 s90, s90, 0x40000\n"
"s_addk_i32 m0, 6208\n"
"s_add_u32 s81, s81, s82\n"
"buffer_load_dwordx4 v229, s[36:39], s90 offen lds\n"
"s_lshl_b32 s82, s78, 2\n"
"s_add_u32 s81, s81, s82\n"
"s_addk_i32 m0, 1984\n"
"s_and_b32 s82, s76, 1\n"
"buffer_load_dwordx4 v229, s[36:39], s90 offen offset:64 lds\n"
"s_lshl_b32 s82, s82, 1\n"
"s_add_u32 s81, s81, s82\n"
"s_lshl_b32 s92, s93, 7\n"
"s_add_u32 m0, s89, 0x4000\n"
"v_lshrrev_b32 v251, 5, v224\n"
"buffer_load_dwordx4 v225, s[32:35], s92 offen lds\n"
"v_add_u32 v141, s81, v251\n"
"v_mul_u32_u24 v252, 0x480, v141\n"
"s_addk_i32 m0, 16320\n"
"v_and_b32 v253, 31, v224\n"
"buffer_load_dwordx4 v225, s[32:35], s92 offen offset:64 lds\n"
"v_mad_u32_u24 v252, v253, 12, v252\n"
"buffer_load_dwordx3 v[128:130], v252, s[32:35], 0 offen\n"
"s_addk_i32 m0, -15296\n"
"buffer_load_dwordx3 v[132:134], v252, s[32:35], 0 offen offset:384\n"
"buffer_load_dwordx4 v226, s[32:35], s92 offen lds\n"
"buffer_load_dwordx3 v[136:138], v252, s[32:35], 0 offen offset:768\n"
"s_lshl_b32 s86, s76, 10\n"
"s_addk_i32 m0, 16320\n"
"s_mov_b32 s96, 0x3f803f80\n"
"buffer_load_dwordx4 v226, s[32:35], s92 offen offset:64 lds\n"
"s_mov_b32 s14, 0xffff0000\n"
"v_lshrrev_b32 v251, 4, v224\n"
"s_addk_i32 m0, -15296\n"
"v_lshlrev_b32 v251, 9, v251\n"
"buffer_load_dwordx4 v227, s[32:35], s92 offen lds\n"
"v_bfe_u32 v252, v224, 2, 2\n"
"v_lshl_add_u32 v251, v252, 6, v251\n"
"s_addk_i32 m0, 16320\n"
"v_and_b32 v252, 3, v224\n"
"buffer_load_dwordx4 v227, s[32:35], s92 offen offset:64 lds\n"
"v_lshl_add_u32 v230, v252, 4, v251\n"
"s_lshl_b32 s81, s76, 11\n"
"s_addk_i32 m0, -15296\n"
"v_add_u32 v197, s81, v230\n"
"buffer_load_dwordx4 v228, s[32:35], s92 offen lds\n"
"v_and_b32 v251, 15, v224\n"
"v_lshlrev_b32 v251, 6, v251\n"
"s_addk_i32 m0, 16320\n"
"v_bfe_u32 v252, v224, 3, 1\n"
"buffer_load_dwordx4 v228, s[32:35], s92 offen offset:64 lds\n"
"v_lshlrev_b32 v252, 1, v252\n"
"v_lshrrev_b32 v253, 4, v224\n"
"s_add_u32 s93, s93, 1\n"
"s_cmp_eq_u32 s93, 9\n"
"s_cselect_b32 s93, 0, s93\n"
"s_lshl_b32 s90, s93, 19\n"
"s_add_u32 m0, s85, 0xc000\n"
"v_xor_b32 v252, v252, v253\n"
"buffer_load_dwordx4 v229, s[36:39], s90 offen lds\n"
"v_lshl_add_u32 v246, v252, 4, v251\n"
"v_add_u32 v192, s89, v246\n"
"s_addk_i32 m0, 1984\n"
"v_add_u32 v246, 0x18000, v246\n"
"buffer_load_dwordx4 v229, s[36:39], s90 offen offset:64 lds\n"
"v_add_u32 v193, 0xc000, v192\n"
"v_add_u32 v195, 0xc000, v230\n"
"s_add_u32 s90, s90, 0x40000\n"
"s_addk_i32 m0, 6208\n"
"v_add_u32 v198, 0xc000, v197\n"
"buffer_load_dwordx4 v229, s[36:39], s90 offen lds\n"
"v_add_u32 v194, 0x18000, v192\n"
"v_add_u32 v196, 0x18000, v230\n"
"s_addk_i32 m0, 1984\n"
"v_add_u32 v199, 0x18000, v197\n"
"buffer_load_dwordx4 v229, s[36:39], s90 offen offset:64 lds\n"
"v_accvgpr_write_b32 a224, s96\n"
"v_accvgpr_write_b32 a225, s96\n"
"s_lshl_b32 s92, s93, 7\n"
"s_add_u32 m0, s89, 0x10000\n"
"v_accvgpr_write_b32 a226, s96\n"
"buffer_load_dwordx4 v225, s[32:35], s92 offen lds\n"
"v_accvgpr_write_b32 a227, s96\n"
"v_lshlrev_b32 v235, 4, v224\n"
"s_addk_i32 m0, 16320\n"
"v_add_u32 v237, 0x20000, v235\n"
"buffer_load_dwordx4 v225, s[32:35], s92 offen offset:64 lds\n"
"s_addk_i32 m0, -15296\n"
"s_nop 0\n"
"buffer_load_dwordx4 v226, s[32:35], s92 offen lds\n"
"s_addk_i32 m0, 16320\n"
"s_nop 0\n"
"buffer_load_dwordx4 v226, s[32:35], s92 offen offset:64 lds\n"
"s_addk_i32 m0, -15296\n"
"s_nop 0\n"
"buffer_load_dwordx4 v227, s[32:35], s92 offen lds\n"
"s_addk_i32 m0, 16320\n"
"s_nop 0\n"
"buffer_load_dwordx4 v227, s[32:35], s92 offen offset:64 lds\n"
"s_addk_i32 m0, -15296\n"
"s_nop 0\n"
"buffer_load_dwordx4 v228, s[32:35], s92 offen lds\n"
"s_addk_i32 m0, 16320\n"
"s_nop 0\n"
"buffer_load_dwordx4 v228, s[32:35], s92 offen offset:64 lds\n"
"s_cmp_ge_u32 s80, 27\n"
"s_cbranch_scc1 L_idle\n"
"L_q:\n"
"s_waitcnt vmcnt(12)\n"
"s_barrier\n"
"s_add_u32 s93, s93, 1\n"
"s_cmp_eq_u32 s93, 9\n"
"s_cselect_b32 s93, 0, s93\n"
"s_lshl_b32 s90, s93, 19\n"
"s_add_u32 m0, s85, 0x18000\n"
"ds_read_b128 a[64:67], v192 offset:16384\n"
"buffer_load_dwordx4 v229, s[36:39], s90 offen lds\n"
"ds_read_b128 a[80:83], v192 offset:32768\n"
"ds_read_b128 a[68:71], v192 offset:17408\n"
"ds_read_b128 a[84:87], v192 offset:33792\n"
"ds_read_b128 a[72:75], v192 offset:18432\n"
"ds_read_b128 a[88:91], v192 offset:34816\n"
"ds_read_b128 a[76:79], v192 offset:19456\n"
"ds_read_b128 a[92:95], v192 offset:35840\n"
"s_addk_i32 m0, 1984\n"
"ds_read_b64_tr_b16 a[0:1], v230\n"
"buffer_load_dwordx4 v229, s[36:39], s90 offen offset:64 lds\n"
"ds_read_b64_tr_b16 a[2:3], v230 offset:256\n"
"ds_read_b64_tr_b16 a[32:33], v230 offset:8192\n"
"ds_read_b64_tr_b16 a[34:35], v230 offset:8448\n"
"ds_read_b64_tr_b16 a[4:5], v230 offset:8\n"
"ds_read_b64_tr_b16 a[6:7], v230 offset:264\n"
"ds_read_b64_tr_b16 a[36:37], v230 offset:8200\n"
"ds_read_b64_tr_b16 a[38:39], v230 offset:8456\n"
"s_add_u32 s90, s90, 0x40000\n"
"s_addk_i32 m0, 6208\n"
"ds_read_b64_tr_b16 a[8:9], v230 offset:2048\n"
"buffer_load_dwordx4 v229, s[36:39], s90 offen lds\n"
"ds_read_b64_tr_b16 a[10:11], v230 offset:2304\n"
"ds_read_b64_tr_b16 a[40:41], v230 offset:10240\n"
"ds_read_b64_tr_b16 a[42:43], v230 offset:10496\n"
"ds_read_b64_tr_b16 a[12:13], v230 offset:2056\n"
"ds_read_b64_tr_b16 a[14:15], v230 offset:2312\n"
"ds_read_b64_tr_b16 a[44:45], v230 offset:10248\n"
"ds_read_b64_tr_b16 a[46:47], v230 offset:10504\n"
"s_addk_i32 m0, 1984\n"
"ds_read_b64_tr_b16 a[16:17], v230 offset:4096\n"
"buffer_load_dwordx4 v229, s[36:39], s90 offen offset:64 lds\n"
"ds_read_b64_tr_b16 a[18:19], v230 offset:4352\n"
"ds_read_b64_tr_b16 a[48:49], v230 offset:12288\n"
"ds_read_b64_tr_b16 a[50:51], v230 offset:12544\n"
"ds_read_b64_tr_b16 a[20:21], v230 offset:4104\n"
"ds_read_b64_tr_b16 a[22:23], v230 offset:4360\n"
"ds_read_b64_tr_b16 a[52:53], v230 offset:12296\n"
"ds_read_b64_tr_b16 a[54:55], v230 offset:12552\n"
"s_lshl_b32 s92, s93, 7\n"
"s_add_u32 m0, s89, 0x1c000\n"
"ds_read_b64_tr_b16 a[24:25], v230 offset:6144\n"
"buffer_load_dwordx4 v225, s[32:35], s92 offen lds\n"
"ds_read_b64_tr_b16 a[26:27], v230 offset:6400\n"
"ds_read_b64_tr_b16 a[56:57], v230 offset:14336\n"
"ds_read_b64_tr_b16 a[58:59], v230 offset:14592\n"
"ds_read_b64_tr_b16 a[28:29], v230 offset:6152\n"
"ds_read_b64_tr_b16 a[30:31], v230 offset:6408\n"
"ds_read_b64_tr_b16 a[60:61], v230 offset:14344\n"
"ds_read_b64_tr_b16 a[62:63], v230 offset:14600\n"
"s_addk_i32 m0, 16320\n"
"ds_read_b64_tr_b16 a[96:97], v197\n"
"buffer_load_dwordx4 v225, s[32:35], s92 offen offset:64 lds\n"
"ds_read_b64_tr_b16 a[98:99], v197 offset:256\n"
"ds_read_b64_tr_b16 a[104:105], v197 offset:8192\n"
"ds_read_b64_tr_b16 a[106:107], v197 offset:8448\n"
"ds_read_b64_tr_b16 a[100:101], v197 offset:8\n"
"ds_read_b64_tr_b16 a[102:103], v197 offset:264\n"
"ds_read_b64_tr_b16 a[108:109], v197 offset:8200\n"
"ds_read_b64_tr_b16 a[110:111], v197 offset:8456\n"
"s_addk_i32 m0, -15296\n"
"s_nop 0\n"
"buffer_load_dwordx4 v226, s[32:35], s92 offen lds\n"
"s_addk_i32 m0, 16320\n"
"s_nop 0\n"
"buffer_load_dwordx4 v226, s[32:35], s92 offen offset:64 lds\n"
"s_addk_i32 m0, -15296\n"
"s_nop 0\n"
"buffer_load_dwordx4 v227, s[32:35], s92 offen lds\n"
"s_addk_i32 m0, 16320\n"
"s_nop 0\n"
"buffer_load_dwordx4 v227, s[32:35], s92 offen offset:64 lds\n"
"s_addk_i32 m0, -15296\n"
"s_nop 0\n"
"buffer_load_dwordx4 v228, s[32:35], s92 offen lds\n"
"s_addk_i32 m0, 16320\n"
"s_nop 0\n"
"buffer_load_dwordx4 v228, s[32:35], s92 offen offset:64 lds\n"
"s_waitcnt vmcnt(12) lgkmcnt(0)\n"
"s_barrier\n"
"v_mfma_f32_16x16x32_bf16 v[0:3], a[0:3], a[64:67], 0\n"
"s_add_u32 s93, s93, 1\n"
"s_cmp_eq_u32 s93, 9\n"
"s_cselect_b32 s93, 0, s93\n"
"s_lshl_b32 s90, s93, 19\n"
"v_mfma_f32_16x16x32_bf16 v[0:3], a[32:35], a[80:83], v[0:3]\n"
"s_add_u32 m0, s85, 0\n"
"ds_read_b128 a[176:179], v193 offset:16384\n"
"v_mfma_f32_16x16x32_bf16 v[32:35], a[0:3], a[68:71], 0\n"
"s_waitcnt vmcnt(29)\n"
"buffer_load_dwordx4 v229, s[36:39], s90 offen lds\n"
"ds_read_b128 a[192:195], v193 offset:32768\n"
"v_mfma_f32_16x16x32_bf16 v[32:35], a[32:35], a[84:87], v[32:35]\n"
"s_addk_i32 m0, 1984\n"
"ds_read_b128 a[180:183], v193 offset:17408\n"
"v_readfirstlane_b32 s95, v140\n"
"v_mfma_f32_16x16x32_bf16 v[64:67], a[0:3], a[72:75], 0\n"
"s_mov_b32 s42, 0x20000\n"
"ds_read_b128 a[196:199], v193 offset:33792\n"
"v_lshlrev_b32 v142, 16, v128\n"
"v_mfma_f32_16x16x32_bf16 v[64:67], a[32:35], a[88:91], v[64:67]\n"
"ds_read_b128 a[184:187], v193 offset:18432\n"
"v_and_b32 v143, s14, v128\n"
"v_mfma_f32_16x16x32_bf16 v[96:99], a[0:3], a[76:79], 0\n"
"ds_read_b128 a[200:203], v193 offset:34816\n"
"v_lshlrev_b32 v144, 16, v129\n"
"v_mfma_f32_16x16x32_bf16 v[96:99], a[32:35], a[92:95], v[96:99]\n"
"ds_read_b128 a[188:191], v193 offset:19456\n"
"v_and_b32 v145, s14, v129\n"
"v_mfma_f32_16x16x32_bf16 v[100:103], a[4:7], a[76:79], 0\n"
"buffer_load_dwordx4 v229, s[36:39], s90 offen offset:64 lds\n"
"v_mfma_f32_16x16x32_bf16 v[100:103], a[36:39], a[92:95], v[100:103]\n"
"s_add_u32 s90, s90, 0x40000\n"
"ds_read_b128 a[204:207], v193 offset:35840\n"
"v_lshlrev_b32 v146, 16, v130\n"
"v_mfma_f32_16x16x32_bf16 v[68:71], a[4:7], a[72:75], 0\n"
"s_addk_i32 m0, 6208\n"
"ds_read_b64_tr_b16 a[112:113], v195\n"
"v_and_b32 v147, s14, v130\n"
"v_mfma_f32_16x16x32_bf16 v[68:71], a[36:39], a[88:91], v[68:71]\n"
"s_mov_b32 s43, 0x20000\n"
"ds_read_b64_tr_b16 a[114:115], v195 offset:256\n"
"v_lshlrev_b32 v148, 16, v132\n"
"v_mfma_f32_16x16x32_bf16 v[36:39], a[4:7], a[68:71], 0\n"
"ds_read_b64_tr_b16 a[144:145], v195 offset:8192\n"
"v_and_b32 v149, s14, v132\n"
"v_mfma_f32_16x16x32_bf16 v[36:39], a[36:39], a[84:87], v[36:39]\n"
"buffer_load_dwordx4 v229, s[36:39], s90 offen lds\n"
"v_lshlrev_b32 v150, 16, v133\n"
"v_mfma_f32_16x16x32_bf16 v[4:7], a[4:7], a[64:67], 0\n"
"s_addk_i32 m0, 1984\n"
"ds_read_b64_tr_b16 a[146:147], v195 offset:8448\n"
"v_and_b32 v151, s14, v133\n"
"v_lshlrev_b32 v152, 16, v134\n"
"v_mfma_f32_16x16x32_bf16 v[4:7], a[36:39], a[80:83], v[4:7]\n"
"ds_read_b64_tr_b16 a[116:117], v195 offset:8\n"
"v_and_b32 v153, s14, v134\n"
"v_mfma_f32_16x16x32_bf16 v[8:11], a[8:11], a[64:67], 0\n"
"s_mov_b32 s46, 0x22000\n"
"ds_read_b64_tr_b16 a[118:119], v195 offset:264\n"
"v_lshlrev_b32 v154, 16, v136\n"
"v_mfma_f32_16x16x32_bf16 v[8:11], a[40:43], a[80:83], v[8:11]\n"
"ds_read_b64_tr_b16 a[148:149], v195 offset:8200\n"
"v_and_b32 v155, s14, v136\n"
"v_lshlrev_b32 v156, 16, v137\n"
"v_mfma_f32_16x16x32_bf16 v[40:43], a[8:11], a[68:71], 0\n"
"v_and_b32 v157, s14, v137\n"
"v_lshlrev_b32 v158, 16, v138\n"
"v_mfma_f32_16x16x32_bf16 v[40:43], a[40:43], a[84:87], v[40:43]\n"
"buffer_load_dwordx4 v229, s[36:39], s90 offen offset:64 lds\n"
"v_mfma_f32_16x16x32_bf16 v[72:75], a[8:11], a[72:75], 0\n"
"s_lshl_b32 s92, s93, 7\n"
"ds_read_b64_tr_b16 a[150:151], v195 offset:8456\n"
"v_and_b32 v159, s14, v138\n"
"v_mfma_f32_16x16x32_bf16 v[72:75], a[40:43], a[88:91], v[72:75]\n"
"s_add_u32 m0, s89, 0x4000\n"
"ds_read_b64_tr_b16 a[120:121], v195 offset:2048\n"
"v_mfma_f32_16x16x32_bf16 v[104:107], a[8:11], a[76:79], 0\n"
"s_add_u32 s95, s95, 1\n"
"ds_read_b64_tr_b16 a[122:123], v195 offset:2304\n"
"v_add_f32 v160, v142, v144\n"
"v_add_f32 v161, v143, v145\n"
"v_mfma_f32_16x16x32_bf16 v[104:107], a[40:43], a[92:95], v[104:107]\n"
"ds_read_b64_tr_b16 a[152:153], v195 offset:10240\n"
"v_add_f32 v160, v160, v146\n"
"v_mfma_f32_16x16x32_bf16 v[108:111], a[12:15], a[76:79], 0\n"
"s_mov_b32 s47, 0x20000\n"
"ds_read_b64_tr_b16 a[154:155], v195 offset:10496\n"
"v_add_f32 v161, v161, v147\n"
"v_mfma_f32_16x16x32_bf16 v[108:111], a[44:47], a[92:95], v[108:111]\n"
"buffer_load_dwordx4 v225, s[32:35], s92 offen lds\n"
"v_add_f32 v160, v160, v148\n"
"v_mfma_f32_16x16x32_bf16 v[76:79], a[12:15], a[72:75], 0\n"
"s_addk_i32 m0, 16320\n"
"ds_read_b64_tr_b16 a[124:125], v195 offset:2056\n"
"v_add_f32 v161, v161, v149\n"
"v_add_f32 v160, v160, v150\n"
"v_mfma_f32_16x16x32_bf16 v[76:79], a[44:47], a[88:91], v[76:79]\n"
"ds_read_b64_tr_b16 a[126:127], v195 offset:2312\n"
"v_add_f32 v161, v161, v151\n"
"v_mfma_f32_16x16x32_bf16 v[44:47], a[12:15], a[68:71], 0\n"
"ds_read_b64_tr_b16 a[156:157], v195 offset:10248\n"
"v_add_f32 v160, v160, v152\n"
"v_mfma_f32_16x16x32_bf16 v[44:47], a[44:47], a[84:87], v[44:47]\n"
"ds_read_b64_tr_b16 a[158:159], v195 offset:10504\n"
"v_add_f32 v161, v161, v153\n"
"v_add_f32 v160, v160, v154\n"
"v_mfma_f32_16x16x32_bf16 v[12:15], a[12:15], a[64:67], 0\n"
"buffer_load_dwordx4 v225, s[32:35], s92 offen offset:64 lds\n"
"v_add_f32 v161, v161, v155\n"
"v_mfma_f32_16x16x32_bf16 v[12:15], a[44:47], a[80:83], v[12:15]\n"
"s_addk_i32 m0, -15296\n"
"ds_read_b64_tr_b16 a[128:129], v195 offset:4096\n"
"v_add_f32 v160, v160, v156\n"
"v_mfma_f32_16x16x32_bf16 v[16:19], a[16:19], a[64:67], 0\n"
"s_mov_b32 s50, 0x1200000\n"
"ds_read_b64_tr_b16 a[130:131], v195 offset:4352\n"
"v_add_f32 v161, v161, v157\n"
"v_mfma_f32_16x16x32_bf16 v[16:19], a[48:51], a[80:83], v[16:19]\n"
"ds_read_b64_tr_b16 a[160:161], v195 offset:12288\n"
"v_add_f32 v160, v160, v158\n"
"v_mfma_f32_16x16x32_bf16 v[48:51], a[16:19], a[68:71], 0\n"
"ds_read_b64_tr_b16 a[162:163], v195 offset:12544\n"
"v_add_f32 v161, v161, v159\n"
"v_add_f32 v160, v160, v161\n"
"v_mfma_f32_16x16x32_bf16 v[48:51], a[48:51], a[84:87], v[48:51]\n"
"ds_read_b64_tr_b16 a[132:133], v195 offset:4104\n"
"v_add_f32 v160, v160, v160 row_ror:8 row_mask:0xf bank_mask:0xf\n"
"v_mfma_f32_16x16x32_bf16 v[80:83], a[16:19], a[72:75], 0\n"
"buffer_load_dwordx4 v226, s[32:35], s92 offen lds\n"
"v_add_f32 v160, v160, v160 row_ror:4 row_mask:0xf bank_mask:0xf\n"
"v_mfma_f32_16x16x32_bf16 v[80:83], a[48:51], a[88:91], v[80:83]\n"
"s_addk_i32 m0, 16320\n"
"ds_read_b64_tr_b16 a[134:135], v195 offset:4360\n"
"v_add_f32 v160, v160, v160 row_ror:2 row_mask:0xf bank_mask:0xf\n"
"v_mfma_f32_16x16x32_bf16 v[112:115], a[16:19], a[76:79], 0\n"
"ds_read_b64_tr_b16 a[164:165], v195 offset:12296\n"
"v_add_f32 v160, v160, v160 row_ror:1 row_mask:0xf bank_mask:0xf\n"
"v_mov_b32 v166, v160\n"
"v_mfma_f32_16x16x32_bf16 v[112:115], a[48:51], a[92:95], v[112:115]\n"
"s_mov_b32 s51, 0x20000\n"
"ds_read_b64_tr_b16 a[166:167], v195 offset:12552\n"
"v_permlane16_swap_b32 v160, v166\n"
"v_mfma_f32_16x16x32_bf16 v[116:119], a[20:23], a[76:79], 0\n"
"ds_read_b64_tr_b16 a[136:137], v195 offset:6144\n"
"v_add_f32 v160, v160, v166\n"
"v_mfma_f32_16x16x32_bf16 v[116:119], a[52:55], a[92:95], v[116:119]\n"
"buffer_load_dwordx4 v226, s[32:35], s92 offen offset:64 lds\n"
"v_mul_f32 v160, 0xbae38e39, v160\n"
"v_mfma_f32_16x16x32_bf16 v[84:87], a[20:23], a[72:75], 0\n"
"s_addk_i32 m0, -15296\n"
"ds_read_b64_tr_b16 a[138:139], v195 offset:6400\n"
"v_add_f32 v142, v142, v160\n"
"v_add_f32 v143, v143, v160\n"
"v_mfma_f32_16x16x32_bf16 v[84:87], a[52:55], a[88:91], v[84:87]\n"
"ds_read_b64_tr_b16 a[168:169], v195 offset:14336\n"
"v_add_f32 v144, v144, v160\n"
"v_mfma_f32_16x16x32_bf16 v[52:55], a[20:23], a[68:71], 0\n"
"ds_read_b64_tr_b16 a[170:171], v195 offset:14592\n"
"v_add_f32 v145, v145, v160\n"
"v_mfma_f32_16x16x32_bf16 v[52:55], a[52:55], a[84:87], v[52:55]\n"
"ds_read_b64_tr_b16 a[140:141], v195 offset:6152\n"
"v_add_f32 v146, v146, v160\n"
"v_mfma_f32_16x16x32_bf16 v[20:23], a[20:23], a[64:67], 0\n"
"s_mov_b32 s54, 0x1200000\n"
"v_add_f32 v147, v147, v160\n"
"v_add_f32 v148, v148, v160\n"
"v_mfma_f32_16x16x32_bf16 v[20:23], a[52:55], a[80:83], v[20:23]\n"
"buffer_load_dwordx4 v227, s[32:35], s92 offen lds\n"
"v_add_f32 v149, v149, v160\n"
"v_mfma_f32_16x16x32_bf16 v[24:27], a[24:27], a[64:67], 0\n"
"s_addk_i32 m0, 16320\n"
"ds_read_b64_tr_b16 a[142:143], v195 offset:6408\n"
"v_add_f32 v150, v150, v160\n"
"v_mfma_f32_16x16x32_bf16 v[24:27], a[56:59], a[80:83], v[24:27]\n"
"ds_read_b64_tr_b16 a[172:173], v195 offset:14344\n"
"v_add_f32 v151, v151, v160\n"
"v_mfma_f32_16x16x32_bf16 v[56:59], a[24:27], a[68:71], 0\n"
"ds_read_b64_tr_b16 a[174:175], v195 offset:14600\n"
"v_add_f32 v152, v152, v160\n"
"v_add_f32 v153, v153, v160\n"
"v_mfma_f32_16x16x32_bf16 v[56:59], a[56:59], a[84:87], v[56:59]\n"
"ds_read_b64_tr_b16 a[208:209], v198\n"
"v_add_f32 v154, v154, v160\n"
"v_mfma_f32_16x16x32_bf16 v[88:91], a[24:27], a[72:75], 0\n"
"ds_read_b64_tr_b16 a[210:211], v198 offset:256\n"
"v_add_f32 v155, v155, v160\n"
"v_mfma_f32_16x16x32_bf16 v[88:91], a[56:59], a[88:91], v[88:91]\n"
"s_mov_b32 s55, 0x20000\n"
"buffer_load_dwordx4 v227, s[32:35], s92 offen offset:64 lds\n"
"v_add_f32 v156, v156, v160\n"
"v_mfma_f32_16x16x32_bf16 v[120:123], a[24:27], a[76:79], 0\n"
"s_addk_i32 m0, -15296\n"
"ds_read_b64_tr_b16 a[216:217], v198 offset:8192\n"
"v_add_f32 v157, v157, v160\n"
"v_add_f32 v158, v158, v160\n"
"v_mfma_f32_16x16x32_bf16 v[120:123], a[56:59], a[92:95], v[120:123]\n"
"ds_read_b64_tr_b16 a[218:219], v198 offset:8448\n"
"v_add_f32 v159, v159, v160\n"
"v_mfma_f32_16x16x32_bf16 v[124:127], a[28:31], a[76:79], 0\n"
"ds_read_b64_tr_b16 a[212:213], v198 offset:8\n"
"v_mul_f32 v162, v142, v142\n"
"v_mfma_f32_16x16x32_bf16 v[124:127], a[60:63], a[92:95], v[124:127]\n"
"ds_read_b64_tr_b16 a[214:215], v198 offset:264\n"
"v_mul_f32 v163, v143, v143\n"
"v_mfma_f32_16x16x32_bf16 v[92:95], a[28:31], a[72:75], 0\n"
"buffer_load_dwordx4 v228, s[32:35], s92 offen lds\n"
"v_fmac_f32 v162, v144, v144\n"
"v_mfma_f32_16x16x32_bf16 v[92:95], a[60:63], a[88:91], v[92:95]\n"
"s_addk_i32 m0, 16320\n"
"ds_read_b64_tr_b16 a[220:221], v198 offset:8200\n"
"v_fmac_f32 v163, v145, v145\n"
"v_fmac_f32 v162, v146, v146\n"
"v_mfma_f32_16x16x32_bf16 v[60:63], a[28:31], a[68:71], 0\n"
"ds_read_b64_tr_b16 a[222:223], v198 offset:8456\n"
"v_fmac_f32 v163, v147, v147\n"
"v_mfma_f32_16x16x32_bf16 v[60:63], a[60:63], a[84:87], v[60:63]\n"
"s_mov_b32 s62, 0x240000\n"
"v_fmac_f32 v162, v148, v148\n"
"v_fmac_f32 v163, v149, v149\n"
"v_mfma_f32_16x16x32_bf16 v[28:31], a[28:31], a[64:67], 0\n"
"v_fmac_f32 v162, v150, v150\n"
"v_fmac_f32 v163, v151, v151\n"
"v_mfma_f32_16x16x32_bf16 v[28:31], a[60:63], a[80:83], v[28:31]\n"
"v_fmac_f32 v162, v152, v152\n"
"v_fmac_f32 v163, v153, v153\n"
"v_mfma_f32_16x16x32_bf16 v[208:211], a[96:99], a[224:227], 0\n"
"buffer_load_dwordx4 v228, s[32:35], s92 offen offset:64 lds\n"
"v_mfma_f32_16x16x32_bf16 v[208:211], a[104:107], a[224:227], v[208:211]\n"
"v_fmac_f32 v162, v154, v154\n"
"v_fmac_f32 v163, v155, v155\n"
"v_mfma_f32_16x16x32_bf16 v[212:215], a[100:103], a[224:227], 0\n"
"v_fmac_f32 v162, v156, v156\n"
"v_fmac_f32 v163, v157, v157\n"
"v_mfma_f32_16x16x32_bf16 v[212:215], a[108:111], a[224:227], v[212:215]\n"
"s_waitcnt vmcnt(12) lgkmcnt(0)\n"
"s_barrier\n"
"v_fmac_f32 v162, v158, v158\n"
"v_mfma_f32_16x16x32_bf16 v[0:3], a[112:115], a[176:179], v[0:3]\n"
"s_add_u32 s93, s93, 1\n"
"s_cmp_eq_u32 s93, 9\n"
"s_cselect_b32 s93, 0, s93\n"
"s_lshl_b32 s90, s93, 19\n"
"v_mfma_f32_16x16x32_bf16 v[0:3], a[144:147], a[192:195], v[0:3]\n"
"s_add_u32 m0, s85, 0xc000\n"
"ds_read_b128 a[64:67], v194 offset:16384\n"
"v_fmac_f32 v163, v159, v159\n"
"v_mfma_f32_16x16x32_bf16 v[32:35], a[112:115], a[180:183], v[32:35]\n"
"s_mov_b32 s63, 0x20000\n"
"buffer_load_dwordx4 v229, s[36:39], s90 offen lds\n"
"ds_read_b128 a[80:83], v194 offset:32768\n"
"v_mfma_f32_16x16x32_bf16 v[32:35], a[144:147], a[196:199], v[32:35]\n"
"s_addk_i32 m0, 1984\n"
"ds_read_b128 a[68:71], v194 offset:17408\n"
"v_add_f32 v162, v162, v163\n"
"s_nop 1\n"
"v_add_f32 v162, v162, v162 row_ror:8 row_mask:0xf bank_mask:0xf\n"
"v_mfma_f32_16x16x32_bf16 v[64:67], a[112:115], a[184:187], v[64:67]\n"
"ds_read_b128 a[84:87], v194 offset:33792\n"
"v_add_f32 v162, v162, v162 row_ror:4 row_mask:0xf bank_mask:0xf\n"
"v_mfma_f32_16x16x32_bf16 v[64:67], a[144:147], a[200:203], v[64:67]\n"
"ds_read_b128 a[72:75], v194 offset:18432\n"
"v_add_f32 v162, v162, v162 row_ror:2 row_mask:0xf bank_mask:0xf\n"
"v_mfma_f32_16x16x32_bf16 v[96:99], a[112:115], a[188:191], v[96:99]\n"
"ds_read_b128 a[88:91], v194 offset:34816\n"
"v_add_f32 v162, v162, v162 row_ror:1 row_mask:0xf bank_mask:0xf\n"
"v_mfma_f32_16x16x32_bf16 v[96:99], a[144:147], a[204:207], v[96:99]\n"
"ds_read_b128 a[76:79], v194 offset:19456\n"
"v_mov_b32 v166, v162\n"
"v_mfma_f32_16x16x32_bf16 v[100:103], a[116:119], a[188:191], v[100:103]\n"
"s_mov_b32 s66, 0x2400\n"
"buffer_load_dwordx4 v229, s[36:39], s90 offen offset:64 lds\n"
"v_mfma_f32_16x16x32_bf16 v[100:103], a[148:151], a[204:207], v[100:103]\n"
"s_add_u32 s90, s90, 0x40000\n"
"ds_read_b128 a[92:95], v194 offset:35840\n"
"v_permlane16_swap_b32 v162, v166\n"
"v_mfma_f32_16x16x32_bf16 v[68:71], a[116:119], a[184:187], v[68:71]\n"
"s_addk_i32 m0, 6208\n"
"ds_read_b64_tr_b16 a[0:1], v196\n"
"v_add_f32 v162, v162, v166\n"
"v_mov_b32 v166, 0x3727c5ac\n"
"v_mfma_f32_16x16x32_bf16 v[68:71], a[148:151], a[200:203], v[68:71]\n"
"ds_read_b64_tr_b16 a[2:3], v196 offset:256\n"
"v_fmac_f32 v166, 0x3ae38e39, v162\n"
"v_mfma_f32_16x16x32_bf16 v[36:39], a[116:119], a[180:183], v[36:39]\n"
"ds_read_b64_tr_b16 a[32:33], v196 offset:8192\n"
"v_rsq_f32 v165, v166\n"
"v_mfma_f32_16x16x32_bf16 v[36:39], a[148:151], a[196:199], v[36:39]\n"
"buffer_load_dwordx4 v229, s[36:39], s90 offen lds\n"
"v_and_b32 v170, 31, v224\n"
"v_mfma_f32_16x16x32_bf16 v[4:7], a[116:119], a[176:179], v[4:7]\n"
"s_addk_i32 m0, 1984\n"
"ds_read_b64_tr_b16 a[34:35], v196 offset:8448\n"
"v_mul_f32 v142, v142, v165\n"
"v_mul_f32 v143, v143, v165\n"
"v_mfma_f32_16x16x32_bf16 v[4:7], a[148:151], a[192:195], v[4:7]\n"
"s_mov_b32 s67, 0x20000\n"
"ds_read_b64_tr_b16 a[4:5], v196 offset:8\n"
"v_mul_f32 v144, v144, v165\n"
"v_mul_f32 v145, v145, v165\n"
"v_mfma_f32_16x16x32_bf16 v[8:11], a[120:123], a[176:179], v[8:11]\n"
"ds_read_b64_tr_b16 a[6:7], v196 offset:264\n"
"v_mul_f32 v146, v146, v165\n"
"v_mul_f32 v147, v147, v165\n"
"v_mfma_f32_16x16x32_bf16 v[8:11], a[152:155], a[192:195], v[8:11]\n"
"ds_read_b64_tr_b16 a[36:37], v196 offset:8200\n"
"v_mul_f32 v148, v148, v165\n"
"v_mfma_f32_16x16x32_bf16 v[40:43], a[120:123], a[180:183], v[40:43]\n"
"v_mul_f32 v149, v149, v165\n"
"v_mul_f32 v150, v150, v165\n"
"v_mfma_f32_16x16x32_bf16 v[40:43], a[152:155], a[196:199], v[40:43]\n"
"buffer_load_dwordx4 v229, s[36:39], s90 offen offset:64 lds\n"
"v_mul_f32 v151, v151, v165\n"
"v_mfma_f32_16x16x32_bf16 v[72:75], a[120:123], a[184:187], v[72:75]\n"
"s_lshl_b32 s92, s93, 7\n"
"ds_read_b64_tr_b16 a[38:39], v196 offset:8456\n"
"v_mul_f32 v152, v152, v165\n"
"v_mfma_f32_16x16x32_bf16 v[72:75], a[152:155], a[200:203], v[72:75]\n"
"s_add_u32 m0, s89, 0x10000\n"
"ds_read_b64_tr_b16 a[8:9], v196 offset:2048\n"
"v_mul_f32 v153, v153, v165\n"
"v_mfma_f32_16x16x32_bf16 v[104:107], a[120:123], a[188:191], v[104:107]\n"
"s_mov_b32 s70, 0x1000\n"
"ds_read_b64_tr_b16 a[10:11], v196 offset:2304\n"
"v_mul_f32 v154, v154, v165\n"
"v_mfma_f32_16x16x32_bf16 v[104:107], a[152:155], a[204:207], v[104:107]\n"
"ds_read_b64_tr_b16 a[40:41], v196 offset:10240\n"
"v_mul_f32 v155, v155, v165\n"
"v_mfma_f32_16x16x32_bf16 v[108:111], a[124:127], a[188:191], v[108:111]\n"
"ds_read_b64_tr_b16 a[42:43], v196 offset:10496\n"
"v_mul_f32 v156, v156, v165\n"
"v_mul_f32 v157, v157, v165\n"
"v_mfma_f32_16x16x32_bf16 v[108:111], a[156:159], a[204:207], v[108:111]\n"
"buffer_load_dwordx4 v225, s[32:35], s92 offen lds\n"
"v_mul_f32 v158, v158, v165\n"
"v_mfma_f32_16x16x32_bf16 v[76:79], a[124:127], a[184:187], v[76:79]\n"
"s_addk_i32 m0, 16320\n"
"ds_read_b64_tr_b16 a[12:13], v196 offset:2056\n"
"v_mul_f32 v159, v159, v165\n"
"v_mfma_f32_16x16x32_bf16 v[76:79], a[156:159], a[200:203], v[76:79]\n"
"ds_read_b64_tr_b16 a[14:15], v196 offset:2312\n"
"v_cvt_pk_bf16_f32 v142, 0, v142\n"
"v_mfma_f32_16x16x32_bf16 v[44:47], a[124:127], a[180:183], v[44:47]\n"
"ds_read_b64_tr_b16 a[44:45], v196 offset:10248\n"
"v_cvt_pk_bf16_f32 v143, 0, v143\n"
"v_cvt_pk_bf16_f32 v144, 0, v144\n"
"v_mfma_f32_16x16x32_bf16 v[44:47], a[156:159], a[196:199], v[44:47]\n"
"ds_read_b64_tr_b16 a[46:47], v196 offset:10504\n"
"v_cvt_pk_bf16_f32 v145, 0, v145\n"
"v_mfma_f32_16x16x32_bf16 v[12:15], a[124:127], a[176:179], v[12:15]\n"
"s_mov_b32 s71, 0x20000\n"
"buffer_load_dwordx4 v225, s[32:35], s92 offen offset:64 lds\n"
"v_cvt_pk_bf16_f32 v146, 0, v146\n"
"v_mfma_f32_16x16x32_bf16 v[12:15], a[156:159], a[192:195], v[12:15]\n"
"s_addk_i32 m0, -15296\n"
"ds_read_b64_tr_b16 a[16:17], v196 offset:4096\n"
"v_cvt_pk_bf16_f32 v147, 0, v147\n"
"v_mfma_f32_16x16x32_bf16 v[16:19], a[128:131], a[176:179], v[16:19]\n"
"ds_read_b64_tr_b16 a[18:19], v196 offset:4352\n"
"v_cvt_pk_bf16_f32 v148, 0, v148\n"
"v_cvt_pk_bf16_f32 v149, 0, v149\n"
"v_mfma_f32_16x16x32_bf16 v[16:19], a[160:163], a[192:195], v[16:19]\n"
"ds_read_b64_tr_b16 a[48:49], v196 offset:12288\n"
"v_cvt_pk_bf16_f32 v150, 0, v150\n"
"v_mfma_f32_16x16x32_bf16 v[48:51], a[128:131], a[180:183], v[48:51]\n"
"ds_read_b64_tr_b16 a[50:51], v196 offset:12544\n"
"v_cvt_pk_bf16_f32 v151, 0, v151\n"
"v_mfma_f32_16x16x32_bf16 v[48:51], a[160:163], a[196:199], v[48:51]\n"
"ds_read_b64_tr_b16 a[20:21], v196 offset:4104\n"
"v_cvt_pk_bf16_f32 v152, 0, v152\n"
"v_mfma_f32_16x16x32_bf16 v[80:83], a[128:131], a[184:187], v[80:83]\n"
"buffer_load_dwordx4 v226, s[32:35], s92 offen lds\n"
"v_cvt_pk_bf16_f32 v153, 0, v153\n"
"v_mfma_f32_16x16x32_bf16 v[80:83], a[160:163], a[200:203], v[80:83]\n"
"s_addk_i32 m0, 16320\n"
"ds_read_b64_tr_b16 a[22:23], v196 offset:4360\n"
"v_cvt_pk_bf16_f32 v154, 0, v154\n"
"v_cvt_pk_bf16_f32 v155, 0, v155\n"
"v_mfma_f32_16x16x32_bf16 v[112:115], a[128:131], a[188:191], v[112:115]\n"
"s_mov_b32 s74, 0x480000\n"
"ds_read_b64_tr_b16 a[52:53], v196 offset:12296\n"
"v_cvt_pk_bf16_f32 v156, 0, v156\n"
"v_mfma_f32_16x16x32_bf16 v[112:115], a[160:163], a[204:207], v[112:115]\n"
"ds_read_b64_tr_b16 a[54:55], v196 offset:12552\n"
"v_cvt_pk_bf16_f32 v157, 0, v157\n"
"v_mfma_f32_16x16x32_bf16 v[116:119], a[132:135], a[188:191], v[116:119]\n"
"ds_read_b64_tr_b16 a[24:25], v196 offset:6144\n"
"v_cvt_pk_bf16_f32 v158, 0, v158\n"
"v_cvt_pk_bf16_f32 v159, 0, v159\n"
"v_mfma_f32_16x16x32_bf16 v[116:119], a[164:167], a[204:207], v[116:119]\n"
"buffer_load_dwordx4 v226, s[32:35], s92 offen offset:64 lds\n"
"v_mov_b32 v168, v160\n"
"v_mfma_f32_16x16x32_bf16 v[84:87], a[132:135], a[184:187], v[84:87]\n"
"s_addk_i32 m0, -15296\n"
"ds_read_b64_tr_b16 a[26:27], v196 offset:6400\n"
"v_mul_f32 v169, 0x3e0293ee, v165\n"
"v_mfma_f32_16x16x32_bf16 v[84:87], a[164:167], a[200:203], v[84:87]\n"
"ds_read_b64_tr_b16 a[56:57], v196 offset:14336\n"
"v_lshrrev_b32 v171, 1, v141\n"
"v_mfma_f32_16x16x32_bf16 v[52:55], a[132:135], a[180:183], v[52:55]\n"
"ds_read_b64_tr_b16 a[58:59], v196 offset:14592\n"
"v_lshlrev_b32 v171, 7, v171\n"
"v_lshrrev_b32 v172, 2, v224\n"
"v_mfma_f32_16x16x32_bf16 v[52:55], a[164:167], a[196:199], v[52:55]\n"
"s_mov_b32 s75, 0x20000\n"
"ds_read_b64_tr_b16 a[28:29], v196 offset:6152\n"
"v_add_u32 v171, v171, v172\n"
"v_mfma_f32_16x16x32_bf16 v[20:23], a[132:135], a[176:179], v[20:23]\n"
"v_mov_b32 v172, 0x7ffffff0\n"
"v_mfma_f32_16x16x32_bf16 v[20:23], a[164:167], a[192:195], v[20:23]\n"
"buffer_load_dwordx4 v227, s[32:35], s92 offen lds\n"
"v_mfma_f32_16x16x32_bf16 v[24:27], a[136:139], a[176:179], v[24:27]\n"
"s_addk_i32 m0, 16320\n"
"ds_read_b64_tr_b16 a[30:31], v196 offset:6408\n"
"v_cmp_eq_u32 vcc, 0, v170\n"
"s_nop 1\n"
"v_cndmask_b32 v171, v172, v171, vcc\n"
"v_mfma_f32_16x16x32_bf16 v[24:27], a[168:171], a[192:195], v[24:27]\n"
"s_mov_b32 s11, 0x38000\n"
"ds_read_b64_tr_b16 a[60:61], v196 offset:14344\n"
"buffer_store_dwordx2 v[168:169], v171, s[56:59], s11 offen sc1\n"
"v_mfma_f32_16x16x32_bf16 v[56:59], a[136:139], a[180:183], v[56:59]\n"
"ds_read_b64_tr_b16 a[62:63], v196 offset:14600\n"
"v_add_f32 v160, v142, v144\n"
"v_add_f32 v161, v143, v145\n"
"v_mfma_f32_16x16x32_bf16 v[56:59], a[168:171], a[196:199], v[56:59]\n"
"ds_read_b64_tr_b16 a[96:97], v199\n"
"v_add_f32 v160, v160, v146\n"
"v_mfma_f32_16x16x32_bf16 v[88:91], a[136:139], a[184:187], v[88:91]\n"
"ds_read_b64_tr_b16 a[98:99], v199 offset:256\n"
"v_add_f32 v161, v161, v147\n"
"v_mfma_f32_16x16x32_bf16 v[88:91], a[168:171], a[200:203], v[88:91]\n"
"buffer_load_dwordx4 v227, s[32:35], s92 offen offset:64 lds\n"
"v_add_f32 v160, v160, v148\n"
"v_mfma_f32_16x16x32_bf16 v[120:123], a[136:139], a[188:191], v[120:123]\n"
"s_addk_i32 m0, -15296\n"
"ds_read_b64_tr_b16 a[104:105], v199 offset:8192\n"
"v_add_f32 v161, v161, v149\n"
"v_add_f32 v160, v160, v150\n"
"v_mfma_f32_16x16x32_bf16 v[120:123], a[168:171], a[204:207], v[120:123]\n"
"ds_read_b64_tr_b16 a[106:107], v199 offset:8448\n"
"v_add_f32 v161, v161, v151\n"
"v_or_b32 v251, s76, v224\n"
"v_mfma_f32_16x16x32_bf16 v[124:127], a[140:143], a[188:191], v[124:127]\n"
"ds_read_b64_tr_b16 a[100:101], v199 offset:8\n"
"v_add_f32 v160, v160, v152\n"
"v_mfma_f32_16x16x32_bf16 v[124:127], a[172:175], a[204:207], v[124:127]\n"
"ds_read_b64_tr_b16 a[102:103], v199 offset:264\n"
"v_add_f32 v161, v161, v153\n"
"v_add_f32 v160, v160, v154\n"
"v_mfma_f32_16x16x32_bf16 v[92:95], a[140:143], a[184:187], v[92:95]\n"
"buffer_load_dwordx4 v228, s[32:35], s92 offen lds\n"
"v_add_f32 v161, v161, v155\n"
"v_mfma_f32_16x16x32_bf16 v[92:95], a[172:175], a[200:203], v[92:95]\n"
"s_addk_i32 m0, 16320\n"
"ds_read_b64_tr_b16 a[108:109], v199 offset:8200\n"
"v_add_f32 v160, v160, v156\n"
"v_mfma_f32_16x16x32_bf16 v[60:63], a[140:143], a[180:183], v[60:63]\n"
"ds_read_b64_tr_b16 a[110:111], v199 offset:8456\n"
"v_add_f32 v161, v161, v157\n"
"v_mfma_f32_16x16x32_bf16 v[60:63], a[172:175], a[196:199], v[60:63]\n"
"v_add_f32 v160, v160, v158\n"
"v_add_f32 v161, v161, v159\n"
"v_mfma_f32_16x16x32_bf16 v[28:31], a[140:143], a[176:179], v[28:31]\n"
"v_add_f32 v160, v160, v161\n"
"v_mov_b32 v252, 0x7ffffff0\n"
"v_mfma_f32_16x16x32_bf16 v[28:31], a[172:175], a[192:195], v[28:31]\n"
"v_mul_f32 v162, v142, v142\n"
"v_mul_f32 v163, v143, v143\n"
"v_mfma_f32_16x16x32_bf16 v[208:211], a[208:211], a[224:227], v[208:211]\n"
"buffer_load_dwordx4 v228, s[32:35], s92 offen offset:64 lds\n"
"v_mfma_f32_16x16x32_bf16 v[208:211], a[216:219], a[224:227], v[208:211]\n"
"v_fmac_f32 v162, v144, v144\n"
"v_fmac_f32 v163, v145, v145\n"
"v_mfma_f32_16x16x32_bf16 v[212:215], a[212:215], a[224:227], v[212:215]\n"
"v_fmac_f32 v162, v146, v146\n"
"v_fmac_f32 v163, v147, v147\n"
"v_mfma_f32_16x16x32_bf16 v[212:215], a[220:223], a[224:227], v[212:215]\n"
"s_waitcnt vmcnt(13) lgkmcnt(0)\n"
"s_barrier\n"
"v_fmac_f32 v162, v148, v148\n"
"v_mfma_f32_16x16x32_bf16 v[0:3], a[0:3], a[64:67], v[0:3]\n"
"s_add_u32 s93, s93, 1\n"
"s_cmp_eq_u32 s93, 9\n"
"s_cselect_b32 s93, 0, s93\n"
"s_lshl_b32 s90, s93, 19\n"
"v_mfma_f32_16x16x32_bf16 v[0:3], a[32:35], a[80:83], v[0:3]\n"
"s_add_u32 m0, s85, 0x18000\n"
"ds_read_b128 a[176:179], v192 offset:16384\n"
"v_fmac_f32 v163, v149, v149\n"
"v_fmac_f32 v162, v150, v150\n"
"v_mfma_f32_16x16x32_bf16 v[32:35], a[0:3], a[68:71], v[32:35]\n"
"buffer_load_dwordx4 v229, s[36:39], s90 offen lds\n"
"ds_read_b128 a[192:195], v192 offset:32768\n"
"v_mfma_f32_16x16x32_bf16 v[32:35], a[32:35], a[84:87], v[32:35]\n"
"s_addk_i32 m0, 1984\n"
"ds_read_b128 a[180:183], v192 offset:17408\n"
"v_fmac_f32 v163, v151, v151\n"
"v_fmac_f32 v162, v152, v152\n"
"v_mfma_f32_16x16x32_bf16 v[64:67], a[0:3], a[72:75], v[64:67]\n"
"ds_read_b128 a[196:199], v192 offset:33792\n"
"v_cmp_eq_u32 vcc, 0, v251\n"
"s_nop 1\n"
"v_cndmask_b32 v234, v252, v251, vcc\n"
"v_mfma_f32_16x16x32_bf16 v[64:67], a[32:35], a[88:91], v[64:67]\n"
"ds_read_b128 a[184:187], v192 offset:18432\n"
"v_fmac_f32 v163, v153, v153\n"
"v_fmac_f32 v162, v154, v154\n"
"v_mfma_f32_16x16x32_bf16 v[96:99], a[0:3], a[76:79], v[96:99]\n"
"s_lshl_b32 s81, s78, 6\n"
"ds_read_b128 a[200:203], v192 offset:34816\n"
"v_fmac_f32 v163, v155, v155\n"
"v_fmac_f32 v162, v156, v156\n"
"v_mfma_f32_16x16x32_bf16 v[96:99], a[32:35], a[92:95], v[96:99]\n"
"ds_read_b128 a[188:191], v192 offset:19456\n"
"v_fmac_f32 v163, v157, v157\n"
"v_mfma_f32_16x16x32_bf16 v[100:103], a[4:7], a[76:79], v[100:103]\n"
"buffer_load_dwordx4 v229, s[36:39], s90 offen offset:64 lds\n"
"v_fmac_f32 v162, v158, v158\n"
"v_mfma_f32_16x16x32_bf16 v[100:103], a[36:39], a[92:95], v[100:103]\n"
"s_add_u32 s90, s90, 0x40000\n"
"ds_read_b128 a[204:207], v192 offset:35840\n"
"v_fmac_f32 v163, v159, v159\n"
"v_mfma_f32_16x16x32_bf16 v[68:71], a[4:7], a[72:75], v[68:71]\n"
"s_addk_i32 m0, 6208\n"
"ds_read_b64_tr_b16 a[112:113], v230\n"
"v_add_f32 v162, v162, v163\n"
"v_add_f32 v160, v160, v160 row_ror:8 row_mask:0xf bank_mask:0xf\n"
"v_mfma_f32_16x16x32_bf16 v[68:71], a[36:39], a[88:91], v[68:71]\n"
"ds_read_b64_tr_b16 a[114:115], v230 offset:256\n"
"v_add_f32 v162, v162, v162 row_ror:8 row_mask:0xf bank_mask:0xf\n"
"v_mfma_f32_16x16x32_bf16 v[36:39], a[4:7], a[68:71], v[36:39]\n"
"ds_read_b64_tr_b16 a[144:145], v230 offset:8192\n"
"v_add_f32 v160, v160, v160 row_ror:4 row_mask:0xf bank_mask:0xf\n"
"v_mfma_f32_16x16x32_bf16 v[36:39], a[36:39], a[84:87], v[36:39]\n"
"buffer_load_dwordx4 v229, s[36:39], s90 offen lds\n"
"v_add_f32 v162, v162, v162 row_ror:4 row_mask:0xf bank_mask:0xf\n"
"v_mfma_f32_16x16x32_bf16 v[4:7], a[4:7], a[64:67], v[4:7]\n"
"s_addk_i32 m0, 1984\n"
"ds_read_b64_tr_b16 a[146:147], v230 offset:8448\n"
"v_add_f32 v160, v160, v160 row_ror:2 row_mask:0xf bank_mask:0xf\n"
"v_add_f32 v162, v162, v162 row_ror:2 row_mask:0xf bank_mask:0xf\n"
"v_mfma_f32_16x16x32_bf16 v[4:7], a[36:39], a[80:83], v[4:7]\n"
"s_lshl_b32 s82, s79, 5\n"
"ds_read_b64_tr_b16 a[116:117], v230 offset:8\n"
"v_add_f32 v160, v160, v160 row_ror:1 row_mask:0xf bank_mask:0xf\n"
"v_mfma_f32_16x16x32_bf16 v[8:11], a[8:11], a[64:67], v[8:11]\n"
"ds_read_b64_tr_b16 a[118:119], v230 offset:264\n"
"v_add_f32 v162, v162, v162 row_ror:1 row_mask:0xf bank_mask:0xf\n"
"v_mfma_f32_16x16x32_bf16 v[8:11], a[40:43], a[80:83], v[8:11]\n"
"ds_read_b64_tr_b16 a[148:149], v230 offset:8200\n"
"v_mov_b32 v166, v160\n"
"v_mfma_f32_16x16x32_bf16 v[40:43], a[8:11], a[68:71], v[40:43]\n"
"v_mov_b32 v167, v162\n"
"v_permlane16_swap_b32 v160, v166\n"
"v_mfma_f32_16x16x32_bf16 v[40:43], a[40:43], a[84:87], v[40:43]\n"
"buffer_load_dwordx4 v229, s[36:39], s90 offen offset:64 lds\n"
"v_mfma_f32_16x16x32_bf16 v[72:75], a[8:11], a[72:75], v[72:75]\n"
"s_lshl_b32 s92, s93, 7\n"
"ds_read_b64_tr_b16 a[150:151], v230 offset:8456\n"
"v_permlane16_swap_b32 v162, v167\n"
"v_mfma_f32_16x16x32_bf16 v[72:75], a[40:43], a[88:91], v[72:75]\n"
"s_add_u32 m0, s89, 0x1c000\n"
"ds_read_b64_tr_b16 a[120:121], v230 offset:2048\n"
"v_add_f32 v160, v160, v166\n"
"v_add_f32 v162, v162, v167\n"
"v_mfma_f32_16x16x32_bf16 v[104:107], a[8:11], a[76:79], v[104:107]\n"
"s_add_u32 s82, s82, s80\n"
"ds_read_b64_tr_b16 a[122:123], v230 offset:2304\n"
"v_mul_f32 v160, 0xbae38e39, v160\n"
"v_mov_b32 v166, 0x3727c5ac\n"
"v_mfma_f32_16x16x32_bf16 v[104:107], a[40:43], a[92:95], v[104:107]\n"
"ds_read_b64_tr_b16 a[152:153], v230 offset:10240\n"
"v_fmac_f32 v166, 0x3ae38e39, v162\n"
"v_mfma_f32_16x16x32_bf16 v[108:111], a[12:15], a[76:79], v[108:111]\n"
"ds_read_b64_tr_b16 a[154:155], v230 offset:10496\n"
"v_fma_f32 v166, -v160, v160, v166\n"
"v_mfma_f32_16x16x32_bf16 v[108:111], a[44:47], a[92:95], v[108:111]\n"
"buffer_load_dwordx4 v225, s[32:35], s92 offen lds\n"
"v_max_f32 v166, 0x3727c5ac, v166\n"
"v_mfma_f32_16x16x32_bf16 v[76:79], a[12:15], a[72:75], v[76:79]\n"
"s_addk_i32 m0, 16320\n"
"ds_read_b64_tr_b16 a[124:125], v230 offset:2056\n"
"v_rsq_f32 v165, v166\n"
"v_mfma_f32_16x16x32_bf16 v[76:79], a[44:47], a[88:91], v[76:79]\n"
"ds_read_b64_tr_b16 a[126:127], v230 offset:2312\n"
"v_mul_u32_u24 v182, 24, v170\n"
"v_mul_f32 v164, v160, v165\n"
"v_mfma_f32_16x16x32_bf16 v[44:47], a[12:15], a[68:71], v[44:47]\n"
"s_add_u32 s81, s81, s82\n"
"ds_read_b64_tr_b16 a[156:157], v230 offset:10248\n"
"v_fma_f32 v142, v142, v165, v164\n"
"v_mfma_f32_16x16x32_bf16 v[44:47], a[44:47], a[84:87], v[44:47]\n"
"ds_read_b64_tr_b16 a[158:159], v230 offset:10504\n"
"v_fma_f32 v143, v143, v165, v164\n"
"v_mfma_f32_16x16x32_bf16 v[12:15], a[12:15], a[64:67], v[12:15]\n"
"buffer_load_dwordx4 v225, s[32:35], s92 offen offset:64 lds\n"
"v_fma_f32 v144, v144, v165, v164\n"
"v_mfma_f32_16x16x32_bf16 v[12:15], a[44:47], a[80:83], v[12:15]\n"
"s_addk_i32 m0, -15296\n"
"ds_read_b64_tr_b16 a[128:129], v230 offset:4096\n"
"v_fma_f32 v145, v145, v165, v164\n"
"v_fma_f32 v146, v146, v165, v164\n"
"v_mfma_f32_16x16x32_bf16 v[16:19], a[16:19], a[64:67], v[16:19]\n"
"ds_read_b64_tr_b16 a[130:131], v230 offset:4352\n"
"v_fma_f32 v147, v147, v165, v164\n"
"v_mfma_f32_16x16x32_bf16 v[16:19], a[48:51], a[80:83], v[16:19]\n"
"ds_read_b64_tr_b16 a[160:161], v230 offset:12288\n"
"v_fma_f32 v148, v148, v165, v164\n"
"v_mfma_f32_16x16x32_bf16 v[48:51], a[16:19], a[68:71], v[48:51]\n"
"ds_read_b64_tr_b16 a[162:163], v230 offset:12544\n"
"v_fma_f32 v149, v149, v165, v164\n"
"v_fma_f32 v150, v150, v165, v164\n"
"v_mfma_f32_16x16x32_bf16 v[48:51], a[48:51], a[84:87], v[48:51]\n"
"ds_read_b64_tr_b16 a[132:133], v230 offset:4104\n"
"v_fma_f32 v151, v151, v165, v164\n"
"v_mfma_f32_16x16x32_bf16 v[80:83], a[16:19], a[72:75], v[80:83]\n"
"s_lshl_b32 s8, s81, 7\n"
"buffer_load_dwordx4 v226, s[32:35], s92 offen lds\n"
"v_fma_f32 v152, v152, v165, v164\n"
"v_mfma_f32_16x16x32_bf16 v[80:83], a[48:51], a[88:91], v[80:83]\n"
"s_addk_i32 m0, 16320\n"
"ds_read_b64_tr_b16 a[134:135], v230 offset:4360\n"
"v_fma_f32 v153, v153, v165, v164\n"
"v_mfma_f32_16x16x32_bf16 v[112:115], a[16:19], a[76:79], v[112:115]\n"
"ds_read_b64_tr_b16 a[164:165], v230 offset:12296\n"
"v_fma_f32 v154, v154, v165, v164\n"
"v_mfma_f32_16x16x32_bf16 v[112:115], a[48:51], a[92:95], v[112:115]\n"
"ds_read_b64_tr_b16 a[166:167], v230 offset:12552\n"
"v_fma_f32 v155, v155, v165, v164\n"
"v_fma_f32 v156, v156, v165, v164\n"
"v_mfma_f32_16x16x32_bf16 v[116:119], a[20:23], a[76:79], v[116:119]\n"
"ds_read_b64_tr_b16 a[136:137], v230 offset:6144\n"
"v_fma_f32 v157, v157, v165, v164\n"
"v_mfma_f32_16x16x32_bf16 v[116:119], a[52:55], a[92:95], v[116:119]\n"
"buffer_load_dwordx4 v226, s[32:35], s92 offen offset:64 lds\n"
"v_fma_f32 v158, v158, v165, v164\n"
"v_mfma_f32_16x16x32_bf16 v[84:87], a[20:23], a[72:75], v[84:87]\n"
"s_addk_i32 m0, -15296\n"
"ds_read_b64_tr_b16 a[138:139], v230 offset:6400\n"
"v_fma_f32 v159, v159, v165, v164\n"
"v_mfma_f32_16x16x32_bf16 v[84:87], a[52:55], a[88:91], v[84:87]\n"
"s_mul_i32 s11, s76, 0xa00\n"
"ds_read_b64_tr_b16 a[168:169], v230 offset:14336\n"
"v_mfma_f32_16x16x32_bf16 v[52:55], a[20:23], a[68:71], v[52:55]\n"
"s_add_u32 s11, s11, 0x24000\n"
"ds_read_b64_tr_b16 a[170:171], v230 offset:14592\n"
"v_add_u32 v182, s11, v182\n"
"v_lshrrev_b32 v171, 5, v224\n"
"v_mfma_f32_16x16x32_bf16 v[52:55], a[52:55], a[84:87], v[52:55]\n"
"s_lshl_b32 s82, s82, 2\n"
"ds_read_b64_tr_b16 a[140:141], v230 offset:6152\n"
"v_mul_u32_u24 v172, 0x5f4, v171\n"
"v_add_u32 v183, v182, v172\n"
"v_mfma_f32_16x16x32_bf16 v[20:23], a[20:23], a[64:67], v[20:23]\n"
"v_mul_u32_u24 v172, 0x30c, v171\n"
"v_add_u32 v182, v182, v172\n"
"v_mfma_f32_16x16x32_bf16 v[20:23], a[52:55], a[80:83], v[20:23]\n"
"buffer_load_dwordx4 v227, s[32:35], s92 offen lds\n"
"v_mfma_f32_16x16x32_bf16 v[24:27], a[24:27], a[64:67], v[24:27]\n"
"s_addk_i32 m0, 16320\n"
"ds_read_b64_tr_b16 a[142:143], v230 offset:6408\n"
"v_permlane32_swap_b32 v142, v151\n"
"v_mfma_f32_16x16x32_bf16 v[24:27], a[56:59], a[80:83], v[24:27]\n"
"ds_read_b64_tr_b16 a[172:173], v230 offset:14344\n"
"v_permlane32_swap_b32 v143, v152\n"
"v_mfma_f32_16x16x32_bf16 v[56:59], a[24:27], a[68:71], v[56:59]\n"
"ds_read_b64_tr_b16 a[174:175], v230 offset:14600\n"
"v_permlane32_swap_b32 v144, v153\n"
"v_mfma_f32_16x16x32_bf16 v[56:59], a[56:59], a[84:87], v[56:59]\n"
"s_add_u32 s82, s82, s78\n"
"v_permlane32_swap_b32 v145, v154\n"
"v_mfma_f32_16x16x32_bf16 v[88:91], a[24:27], a[72:75], v[88:91]\n"
"ds_read_b64_tr_b16 a[208:209], v197\n"
"v_permlane32_swap_b32 v146, v155\n"
"v_mfma_f32_16x16x32_bf16 v[88:91], a[56:59], a[88:91], v[88:91]\n"
"buffer_load_dwordx4 v227, s[32:35], s92 offen offset:64 lds\n"
"ds_read_b64_tr_b16 a[210:211], v197 offset:256\n"
"v_mfma_f32_16x16x32_bf16 v[120:123], a[24:27], a[76:79], v[120:123]\n"
"s_addk_i32 m0, -15296\n"
"ds_read_b64_tr_b16 a[216:217], v197 offset:8192\n"
"v_permlane32_swap_b32 v147, v156\n"
"v_mfma_f32_16x16x32_bf16 v[120:123], a[56:59], a[92:95], v[120:123]\n"
"ds_read_b64_tr_b16 a[218:219], v197 offset:8448\n"
"v_permlane32_swap_b32 v148, v157\n"
"v_mfma_f32_16x16x32_bf16 v[124:127], a[28:31], a[76:79], v[124:127]\n"
"v_permlane32_swap_b32 v149, v158\n"
"v_mfma_f32_16x16x32_bf16 v[124:127], a[60:63], a[92:95], v[124:127]\n"
"v_permlane32_swap_b32 v150, v159\n"
"v_cvt_pk_bf16_f32 v173, v142, v151\n"
"v_mfma_f32_16x16x32_bf16 v[92:95], a[28:31], a[72:75], v[92:95]\n"
"s_lshl_b32 s9, s82, 7\n"
"buffer_load_dwordx4 v228, s[32:35], s92 offen lds\n"
"ds_read_b64_tr_b16 a[212:213], v197 offset:8\n"
"v_mfma_f32_16x16x32_bf16 v[92:95], a[60:63], a[88:91], v[92:95]\n"
"s_addk_i32 m0, 16320\n"
"ds_read_b64_tr_b16 a[214:215], v197 offset:264\n"
"v_cvt_pk_bf16_f32 v174, v143, v152\n"
"v_cvt_pk_bf16_f32 v175, v144, v153\n"
"v_mfma_f32_16x16x32_bf16 v[60:63], a[28:31], a[68:71], v[60:63]\n"
"ds_read_b64_tr_b16 a[220:221], v197 offset:8200\n"
"v_cvt_pk_bf16_f32 v176, v145, v154\n"
"v_cvt_pk_bf16_f32 v177, v146, v155\n"
"v_mfma_f32_16x16x32_bf16 v[60:63], a[60:63], a[84:87], v[60:63]\n"
"v_cvt_pk_bf16_f32 v178, v147, v156\n"
"v_cvt_pk_bf16_f32 v179, v148, v157\n"
"v_cvt_pk_bf16_f32 v180, v149, v158\n"
"v_cvt_pk_bf16_f32 v181, v150, v159\n"
"v_mfma_f32_16x16x32_bf16 v[28:31], a[28:31], a[64:67], v[28:31]\n"
"ds_read_b64_tr_b16 a[222:223], v197 offset:8456\n"
"v_mfma_f32_16x16x32_bf16 v[28:31], a[60:63], a[80:83], v[28:31]\n"
"v_mfma_f32_16x16x32_bf16 v[208:211], a[96:99], a[224:227], v[208:211]\n"
"ds_write_b32 v182, v173\n"
"ds_write_b32 v182, v174 offset:4\n"
"ds_write_b32 v182, v175 offset:8\n"
"ds_write_b32 v183, v176 offset:12\n"
"ds_write_b32 v183, v177 offset:16\n"
"ds_write_b32 v183, v178 offset:20\n"
"ds_write_b32 v182, v179 offset:768\n"
"ds_write_b32 v182, v180 offset:772\n"
"ds_write_b32 v182, v181 offset:776\n"
"v_mfma_f32_16x16x32_bf16 v[208:211], a[104:107], a[224:227], v[208:211]\n"
"buffer_load_dwordx4 v228, s[32:35], s92 offen offset:64 lds\n"
"v_mfma_f32_16x16x32_bf16 v[212:215], a[100:103], a[224:227], v[212:215]\n"
"v_mfma_f32_16x16x32_bf16 v[212:215], a[108:111], a[224:227], v[212:215]\n"
"s_waitcnt vmcnt(12) lgkmcnt(0)\n"
"s_barrier\n"
"v_mfma_f32_16x16x32_bf16 v[0:3], a[112:115], a[176:179], v[0:3]\n"
"s_add_u32 s93, s93, 1\n"
"s_cmp_eq_u32 s93, 9\n"
"s_cselect_b32 s93, 0, s93\n"
"s_lshl_b32 s90, s93, 19\n"
"v_mfma_f32_16x16x32_bf16 v[0:3], a[144:147], a[192:195], v[0:3]\n"
"s_add_u32 m0, s85, 0\n"
"ds_read_b128 a[64:67], v193 offset:16384\n"
"v_mfma_f32_16x16x32_bf16 v[32:35], a[112:115], a[180:183], v[32:35]\n"
"s_lshr_b32 s81, s19, 1\n"
"buffer_load_dwordx4 v229, s[36:39], s90 offen lds\n"
"ds_read_b128 a[80:83], v193 offset:32768\n"
"v_mfma_f32_16x16x32_bf16 v[32:35], a[144:147], a[196:199], v[32:35]\n"
"s_addk_i32 m0, 1984\n"
"ds_read_b128 a[68:71], v193 offset:17408\n"
"v_mfma_f32_16x16x32_bf16 v[64:67], a[112:115], a[184:187], v[64:67]\n"
"s_lshl_b32 s11, s76, 8\n"
"ds_read_b128 a[84:87], v193 offset:33792\n"
"v_mfma_f32_16x16x32_bf16 v[64:67], a[144:147], a[200:203], v[64:67]\n"
"s_lshl_b32 s81, s81, 19\n"
"ds_read_b128 a[72:75], v193 offset:18432\n"
"v_mfma_f32_16x16x32_bf16 v[96:99], a[112:115], a[188:191], v[96:99]\n"
"s_add_u32 s11, s11, 0x24000\n"
"ds_read_b128 a[88:91], v193 offset:34816\n"
"v_lshl_add_u32 v184, v224, 2, s11\n"
"v_mfma_f32_16x16x32_bf16 v[96:99], a[144:147], a[204:207], v[96:99]\n"
"s_add_u32 s82, s81, 0x60000\n"
"ds_read_b128 a[76:79], v193 offset:19456\n"
"ds_read2st64_b32 v[128:129], v184 offset0:0 offset1:10\n"
"v_mfma_f32_16x16x32_bf16 v[100:103], a[116:119], a[188:191], v[100:103]\n"
"s_add_u32 s83, s81, 0x5e000\n"
"buffer_load_dwordx4 v229, s[36:39], s90 offen offset:64 lds\n"
"v_mfma_f32_16x16x32_bf16 v[100:103], a[148:151], a[204:207], v[100:103]\n"
"s_add_u32 s90, s90, 0x40000\n"
"ds_read_b128 a[92:95], v193 offset:35840\n"
"s_add_u32 s40, s40, s82\n"
"s_addc_u32 s41, s41, 0\n"
"v_mfma_f32_16x16x32_bf16 v[68:71], a[116:119], a[184:187], v[68:71]\n"
"s_addk_i32 m0, 6208\n"
"ds_read_b64_tr_b16 a[0:1], v195\n"
"s_add_u32 s44, s44, s83\n"
"s_addc_u32 s45, s45, 0\n"
"v_mfma_f32_16x16x32_bf16 v[68:71], a[148:151], a[200:203], v[68:71]\n"
"ds_read_b64_tr_b16 a[2:3], v195 offset:256\n"
"ds_read2st64_b32 v[130:131], v184 offset0:20 offset1:30\n"
"v_lshrrev_b32 v251, 2, v224\n"
"v_mfma_f32_16x16x32_bf16 v[36:39], a[116:119], a[180:183], v[36:39]\n"
"ds_read_b64_tr_b16 a[32:33], v195 offset:8192\n"
"v_mul_u32_u24 v251, 0x100, v251\n"
"v_mfma_f32_16x16x32_bf16 v[36:39], a[148:151], a[196:199], v[36:39]\n"
"buffer_load_dwordx4 v229, s[36:39], s90 offen lds\n"
"ds_read2st64_b32 v[132:133], v184 offset0:4 offset1:14\n"
"v_mfma_f32_16x16x32_bf16 v[4:7], a[116:119], a[176:179], v[4:7]\n"
"s_addk_i32 m0, 1984\n"
"ds_read_b64_tr_b16 a[34:35], v195 offset:8448\n"
"v_and_b32 v252, 3, v224\n"
"v_mfma_f32_16x16x32_bf16 v[4:7], a[148:151], a[192:195], v[4:7]\n"
"ds_read_b64_tr_b16 a[4:5], v195 offset:8\n"
"ds_read2st64_b32 v[134:135], v184 offset0:24 offset1:34\n"
"v_bfe_u32 v253, v224, 5, 1\n"
"v_mfma_f32_16x16x32_bf16 v[8:11], a[120:123], a[176:179], v[8:11]\n"
"ds_read_b64_tr_b16 a[6:7], v195 offset:264\n"
"ds_read2st64_b32 v[136:137], v184 offset0:8 offset1:18\n"
"v_lshlrev_b32 v253, 1, v253\n"
"v_mfma_f32_16x16x32_bf16 v[8:11], a[152:155], a[192:195], v[8:11]\n"
"ds_read_b64_tr_b16 a[36:37], v195 offset:8200\n"
"v_xor_b32 v252, v252, v253\n"
"v_mfma_f32_16x16x32_bf16 v[40:43], a[120:123], a[180:183], v[40:43]\n"
"ds_read2st64_b32 v[138:139], v184 offset0:28 offset1:38\n"
"v_lshl_add_u32 v251, v252, 4, v251\n"
"v_mfma_f32_16x16x32_bf16 v[40:43], a[152:155], a[196:199], v[40:43]\n"
"s_mul_i32 s82, s76, 0x1000\n"
"buffer_load_dwordx4 v229, s[36:39], s90 offen offset:64 lds\n"
"v_mfma_f32_16x16x32_bf16 v[72:75], a[120:123], a[184:187], v[72:75]\n"
"s_lshl_b32 s92, s93, 7\n"
"ds_read_b64_tr_b16 a[38:39], v195 offset:8456\n"
"v_lshrrev_b32 v182, 4, v224\n"
"v_mfma_f32_16x16x32_bf16 v[72:75], a[152:155], a[200:203], v[72:75]\n"
"s_add_u32 m0, s89, 0x4000\n"
"ds_read_b64_tr_b16 a[8:9], v195 offset:2048\n"
"v_add_u32 v244, s82, v251\n"
"v_mfma_f32_16x16x32_bf16 v[104:107], a[120:123], a[188:191], v[104:107]\n"
"s_lshl_b32 s11, s76, 2\n"
"ds_read_b64_tr_b16 a[10:11], v195 offset:2304\n"
"v_lshrrev_b32 v251, 2, v224\n"
"v_mfma_f32_16x16x32_bf16 v[104:107], a[152:155], a[204:207], v[104:107]\n"
"ds_read_b64_tr_b16 a[40:41], v195 offset:10240\n"
"v_add_u32 v182, s11, v182\n"
"v_mfma_f32_16x16x32_bf16 v[108:111], a[124:127], a[188:191], v[108:111]\n"
"ds_read_b64_tr_b16 a[42:43], v195 offset:10496\n"
"v_mul_u32_u24 v251, 0x100, v251\n"
"v_mfma_f32_16x16x32_bf16 v[108:111], a[156:159], a[204:207], v[108:111]\n"
"buffer_load_dwordx4 v225, s[32:35], s92 offen lds\n"
"v_mfma_f32_16x16x32_bf16 v[76:79], a[124:127], a[184:187], v[76:79]\n"
"s_addk_i32 m0, 16320\n"
"ds_read_b64_tr_b16 a[12:13], v195 offset:2056\n"
"v_lshlrev_b32 v182, 11, v182\n"
"v_and_b32 v252, 3, v224\n"
"v_mfma_f32_16x16x32_bf16 v[76:79], a[156:159], a[200:203], v[76:79]\n"
"ds_read_b64_tr_b16 a[14:15], v195 offset:2312\n"
"v_and_b32 v183, 15, v224\n"
"v_bfe_u32 v253, v224, 4, 1\n"
"v_mfma_f32_16x16x32_bf16 v[44:47], a[124:127], a[180:183], v[44:47]\n"
"ds_read_b64_tr_b16 a[44:45], v195 offset:10248\n"
"v_lshlrev_b32 v253, 1, v253\n"
"v_mfma_f32_16x16x32_bf16 v[44:47], a[156:159], a[196:199], v[44:47]\n"
"ds_read_b64_tr_b16 a[46:47], v195 offset:10504\n"
"v_lshl_add_u32 v182, v183, 4, v182\n"
"v_mfma_f32_16x16x32_bf16 v[12:15], a[124:127], a[176:179], v[12:15]\n"
"s_cmp_eq_u32 s76, 0\n"
"s_cselect_b32 s12, 0, 0x7ffffff0\n"
"ds_read_b64_tr_b16 a[16:17], v195 offset:4096\n"
"v_xor_b32 v252, v252, v253\n"
"v_mfma_f32_16x16x32_bf16 v[12:15], a[156:159], a[192:195], v[12:15]\n"
"buffer_load_dwordx4 v225, s[32:35], s92 offen offset:64 lds\n"
"v_lshl_add_u32 v251, v252, 4, v251\n"
"v_mfma_f32_16x16x32_bf16 v[16:19], a[128:131], a[176:179], v[16:19]\n"
"s_addk_i32 m0, -15296\n"
"ds_read_b64_tr_b16 a[18:19], v195 offset:4352\n"
"v_or_b32 v183, s12, v182\n"
"v_mfma_f32_16x16x32_bf16 v[16:19], a[160:163], a[192:195], v[16:19]\n"
"s_mul_i32 s82, s76, 0x1000\n"
"ds_read_b64_tr_b16 a[48:49], v195 offset:12288\n"
"v_add_u32 v245, s82, v251\n"
"v_mfma_f32_16x16x32_bf16 v[48:51], a[128:131], a[180:183], v[48:51]\n"
"s_lshr_b32 s11, s77, 3\n"
"ds_read_b64_tr_b16 a[50:51], v195 offset:12544\n"
"v_lshrrev_b32 v251, 2, v224\n"
"v_mfma_f32_16x16x32_bf16 v[48:51], a[160:163], a[196:199], v[48:51]\n"
"s_mul_i32 s11, s11, 0x12000\n"
"ds_read_b64_tr_b16 a[20:21], v195 offset:4104\n"
"v_lshlrev_b32 v251, 6, v251\n"
"v_mfma_f32_16x16x32_bf16 v[80:83], a[128:131], a[184:187], v[80:83]\n"
"s_lshl_b32 s12, s79, 10\n"
"buffer_load_dwordx4 v226, s[32:35], s92 offen lds\n"
"v_mfma_f32_16x16x32_bf16 v[80:83], a[160:163], a[200:203], v[80:83]\n"
"s_addk_i32 m0, 16320\n"
"ds_read_b64_tr_b16 a[22:23], v195 offset:4360\n"
"v_and_b32 v252, 3, v224\n"
"v_mfma_f32_16x16x32_bf16 v[112:115], a[128:131], a[188:191], v[112:115]\n"
"s_add_u32 s11, s11, s12\n"
"ds_read_b64_tr_b16 a[52:53], v195 offset:12296\n"
"v_lshl_add_u32 v251, v252, 3, v251\n"
"v_mfma_f32_16x16x32_bf16 v[112:115], a[160:163], a[204:207], v[112:115]\n"
"s_lshl_b32 s12, s78, 8\n"
"ds_read_b64_tr_b16 a[54:55], v195 offset:12552\n"
"v_bfe_u32 v253, v224, 4, 1\n"
"v_mfma_f32_16x16x32_bf16 v[116:119], a[132:135], a[188:191], v[116:119]\n"
"ds_read_b64_tr_b16 a[24:25], v195 offset:6144\n"
"v_lshl_add_u32 v247, v253, 5, v251\n"
"v_mfma_f32_16x16x32_bf16 v[116:119], a[164:167], a[204:207], v[116:119]\n"
"s_add_u32 s11, s11, s12\n"
"buffer_load_dwordx4 v226, s[32:35], s92 offen offset:64 lds\n"
"v_mfma_f32_16x16x32_bf16 v[84:87], a[132:135], a[184:187], v[84:87]\n"
"s_addk_i32 m0, -15296\n"
"ds_read_b64_tr_b16 a[26:27], v195 offset:6400\n"
"v_xor_b32 v248, 32, v247\n"
"v_mfma_f32_16x16x32_bf16 v[84:87], a[164:167], a[200:203], v[84:87]\n"
"s_add_u32 s11, s11, 0x58000\n"
"ds_read_b64_tr_b16 a[56:57], v195 offset:14336\n"
"v_add_u32 v236, 0x10000, v235\n"
"v_mfma_f32_16x16x32_bf16 v[52:55], a[132:135], a[180:183], v[52:55]\n"
"s_mul_i32 s87, s76, 0x1800\n"
"ds_read_b64_tr_b16 a[58:59], v195 offset:14592\n"
"v_mfma_f32_16x16x32_bf16 v[52:55], a[164:167], a[196:199], v[52:55]\n"
"s_waitcnt lgkmcnt(15)\n"
"ds_read_b64_tr_b16 a[28:29], v195 offset:6152\n"
"v_mfma_f32_16x16x32_bf16 v[20:23], a[132:135], a[176:179], v[20:23]\n"
"s_mul_i32 s88, s76, 0x1200\n"
"buffer_store_dwordx4 v[128:131], v182, s[56:59], s11 offen sc1\n"
"v_mfma_f32_16x16x32_bf16 v[20:23], a[164:167], a[192:195], v[20:23]\n"
"s_mov_b32 s97, 0\n"
"buffer_load_dwordx4 v227, s[32:35], s92 offen lds\n"
"v_mfma_f32_16x16x32_bf16 v[24:27], a[136:139], a[176:179], v[24:27]\n"
"s_addk_i32 m0, 16320\n"
"ds_read_b64_tr_b16 a[30:31], v195 offset:6408\n"
"v_mfma_f32_16x16x32_bf16 v[24:27], a[168:171], a[192:195], v[24:27]\n"
"s_cmp_lt_u32 s80, 27\n"
"s_cselect_b32 s7, 1, 0\n"
"ds_read_b64_tr_b16 a[60:61], v195 offset:14344\n"
"v_mfma_f32_16x16x32_bf16 v[56:59], a[136:139], a[180:183], v[56:59]\n"
"s_add_u32 s11, s11, 0x8000\n"
"ds_read_b64_tr_b16 a[62:63], v195 offset:14600\n"
"v_mfma_f32_16x16x32_bf16 v[56:59], a[168:171], a[196:199], v[56:59]\n"
"s_lshr_b32 s81, s10, 4\n"
"ds_read_b64_tr_b16 a[96:97], v198\n"
"v_mfma_f32_16x16x32_bf16 v[88:91], a[136:139], a[184:187], v[88:91]\n"
"s_lshl_b32 s81, s81, 17\n"
"ds_read_b64_tr_b16 a[98:99], v198 offset:256\n"
"v_mfma_f32_16x16x32_bf16 v[88:91], a[168:171], a[200:203], v[88:91]\n"
"s_lshl_b32 s82, s19, 12\n"
"buffer_load_dwordx4 v227, s[32:35], s92 offen offset:64 lds\n"
"v_mfma_f32_16x16x32_bf16 v[120:123], a[136:139], a[188:191], v[120:123]\n"
"s_addk_i32 m0, -15296\n"
"ds_read_b64_tr_b16 a[104:105], v198 offset:8192\n"
"buffer_store_dwordx4 v[132:135], v182, s[56:59], s11 offen sc1\n"
"v_mfma_f32_16x16x32_bf16 v[120:123], a[168:171], a[204:207], v[120:123]\n"
"s_add_u32 s11, s11, 0x8000\n"
"ds_read_b64_tr_b16 a[106:107], v198 offset:8448\n"
"buffer_store_dwordx4 v[136:139], v183, s[56:59], s11 offen sc1\n"
"v_mfma_f32_16x16x32_bf16 v[124:127], a[140:143], a[188:191], v[124:127]\n"
"s_add_u32 s81, s81, s82\n"
"ds_read_b64_tr_b16 a[100:101], v198 offset:8\n"
"v_mfma_f32_16x16x32_bf16 v[124:127], a[172:175], a[204:207], v[124:127]\n"
"s_add_u32 s1, s81, 0xc28000\n"
"ds_read_b64_tr_b16 a[102:103], v198 offset:264\n"
"v_mfma_f32_16x16x32_bf16 v[92:95], a[140:143], a[184:187], v[92:95]\n"
"s_mul_i32 s81, s79, 0x900000\n"
"buffer_load_dwordx4 v228, s[32:35], s92 offen lds\n"
"v_mfma_f32_16x16x32_bf16 v[92:95], a[172:175], a[200:203], v[92:95]\n"
"s_addk_i32 m0, 16320\n"
"ds_read_b64_tr_b16 a[108:109], v198 offset:8200\n"
"v_mfma_f32_16x16x32_bf16 v[60:63], a[140:143], a[180:183], v[60:63]\n"
"s_mul_i32 s82, s78, 0x900\n"
"ds_read_b64_tr_b16 a[110:111], v198 offset:8456\n"
"v_mfma_f32_16x16x32_bf16 v[60:63], a[172:175], a[196:199], v[60:63]\n"
"s_add_u32 s82, s81, s82\n"
"s_add_u32 s24, s48, s82\n"
"s_addc_u32 s25, s49, 0\n"
"v_mfma_f32_16x16x32_bf16 v[28:31], a[140:143], a[176:179], v[28:31]\n"
"s_sub_u32 s26, s50, s82\n"
"v_mfma_f32_16x16x32_bf16 v[28:31], a[172:175], a[192:195], v[28:31]\n"
"s_mov_b32 s27, 0x20000\n"
"v_mfma_f32_16x16x32_bf16 v[208:211], a[208:211], a[224:227], v[208:211]\n"
"s_mul_i32 s82, s78, 0x480000\n"
"buffer_load_dwordx4 v228, s[32:35], s92 offen offset:64 lds\n"
"v_mfma_f32_16x16x32_bf16 v[208:211], a[216:219], a[224:227], v[208:211]\n"
"s_lshl_b32 s83, s79, 11\n"
"v_mfma_f32_16x16x32_bf16 v[212:215], a[212:215], a[224:227], v[212:215]\n"
"s_add_u32 s82, s82, s83\n"
"v_mfma_f32_16x16x32_bf16 v[212:215], a[220:223], a[224:227], v[212:215]\n"
"s_waitcnt vmcnt(15) lgkmcnt(0)\n"
"s_barrier\n"
"v_mfma_f32_16x16x32_bf16 v[0:3], a[0:3], a[64:67], v[0:3]\n"
"s_add_u32 s93, s93, 1\n"
"s_cmp_eq_u32 s93, 9\n"
"s_cselect_b32 s93, 0, s93\n"
"s_lshl_b32 s90, s93, 19\n"
"v_mfma_f32_16x16x32_bf16 v[0:3], a[32:35], a[80:83], v[0:3]\n"
"s_add_u32 m0, s85, 0xc000\n"
"ds_read_b128 a[176:179], v194 offset:16384\n"
"v_mfma_f32_16x16x32_bf16 v[32:35], a[0:3], a[68:71], v[32:35]\n"
"s_add_u32 s28, s52, s82\n"
"s_addc_u32 s29, s53, 0\n"
"ds_read_b128 a[192:195], v194 offset:32768\n"
"v_mfma_f32_16x16x32_bf16 v[32:35], a[32:35], a[84:87], v[32:35]\n"
"s_sub_u32 s30, s54, s82\n"
"buffer_load_dwordx4 v229, s[36:39], s90 offen lds\n"
"ds_read_b128 a[180:183], v194 offset:17408\n"
"v_mfma_f32_16x16x32_bf16 v[64:67], a[0:3], a[72:75], v[64:67]\n"
"s_addk_i32 m0, 1984\n"
"ds_read_b128 a[196:199], v194 offset:33792\n"
"v_mfma_f32_16x16x32_bf16 v[64:67], a[32:35], a[88:91], v[64:67]\n"
"s_mov_b32 s31, 0x20000\n"
"ds_read_b128 a[184:187], v194 offset:18432\n"
"v_mfma_f32_16x16x32_bf16 v[96:99], a[0:3], a[76:79], v[96:99]\n"
"s_min_u32 s82, s80, 26\n"
"ds_read_b128 a[200:203], v194 offset:34816\n"
"v_mfma_f32_16x16x32_bf16 v[96:99], a[32:35], a[92:95], v[96:99]\n"
"s_mul_i32 s83, s82, 0x56\n"
"ds_read_b128 a[188:191], v194 offset:19456\n"
"v_mfma_f32_16x16x32_bf16 v[100:103], a[4:7], a[76:79], v[100:103]\n"
"s_lshr_b32 s83, s83, 8\n"
"buffer_load_dwordx4 v229, s[36:39], s90 offen offset:64 lds\n"
"v_mfma_f32_16x16x32_bf16 v[100:103], a[36:39], a[92:95], v[100:103]\n"
"s_add_u32 s90, s90, 0x40000\n"
"ds_read_b128 a[204:207], v194 offset:35840\n"
"v_mfma_f32_16x16x32_bf16 v[68:71], a[4:7], a[72:75], v[68:71]\n"
"s_addk_i32 m0, 6208\n"
"ds_read_b64_tr_b16 a[112:113], v196\n"
"v_mfma_f32_16x16x32_bf16 v[68:71], a[36:39], a[88:91], v[68:71]\n"
"s_mul_i32 s17, s83, 3\n"
"ds_read_b64_tr_b16 a[114:115], v196 offset:256\n"
"v_mfma_f32_16x16x32_bf16 v[36:39], a[4:7], a[68:71], v[36:39]\n"
"s_sub_u32 s18, s82, s17\n"
"ds_read_b64_tr_b16 a[144:145], v196 offset:8192\n"
"v_mfma_f32_16x16x32_bf16 v[36:39], a[36:39], a[84:87], v[36:39]\n"
"s_mul_i32 s17, s78, 9\n"
"buffer_load_dwordx4 v229, s[36:39], s90 offen lds\n"
"v_mfma_f32_16x16x32_bf16 v[4:7], a[4:7], a[64:67], v[4:7]\n"
"s_addk_i32 m0, 1984\n"
"ds_read_b64_tr_b16 a[146:147], v196 offset:8448\n"
"v_mfma_f32_16x16x32_bf16 v[4:7], a[36:39], a[80:83], v[4:7]\n"
"s_add_u32 s17, s17, s83\n"
"ds_read_b64_tr_b16 a[116:117], v196 offset:8\n"
"v_mfma_f32_16x16x32_bf16 v[8:11], a[8:11], a[64:67], v[8:11]\n"
"s_lshl_b32 s82, s17, 8\n"
"ds_read_b64_tr_b16 a[118:119], v196 offset:264\n"
"v_mfma_f32_16x16x32_bf16 v[8:11], a[40:43], a[80:83], v[8:11]\n"
"s_add_u32 s81, s81, s82\n"
"ds_read_b64_tr_b16 a[148:149], v196 offset:8200\n"
"v_mfma_f32_16x16x32_bf16 v[40:43], a[8:11], a[68:71], v[40:43]\n"
"s_add_u32 s48, s48, s81\n"
"s_addc_u32 s49, s49, 0\n"
"v_mfma_f32_16x16x32_bf16 v[40:43], a[40:43], a[84:87], v[40:43]\n"
"s_sub_u32 s50, s50, s81\n"
"buffer_load_dwordx4 v229, s[36:39], s90 offen offset:64 lds\n"
"v_mfma_f32_16x16x32_bf16 v[72:75], a[8:11], a[72:75], v[72:75]\n"
"s_lshl_b32 s92, s93, 7\n"
"ds_read_b64_tr_b16 a[150:151], v196 offset:8456\n"
"v_mfma_f32_16x16x32_bf16 v[72:75], a[40:43], a[88:91], v[72:75]\n"
"s_add_u32 m0, s89, 0x10000\n"
"ds_read_b64_tr_b16 a[120:121], v196 offset:2048\n"
"v_mfma_f32_16x16x32_bf16 v[104:107], a[8:11], a[76:79], v[104:107]\n"
"s_add_u32 s64, s64, s82\n"
"s_addc_u32 s65, s65, 0\n"
"ds_read_b64_tr_b16 a[122:123], v196 offset:2304\n"
"v_mfma_f32_16x16x32_bf16 v[104:107], a[40:43], a[92:95], v[104:107]\n"
"s_sub_u32 s66, s66, s82\n"
"ds_read_b64_tr_b16 a[152:153], v196 offset:10240\n"
"v_mfma_f32_16x16x32_bf16 v[108:111], a[12:15], a[76:79], v[108:111]\n"
"s_lshl_b32 s83, s79, 3\n"
"ds_read_b64_tr_b16 a[154:155], v196 offset:10496\n"
"v_mfma_f32_16x16x32_bf16 v[108:111], a[44:47], a[92:95], v[108:111]\n"
"s_lshr_b32 s81, s80, 2\n"
"buffer_load_dwordx4 v225, s[32:35], s92 offen lds\n"
"v_mfma_f32_16x16x32_bf16 v[76:79], a[12:15], a[72:75], v[76:79]\n"
"s_addk_i32 m0, 16320\n"
"ds_read_b64_tr_b16 a[124:125], v196 offset:2056\n"
"v_mfma_f32_16x16x32_bf16 v[76:79], a[44:47], a[88:91], v[76:79]\n"
"s_add_u32 s83, s83, s81\n"
"ds_read_b64_tr_b16 a[126:127], v196 offset:2312\n"
"v_mfma_f32_16x16x32_bf16 v[44:47], a[12:15], a[68:71], v[44:47]\n"
"s_mul_i32 s81, s78, 0x480000\n"
"ds_read_b64_tr_b16 a[156:157], v196 offset:10248\n"
"v_mfma_f32_16x16x32_bf16 v[44:47], a[44:47], a[84:87], v[44:47]\n"
"s_lshl_b32 s82, s83, 8\n"
"ds_read_b64_tr_b16 a[158:159], v196 offset:10504\n"
"v_mfma_f32_16x16x32_bf16 v[12:15], a[12:15], a[64:67], v[12:15]\n"
"s_add_u32 s81, s81, s82\n"
"buffer_load_dwordx4 v225, s[32:35], s92 offen offset:64 lds\n"
"v_mfma_f32_16x16x32_bf16 v[12:15], a[44:47], a[80:83], v[12:15]\n"
"s_addk_i32 m0, -15296\n"
"ds_read_b64_tr_b16 a[128:129], v196 offset:4096\n"
"v_mfma_f32_16x16x32_bf16 v[16:19], a[16:19], a[64:67], v[16:19]\n"
"s_add_u32 s52, s52, s81\n"
"s_addc_u32 s53, s53, 0\n"
"ds_read_b64_tr_b16 a[130:131], v196 offset:4352\n"
"v_mfma_f32_16x16x32_bf16 v[16:19], a[48:51], a[80:83], v[16:19]\n"
"s_waitcnt vmcnt(8)\n"
"s_barrier\n"
"ds_read_b64_tr_b16 a[160:161], v196 offset:12288\n"
"v_mfma_f32_16x16x32_bf16 v[48:51], a[16:19], a[68:71], v[48:51]\n"
"s_sub_u32 s54, s54, s81\n"
"ds_read_b64_tr_b16 a[162:163], v196 offset:12544\n"
"v_mfma_f32_16x16x32_bf16 v[48:51], a[48:51], a[84:87], v[48:51]\n"
"s_lshl_b32 s81, s78, 5\n"
"ds_read_b64_tr_b16 a[132:133], v196 offset:4104\n"
"v_mfma_f32_16x16x32_bf16 v[80:83], a[16:19], a[72:75], v[80:83]\n"
"s_add_u32 s81, s81, s80\n"
"buffer_load_dwordx4 v226, s[32:35], s92 offen lds\n"
"v_mfma_f32_16x16x32_bf16 v[80:83], a[48:51], a[88:91], v[80:83]\n"
"s_addk_i32 m0, 16320\n"
"ds_read_b64_tr_b16 a[134:135], v196 offset:4360\n"
"v_mfma_f32_16x16x32_bf16 v[112:115], a[16:19], a[76:79], v[112:115]\n"
"s_add_u32 s11, s8, 0x8000\n"
"ds_read_b64_tr_b16 a[164:165], v196 offset:12296\n"
"v_mov_b32 v182, s95\n"
"v_mfma_f32_16x16x32_bf16 v[112:115], a[48:51], a[92:95], v[112:115]\n"
"s_mul_i32 s82, s81, 0x9000\n"
"ds_read_b64_tr_b16 a[166:167], v196 offset:12552\n"
"buffer_store_dword v182, v234, s[56:59], s11 offen sc1\n"
"v_mfma_f32_16x16x32_bf16 v[116:119], a[20:23], a[76:79], v[116:119]\n"
"s_mul_i32 s83, s79, 0x240\n"
"ds_read_b64_tr_b16 a[136:137], v196 offset:6144\n"
"v_mfma_f32_16x16x32_bf16 v[116:119], a[52:55], a[92:95], v[116:119]\n"
"s_add_u32 s82, s82, s83\n"
"buffer_load_dwordx4 v226, s[32:35], s92 offen offset:64 lds\n"
"v_mfma_f32_16x16x32_bf16 v[84:87], a[20:23], a[72:75], v[84:87]\n"
"s_addk_i32 m0, -15296\n"
"ds_read_b64_tr_b16 a[138:139], v196 offset:6400\n"
"s_add_u32 s72, s72, s82\n"
"s_addc_u32 s73, s73, 0\n"
"v_mfma_f32_16x16x32_bf16 v[84:87], a[52:55], a[88:91], v[84:87]\n"
"s_sub_u32 s74, s74, s82\n"
"ds_read_b64_tr_b16 a[168:169], v196 offset:14336\n"
"v_mfma_f32_16x16x32_bf16 v[52:55], a[20:23], a[68:71], v[52:55]\n"
"s_lshr_b32 s82, s81, 1\n"
"ds_read_b64_tr_b16 a[170:171], v196 offset:14592\n"
"v_mfma_f32_16x16x32_bf16 v[52:55], a[52:55], a[84:87], v[52:55]\n"
"s_mul_i32 s82, s82, 36\n"
"ds_read_b64_tr_b16 a[140:141], v196 offset:6152\n"
"v_mfma_f32_16x16x32_bf16 v[20:23], a[20:23], a[64:67], v[20:23]\n"
"s_mul_i32 s83, s79, 18\n"
"v_mfma_f32_16x16x32_bf16 v[20:23], a[52:55], a[80:83], v[20:23]\n"
"s_add_u32 s82, s82, s83\n"
"buffer_load_dwordx4 v227, s[32:35], s92 offen lds\n"
"v_mfma_f32_16x16x32_bf16 v[24:27], a[24:27], a[64:67], v[24:27]\n"
"s_addk_i32 m0, 16320\n"
"ds_read_b64_tr_b16 a[142:143], v196 offset:6408\n"
"v_mfma_f32_16x16x32_bf16 v[24:27], a[56:59], a[80:83], v[24:27]\n"
"s_add_u32 s82, s82, s76\n"
"ds_read_b64_tr_b16 a[172:173], v196 offset:14344\n"
"v_mfma_f32_16x16x32_bf16 v[56:59], a[24:27], a[68:71], v[56:59]\n"
"s_lshl_b32 s82, s82, 1\n"
"ds_read_b64_tr_b16 a[174:175], v196 offset:14600\n"
"v_mfma_f32_16x16x32_bf16 v[56:59], a[56:59], a[84:87], v[56:59]\n"
"s_and_b32 s81, s81, 1\n"
"ds_read_b64_tr_b16 a[208:209], v199\n"
"v_mfma_f32_16x16x32_bf16 v[88:91], a[24:27], a[72:75], v[88:91]\n"
"s_add_u32 s82, s82, s81\n"
"ds_read_b64_tr_b16 a[210:211], v199 offset:256\n"
"v_mfma_f32_16x16x32_bf16 v[88:91], a[56:59], a[88:91], v[88:91]\n"
"s_lshl_b32 s82, s82, 10\n"
"buffer_load_dwordx4 v227, s[32:35], s92 offen offset:64 lds\n"
"v_mfma_f32_16x16x32_bf16 v[120:123], a[24:27], a[76:79], v[120:123]\n"
"s_addk_i32 m0, -15296\n"
"ds_read_b64_tr_b16 a[216:217], v199 offset:8192\n"
"v_mfma_f32_16x16x32_bf16 v[120:123], a[56:59], a[92:95], v[120:123]\n"
"s_add_u32 s16, s82, 0x298000\n"
"ds_read_b64_tr_b16 a[218:219], v199 offset:8448\n"
"v_mfma_f32_16x16x32_bf16 v[124:127], a[28:31], a[76:79], v[124:127]\n"
"s_lshl_b32 s81, s17, 1\n"
"ds_read_b64_tr_b16 a[212:213], v199 offset:8\n"
"v_mfma_f32_16x16x32_bf16 v[124:127], a[60:63], a[92:95], v[124:127]\n"
"s_lshr_b32 s82, s76, 1\n"
"ds_read_b64_tr_b16 a[214:215], v199 offset:264\n"
"v_mfma_f32_16x16x32_bf16 v[92:95], a[28:31], a[72:75], v[92:95]\n"
"buffer_load_dwordx4 v228, s[32:35], s92 offen lds\n"
"v_mfma_f32_16x16x32_bf16 v[92:95], a[60:63], a[88:91], v[92:95]\n"
"s_addk_i32 m0, 16320\n"
"ds_read_b64_tr_b16 a[220:221], v199 offset:8200\n"
"v_mfma_f32_16x16x32_bf16 v[60:63], a[28:31], a[68:71], v[60:63]\n"
"s_add_u32 s81, s81, s82\n"
"ds_read_b64_tr_b16 a[222:223], v199 offset:8456\n"
"v_mfma_f32_16x16x32_bf16 v[60:63], a[60:63], a[84:87], v[60:63]\n"
"s_mul_i32 s81, s81, 36\n"
"v_mfma_f32_16x16x32_bf16 v[28:31], a[28:31], a[64:67], v[28:31]\n"
"s_mul_i32 s82, s18, 12\n"
"v_mfma_f32_16x16x32_bf16 v[28:31], a[60:63], a[80:83], v[28:31]\n"
"s_add_u32 s81, s81, s82\n"
"v_mfma_f32_16x16x32_bf16 v[208:211], a[96:99], a[224:227], v[208:211]\n"
"s_mul_i32 s83, s79, 6\n"
"buffer_load_dwordx4 v228, s[32:35], s92 offen offset:64 lds\n"
"v_mfma_f32_16x16x32_bf16 v[208:211], a[104:107], a[224:227], v[208:211]\n"
"s_add_u32 s81, s81, s83\n"
"v_mfma_f32_16x16x32_bf16 v[212:215], a[100:103], a[224:227], v[212:215]\n"
"v_mfma_f32_16x16x32_bf16 v[212:215], a[108:111], a[224:227], v[212:215]\n"
"s_waitcnt vmcnt(13) lgkmcnt(0)\n"
"s_barrier\n"
"v_mfma_f32_16x16x32_bf16 v[0:3], a[112:115], a[176:179], v[0:3]\n"
"s_add_u32 s93, s93, 1\n"
"s_cmp_eq_u32 s93, 9\n"
"s_cselect_b32 s93, 0, s93\n"
"s_lshl_b32 s90, s93, 19\n"
"v_mfma_f32_16x16x32_bf16 v[0:3], a[144:147], a[192:195], v[0:3]\n"
"s_add_u32 m0, s85, 0x18000\n"
"ds_read_b128 a[64:67], v192 offset:16384\n"
"v_mfma_f32_16x16x32_bf16 v[32:35], a[112:115], a[180:183], v[32:35]\n"
"s_lshl_b32 s81, s81, 1\n"
"buffer_load_dwordx4 v229, s[36:39], s90 offen lds\n"
"ds_read_b128 a[80:83], v192 offset:32768\n"
"v_mfma_f32_16x16x32_bf16 v[32:35], a[144:147], a[196:199], v[32:35]\n"
"s_addk_i32 m0, 1984\n"
"ds_read_b128 a[68:71], v192 offset:17408\n"
"v_mfma_f32_16x16x32_bf16 v[64:67], a[112:115], a[184:187], v[64:67]\n"
"s_and_b32 s83, s76, 1\n"
"ds_read_b128 a[84:87], v192 offset:33792\n"
"v_mfma_f32_16x16x32_bf16 v[64:67], a[144:147], a[200:203], v[64:67]\n"
"s_add_u32 s81, s81, s83\n"
"ds_read_b128 a[72:75], v192 offset:18432\n"
"v_mfma_f32_16x16x32_bf16 v[96:99], a[112:115], a[188:191], v[96:99]\n"
"s_lshl_b32 s81, s81, 10\n"
"ds_read_b128 a[88:91], v192 offset:34816\n"
"v_mfma_f32_16x16x32_bf16 v[96:99], a[144:147], a[204:207], v[96:99]\n"
"s_add_u32 s0, s81, 0x718000\n"
"buffer_load_dwordx4 v229, s[36:39], s90 offen offset:64 lds\n"
"v_mfma_f32_16x16x32_bf16 v[100:103], a[116:119], a[188:191], v[100:103]\n"
"s_add_u32 s90, s90, 0x40000\n"
"ds_read_b128 a[76:79], v192 offset:19456\n"
"v_mfma_f32_16x16x32_bf16 v[100:103], a[148:151], a[204:207], v[100:103]\n"
"s_addk_i32 m0, 6208\n"
"ds_read_b128 a[92:95], v192 offset:35840\n"
"v_mfma_f32_16x16x32_bf16 v[68:71], a[116:119], a[184:187], v[68:71]\n"
"s_mul_i32 s81, s79, 0x240\n"
"ds_read_b64_tr_b16 a[0:1], v230\n"
"v_mfma_f32_16x16x32_bf16 v[68:71], a[148:151], a[200:203], v[68:71]\n"
"s_add_u32 s81, s81, s82\n"
"buffer_load_dwordx4 v229, s[36:39], s90 offen lds\n"
"v_mfma_f32_16x16x32_bf16 v[36:39], a[116:119], a[180:183], v[36:39]\n"
"s_addk_i32 m0, 1984\n"
"ds_read_b64_tr_b16 a[2:3], v230 offset:256\n"
"v_mfma_f32_16x16x32_bf16 v[36:39], a[148:151], a[196:199], v[36:39]\n"
"s_lshl_b32 s81, s81, 11\n"
"ds_read_b64_tr_b16 a[32:33], v230 offset:8192\n"
"v_mfma_f32_16x16x32_bf16 v[4:7], a[116:119], a[176:179], v[4:7]\n"
"s_add_u32 s6, s81, 0x58000\n"
"ds_read_b64_tr_b16 a[34:35], v230 offset:8448\n"
"v_mfma_f32_16x16x32_bf16 v[4:7], a[148:151], a[192:195], v[4:7]\n"
"s_mul_i32 s81, s8, 0xc0\n"
"ds_read_b64_tr_b16 a[4:5], v230 offset:8\n"
"v_mfma_f32_16x16x32_bf16 v[8:11], a[120:123], a[176:179], v[8:11]\n"
"s_add_u32 s21, s81, s87\n"
"buffer_load_dwordx4 v229, s[36:39], s90 offen offset:64 lds\n"
"v_mfma_f32_16x16x32_bf16 v[8:11], a[152:155], a[192:195], v[8:11]\n"
"s_lshl_b32 s92, s93, 7\n"
"ds_read_b64_tr_b16 a[6:7], v230 offset:264\n"
"v_mfma_f32_16x16x32_bf16 v[40:43], a[120:123], a[180:183], v[40:43]\n"
"s_add_u32 m0, s89, 0x1c000\n"
"ds_read_b64_tr_b16 a[36:37], v230 offset:8200\n"
"v_mfma_f32_16x16x32_bf16 v[40:43], a[152:155], a[196:199], v[40:43]\n"
"s_lshl_b32 s83, s80, 2\n"
"ds_read_b64_tr_b16 a[38:39], v230 offset:8456\n"
"v_mfma_f32_16x16x32_bf16 v[72:75], a[120:123], a[184:187], v[72:75]\n"
"s_add_u32 s83, s83, s76\n"
"ds_read_b64_tr_b16 a[8:9], v230 offset:2048\n"
"v_mfma_f32_16x16x32_bf16 v[72:75], a[152:155], a[200:203], v[72:75]\n"
"s_mul_i32 s81, s83, 24\n"
"buffer_load_dwordx4 v225, s[32:35], s92 offen lds\n"
"v_add_u32 v251, s81, v224\n"
"v_mfma_f32_16x16x32_bf16 v[104:107], a[120:123], a[188:191], v[104:107]\n"
"s_addk_i32 m0, 16320\n"
"ds_read_b64_tr_b16 a[10:11], v230 offset:2304\n"
"v_mul_u32_u24 v252, 0xe39, v251\n"
"v_mfma_f32_16x16x32_bf16 v[104:107], a[152:155], a[204:207], v[104:107]\n"
"ds_read_b64_tr_b16 a[40:41], v230 offset:10240\n"
"v_lshrrev_b32 v252, 16, v252\n"
"v_mfma_f32_16x16x32_bf16 v[108:111], a[124:127], a[188:191], v[108:111]\n"
"ds_read_b64_tr_b16 a[42:43], v230 offset:10496\n"
"v_mul_u32_u24 v253, 18, v252\n"
"v_mfma_f32_16x16x32_bf16 v[108:111], a[156:159], a[204:207], v[108:111]\n"
"ds_read_b64_tr_b16 a[12:13], v230 offset:2056\n"
"v_sub_u32 v253, v251, v253\n"
"v_mfma_f32_16x16x32_bf16 v[76:79], a[124:127], a[184:187], v[76:79]\n"
"buffer_load_dwordx4 v225, s[32:35], s92 offen offset:64 lds\n"
"v_mfma_f32_16x16x32_bf16 v[76:79], a[156:159], a[200:203], v[76:79]\n"
"s_addk_i32 m0, -15296\n"
"ds_read_b64_tr_b16 a[14:15], v230 offset:2312\n"
"v_mul_u32_u24 v252, 0x2400, v252\n"
"v_mfma_f32_16x16x32_bf16 v[44:47], a[124:127], a[180:183], v[44:47]\n"
"ds_read_b64_tr_b16 a[44:45], v230 offset:10248\n"
"v_lshl_add_u32 v186, v253, 7, v252\n"
"v_mfma_f32_16x16x32_bf16 v[44:47], a[156:159], a[196:199], v[44:47]\n"
"ds_read_b64_tr_b16 a[46:47], v230 offset:10504\n"
"v_lshlrev_b32 v251, 7, v224\n"
"v_mfma_f32_16x16x32_bf16 v[12:15], a[124:127], a[176:179], v[12:15]\n"
"ds_read_b64_tr_b16 a[16:17], v230 offset:4096\n"
"v_mov_b32 v252, 0x7ffffff0\n"
"v_mfma_f32_16x16x32_bf16 v[12:15], a[156:159], a[192:195], v[12:15]\n"
"s_mul_i32 s81, s83, 0x1200\n"
"buffer_load_dwordx4 v226, s[32:35], s92 offen lds\n"
"v_mfma_f32_16x16x32_bf16 v[16:19], a[128:131], a[176:179], v[16:19]\n"
"s_addk_i32 m0, 16320\n"
"ds_read_b64_tr_b16 a[18:19], v230 offset:4352\n"
"v_add_u32 v187, s81, v251\n"
"v_mfma_f32_16x16x32_bf16 v[16:19], a[160:163], a[192:195], v[16:19]\n"
"ds_read_b64_tr_b16 a[48:49], v230 offset:12288\n"
"v_mfma_f32_16x16x32_bf16 v[48:51], a[128:131], a[180:183], v[48:51]\n"
"ds_read_b64_tr_b16 a[50:51], v230 offset:12544\n"
"v_mfma_f32_16x16x32_bf16 v[48:51], a[160:163], a[196:199], v[48:51]\n"
"buffer_load_dwordx4 v226, s[32:35], s92 offen offset:64 lds\n"
"v_mfma_f32_16x16x32_bf16 v[80:83], a[128:131], a[184:187], v[80:83]\n"
"s_addk_i32 m0, -15296\n"
"ds_read_b64_tr_b16 a[20:21], v230 offset:4104\n"
"v_cmp_gt_u32 vcc, 24, v224\n"
"s_nop 1\n"
"v_cndmask_b32 v186, v252, v186, vcc\n"
"v_mfma_f32_16x16x32_bf16 v[80:83], a[160:163], a[200:203], v[80:83]\n"
"ds_read_b64_tr_b16 a[22:23], v230 offset:4360\n"
"v_cmp_gt_u32 vcc, 36, v224\n"
"s_nop 1\n"
"v_cndmask_b32 v187, v252, v187, vcc\n"
"v_mfma_f32_16x16x32_bf16 v[112:115], a[128:131], a[188:191], v[112:115]\n"
"ds_read_b64_tr_b16 a[52:53], v230 offset:12296\n"
"v_lshrrev_b32 v251, 2, v224\n"
"v_mfma_f32_16x16x32_bf16 v[112:115], a[160:163], a[204:207], v[112:115]\n"
"ds_read_b64_tr_b16 a[54:55], v230 offset:12552\n"
"v_mul_u32_u24 v251, 0x2400, v251\n"
"v_mfma_f32_16x16x32_bf16 v[116:119], a[132:135], a[188:191], v[116:119]\n"
"buffer_load_dwordx4 v227, s[32:35], s92 offen lds\n"
"v_mfma_f32_16x16x32_bf16 v[116:119], a[164:167], a[204:207], v[116:119]\n"
"s_addk_i32 m0, 16320\n"
"ds_read_b64_tr_b16 a[24:25], v230 offset:6144\n"
"v_and_b32 v252, 3, v224\n"
"v_mfma_f32_16x16x32_bf16 v[84:87], a[132:135], a[184:187], v[84:87]\n"
"ds_read_b64_tr_b16 a[26:27], v230 offset:6400\n"
"v_bfe_u32 v253, v224, 4, 1\n"
"v_mfma_f32_16x16x32_bf16 v[84:87], a[164:167], a[200:203], v[84:87]\n"
"ds_read_b64_tr_b16 a[56:57], v230 offset:14336\n"
"v_lshlrev_b32 v253, 1, v253\n"
"v_mfma_f32_16x16x32_bf16 v[52:55], a[132:135], a[180:183], v[52:55]\n"
"ds_read_b64_tr_b16 a[58:59], v230 offset:14592\n"
"v_xor_b32 v252, v252, v253\n"
"v_mfma_f32_16x16x32_bf16 v[52:55], a[164:167], a[196:199], v[52:55]\n"
"buffer_load_dwordx4 v227, s[32:35], s92 offen offset:64 lds\n"
"v_mfma_f32_16x16x32_bf16 v[20:23], a[132:135], a[176:179], v[20:23]\n"
"s_addk_i32 m0, -15296\n"
"ds_read_b64_tr_b16 a[28:29], v230 offset:6152\n"
"v_lshl_add_u32 v251, v252, 4, v251\n"
"v_mfma_f32_16x16x32_bf16 v[20:23], a[164:167], a[192:195], v[20:23]\n"
"s_lshr_b32 s82, s76, 1\n"
"ds_read_b64_tr_b16 a[30:31], v230 offset:6408\n"
"v_mfma_f32_16x16x32_bf16 v[24:27], a[136:139], a[176:179], v[24:27]\n"
"s_mul_i32 s82, s82, 0x24000\n"
"ds_read_b64_tr_b16 a[60:61], v230 offset:14344\n"
"v_mfma_f32_16x16x32_bf16 v[24:27], a[168:171], a[192:195], v[24:27]\n"
"s_and_b32 s83, s76, 1\n"
"ds_read_b64_tr_b16 a[62:63], v230 offset:14600\n"
"v_mfma_f32_16x16x32_bf16 v[56:59], a[136:139], a[180:183], v[56:59]\n"
"s_lshl_b32 s83, s83, 7\n"
"buffer_load_dwordx4 v228, s[32:35], s92 offen lds\n"
"v_mfma_f32_16x16x32_bf16 v[56:59], a[168:171], a[196:199], v[56:59]\n"
"s_addk_i32 m0, 16320\n"
"ds_read_b64_tr_b16 a[96:97], v197\n"
"v_mfma_f32_16x16x32_bf16 v[88:91], a[136:139], a[184:187], v[88:91]\n"
"s_add_u32 s82, s82, s83\n"
"ds_read_b64_tr_b16 a[98:99], v197 offset:256\n"
"v_add_u32 v231, s82, v251\n"
"v_mfma_f32_16x16x32_bf16 v[88:91], a[168:171], a[200:203], v[88:91]\n"
"ds_read_b64_tr_b16 a[104:105], v197 offset:8192\n"
"v_mfma_f32_16x16x32_bf16 v[120:123], a[136:139], a[188:191], v[120:123]\n"
"ds_read_b64_tr_b16 a[106:107], v197 offset:8448\n"
"v_mfma_f32_16x16x32_bf16 v[120:123], a[168:171], a[204:207], v[120:123]\n"
"buffer_load_dwordx4 v228, s[32:35], s92 offen offset:64 lds\n"
"v_mfma_f32_16x16x32_bf16 v[124:127], a[140:143], a[188:191], v[124:127]\n"
"s_mov_b32 s84, 0x482400\n"
"ds_read_b64_tr_b16 a[100:101], v197 offset:8\n"
"v_mfma_f32_16x16x32_bf16 v[124:127], a[172:175], a[204:207], v[124:127]\n"
"ds_read_b64_tr_b16 a[102:103], v197 offset:264\n"
"v_mfma_f32_16x16x32_bf16 v[92:95], a[140:143], a[184:187], v[92:95]\n"
"ds_read_b64_tr_b16 a[108:109], v197 offset:8200\n"
"v_mfma_f32_16x16x32_bf16 v[92:95], a[172:175], a[200:203], v[92:95]\n"
"buffer_load_dword v250, v186, s[24:27], s84 offen\n"
"v_mfma_f32_16x16x32_bf16 v[60:63], a[140:143], a[180:183], v[60:63]\n"
"s_mov_b32 s84, 0x603000\n"
"ds_read_b64_tr_b16 a[110:111], v197 offset:8456\n"
"v_mfma_f32_16x16x32_bf16 v[60:63], a[172:175], a[196:199], v[60:63]\n"
"v_mfma_f32_16x16x32_bf16 v[28:31], a[140:143], a[176:179], v[28:31]\n"
"v_mfma_f32_16x16x32_bf16 v[28:31], a[172:175], a[192:195], v[28:31]\n"
"v_mfma_f32_16x16x32_bf16 v[208:211], a[208:211], a[224:227], v[208:211]\n"
"buffer_load_dword v250, v186, s[24:27], s84 offen\n"
"v_mfma_f32_16x16x32_bf16 v[208:211], a[216:219], a[224:227], v[208:211]\n"
"v_mfma_f32_16x16x32_bf16 v[212:215], a[212:215], a[224:227], v[212:215]\n"
"v_mfma_f32_16x16x32_bf16 v[212:215], a[220:223], a[224:227], v[212:215]\n"
"s_waitcnt vmcnt(14) lgkmcnt(0)\n"
"s_barrier\n"
"v_mfma_f32_16x16x32_bf16 v[0:3], a[0:3], a[64:67], v[0:3]\n"
"s_lshl_b32 s81, s76, 11\n"
"ds_read_b128 a[176:179], v193 offset:16384\n"
"v_mfma_f32_16x16x32_bf16 v[0:3], a[32:35], a[80:83], v[0:3]\n"
"ds_read_b128 a[192:195], v193 offset:32768\n"
"v_mfma_f32_16x16x32_bf16 v[32:35], a[0:3], a[68:71], v[32:35]\n"
"ds_read_b128 a[180:183], v193 offset:17408\n"
"v_add_u32 v238, s81, v247\n"
"v_mfma_f32_16x16x32_bf16 v[32:35], a[32:35], a[84:87], v[32:35]\n"
"ds_read_b128 a[196:199], v193 offset:33792\n"
"v_mfma_f32_16x16x32_bf16 v[64:67], a[0:3], a[72:75], v[64:67]\n"
"v_mfma_f32_16x16x32_bf16 v[64:67], a[32:35], a[88:91], v[64:67]\n"
"ds_read_b128 a[184:187], v193 offset:18432\n"
"v_add_u32 v240, 0x10000, v238\n"
"v_mfma_f32_16x16x32_bf16 v[96:99], a[0:3], a[76:79], v[96:99]\n"
"ds_read_b128 a[200:203], v193 offset:34816\n"
"v_mfma_f32_16x16x32_bf16 v[96:99], a[32:35], a[92:95], v[96:99]\n"
"ds_read_b128 a[188:191], v193 offset:19456\n"
"v_mfma_f32_16x16x32_bf16 v[100:103], a[4:7], a[76:79], v[100:103]\n"
"ds_read_b128 a[204:207], v193 offset:35840\n"
"v_add_u32 v242, 0x20000, v238\n"
"v_mfma_f32_16x16x32_bf16 v[100:103], a[36:39], a[92:95], v[100:103]\n"
"v_mfma_f32_16x16x32_bf16 v[68:71], a[4:7], a[72:75], v[68:71]\n"
"ds_read_b64_tr_b16 a[112:113], v195\n"
"v_mfma_f32_16x16x32_bf16 v[68:71], a[36:39], a[88:91], v[68:71]\n"
"ds_read_b64_tr_b16 a[114:115], v195 offset:256\n"
"v_add_u32 v239, s81, v248\n"
"v_mfma_f32_16x16x32_bf16 v[36:39], a[4:7], a[68:71], v[36:39]\n"
"ds_read_b64_tr_b16 a[144:145], v195 offset:8192\n"
"v_mfma_f32_16x16x32_bf16 v[36:39], a[36:39], a[84:87], v[36:39]\n"
"v_mfma_f32_16x16x32_bf16 v[4:7], a[4:7], a[64:67], v[4:7]\n"
"ds_read_b64_tr_b16 a[146:147], v195 offset:8448\n"
"v_add_u32 v241, 0x10000, v239\n"
"v_mfma_f32_16x16x32_bf16 v[4:7], a[36:39], a[80:83], v[4:7]\n"
"ds_read_b64_tr_b16 a[116:117], v195 offset:8\n"
"v_mfma_f32_16x16x32_bf16 v[8:11], a[8:11], a[64:67], v[8:11]\n"
"ds_read_b64_tr_b16 a[118:119], v195 offset:264\n"
"v_mfma_f32_16x16x32_bf16 v[8:11], a[40:43], a[80:83], v[8:11]\n"
"ds_read_b64_tr_b16 a[148:149], v195 offset:8200\n"
"v_add_u32 v243, 0x20000, v239\n"
"v_mfma_f32_16x16x32_bf16 v[40:43], a[8:11], a[68:71], v[40:43]\n"
"v_mfma_f32_16x16x32_bf16 v[40:43], a[40:43], a[84:87], v[40:43]\n"
"ds_read_b64_tr_b16 a[150:151], v195 offset:8456\n"
"v_mfma_f32_16x16x32_bf16 v[72:75], a[8:11], a[72:75], v[72:75]\n"
"s_and_b32 s81, s76, 1\n"
"ds_read_b64_tr_b16 a[120:121], v195 offset:2048\n"
"v_mfma_f32_16x16x32_bf16 v[72:75], a[40:43], a[88:91], v[72:75]\n"
"ds_read_b64_tr_b16 a[122:123], v195 offset:2304\n"
"v_mfma_f32_16x16x32_bf16 v[104:107], a[8:11], a[76:79], v[104:107]\n"
"ds_read_b64_tr_b16 a[152:153], v195 offset:10240\n"
"v_mfma_f32_16x16x32_bf16 v[104:107], a[40:43], a[92:95], v[104:107]\n"
"s_mul_i32 s81, s81, 0x1800\n"
"v_mfma_f32_16x16x32_bf16 v[108:111], a[12:15], a[76:79], v[108:111]\n"
"ds_read_b64_tr_b16 a[154:155], v195 offset:10496\n"
"v_mfma_f32_16x16x32_bf16 v[108:111], a[44:47], a[92:95], v[108:111]\n"
"ds_read_b64_tr_b16 a[124:125], v195 offset:2056\n"
"v_mfma_f32_16x16x32_bf16 v[76:79], a[12:15], a[72:75], v[76:79]\n"
"s_lshr_b32 s82, s76, 1\n"
"ds_read_b64_tr_b16 a[126:127], v195 offset:2312\n"
"v_mfma_f32_16x16x32_bf16 v[76:79], a[44:47], a[88:91], v[76:79]\n"
"v_mfma_f32_16x16x32_bf16 v[44:47], a[12:15], a[68:71], v[44:47]\n"
"ds_read_b64_tr_b16 a[156:157], v195 offset:10248\n"
"v_mfma_f32_16x16x32_bf16 v[44:47], a[44:47], a[84:87], v[44:47]\n"
"s_xor_b32 s82, s82, s79\n"
"ds_read_b64_tr_b16 a[158:159], v195 offset:10504\n"
"v_mfma_f32_16x16x32_bf16 v[12:15], a[12:15], a[64:67], v[12:15]\n"
"s_lshl_b32 s84, s76, 13\n"
"ds_read_b64_tr_b16 a[128:129], v195 offset:4096\n"
"v_lshlrev_b32 v254, 7, v224\n"
"v_mfma_f32_16x16x32_bf16 v[12:15], a[44:47], a[80:83], v[12:15]\n"
"s_add_u32 s84, s84, 0x8000\n"
"ds_read_b64_tr_b16 a[130:131], v195 offset:4352\n"
"v_mfma_f32_16x16x32_bf16 v[16:19], a[16:19], a[64:67], v[16:19]\n"
"s_mul_i32 s82, s82, 0x3000\n"
"v_mfma_f32_16x16x32_bf16 v[16:19], a[48:51], a[80:83], v[16:19]\n"
"buffer_load_dword v249, v254, s[56:59], s84 offen sc1\n"
"v_mfma_f32_16x16x32_bf16 v[48:51], a[16:19], a[68:71], v[48:51]\n"
"s_mov_b32 s91, 0\n"
"ds_read_b64_tr_b16 a[160:161], v195 offset:12288\n"
"v_mfma_f32_16x16x32_bf16 v[48:51], a[48:51], a[84:87], v[48:51]\n"
"s_add_u32 m0, s85, 0x6000\n"
"ds_read_b64_tr_b16 a[162:163], v195 offset:12544\n"
"v_mfma_f32_16x16x32_bf16 v[80:83], a[16:19], a[72:75], v[80:83]\n"
"s_add_u32 s81, s81, s82\n"
"ds_read_b64_tr_b16 a[132:133], v195 offset:4104\n"
"v_mfma_f32_16x16x32_bf16 v[80:83], a[48:51], a[88:91], v[80:83]\n"
"ds_read_b64_tr_b16 a[134:135], v195 offset:4360\n"
"v_add_u32 v232, s81, v235\n"
"v_mfma_f32_16x16x32_bf16 v[112:115], a[16:19], a[76:79], v[112:115]\n"
"ds_read_b64_tr_b16 a[164:165], v195 offset:12296\n"
"v_mfma_f32_16x16x32_bf16 v[112:115], a[48:51], a[92:95], v[112:115]\n"
"ds_read_b64_tr_b16 a[166:167], v195 offset:12552\n"
"v_mfma_f32_16x16x32_bf16 v[116:119], a[20:23], a[76:79], v[116:119]\n"
"ds_read_b64_tr_b16 a[136:137], v195 offset:6144\n"
"v_add_u32 v233, s88, v235\n"
"v_mfma_f32_16x16x32_bf16 v[116:119], a[52:55], a[92:95], v[116:119]\n"
"buffer_load_dwordx4 v231, s[48:51], s91 offen lds\n"
"v_mfma_f32_16x16x32_bf16 v[84:87], a[20:23], a[72:75], v[84:87]\n"
"s_addk_i32 m0, 1984\n"
"ds_read_b64_tr_b16 a[138:139], v195 offset:6400\n"
"v_mfma_f32_16x16x32_bf16 v[84:87], a[52:55], a[88:91], v[84:87]\n"
"ds_read_b64_tr_b16 a[168:169], v195 offset:14336\n"
"v_add_u32 v247, 0x1c000, v247\n"
"v_mfma_f32_16x16x32_bf16 v[52:55], a[20:23], a[68:71], v[52:55]\n"
"ds_read_b64_tr_b16 a[170:171], v195 offset:14592\n"
"v_mfma_f32_16x16x32_bf16 v[52:55], a[52:55], a[84:87], v[52:55]\n"
"ds_read_b64_tr_b16 a[140:141], v195 offset:6152\n"
"v_mfma_f32_16x16x32_bf16 v[20:23], a[20:23], a[64:67], v[20:23]\n"
"v_add_u32 v248, 0x1c000, v248\n"
"v_mfma_f32_16x16x32_bf16 v[20:23], a[52:55], a[80:83], v[20:23]\n"
"ds_read_b64_tr_b16 a[142:143], v195 offset:6408\n"
"v_mfma_f32_16x16x32_bf16 v[24:27], a[24:27], a[64:67], v[24:27]\n"
"buffer_load_dwordx4 v231, s[48:51], s91 offen offset:64 lds\n"
"v_mfma_f32_16x16x32_bf16 v[24:27], a[56:59], a[80:83], v[24:27]\n"
"s_add_u32 s91, s91, 0x48000\n"
"ds_read_b64_tr_b16 a[172:173], v195 offset:14344\n"
"v_lshrrev_b32 v251, 4, v224\n"
"v_mfma_f32_16x16x32_bf16 v[56:59], a[24:27], a[68:71], v[56:59]\n"
"s_addk_i32 m0, 6208\n"
"ds_read_b64_tr_b16 a[174:175], v195 offset:14600\n"
"v_mfma_f32_16x16x32_bf16 v[56:59], a[56:59], a[84:87], v[56:59]\n"
"ds_read_b64_tr_b16 a[208:209], v198\n"
"v_mfma_f32_16x16x32_bf16 v[88:91], a[24:27], a[72:75], v[88:91]\n"
"ds_read_b64_tr_b16 a[210:211], v198 offset:256\n"
"v_lshlrev_b32 v251, 3, v251\n"
"v_mfma_f32_16x16x32_bf16 v[88:91], a[56:59], a[88:91], v[88:91]\n"
"ds_read_b64_tr_b16 a[216:217], v198 offset:8192\n"
"v_mfma_f32_16x16x32_bf16 v[120:123], a[24:27], a[76:79], v[120:123]\n"
"ds_read_b64_tr_b16 a[218:219], v198 offset:8448\n"
"v_mfma_f32_16x16x32_bf16 v[120:123], a[56:59], a[92:95], v[120:123]\n"
"s_lshl_b32 s81, s76, 6\n"
"buffer_load_dwordx4 v231, s[48:51], s91 offen lds\n"
"v_mfma_f32_16x16x32_bf16 v[124:127], a[28:31], a[76:79], v[124:127]\n"
"s_addk_i32 m0, 1984\n"
"ds_read_b64_tr_b16 a[212:213], v198 offset:8\n"
"v_mfma_f32_16x16x32_bf16 v[124:127], a[60:63], a[92:95], v[124:127]\n"
"ds_read_b64_tr_b16 a[214:215], v198 offset:264\n"
"v_mfma_f32_16x16x32_bf16 v[92:95], a[28:31], a[72:75], v[92:95]\n"
"ds_read_b64_tr_b16 a[220:221], v198 offset:8200\n"
"v_mfma_f32_16x16x32_bf16 v[92:95], a[60:63], a[88:91], v[92:95]\n"
"ds_read_b64_tr_b16 a[222:223], v198 offset:8456\n"
"v_mfma_f32_16x16x32_bf16 v[60:63], a[28:31], a[68:71], v[60:63]\n"
"buffer_load_dwordx2 a[228:229], v251, s[64:67], s81 offen\n"
"v_mfma_f32_16x16x32_bf16 v[60:63], a[60:63], a[84:87], v[60:63]\n"
"buffer_load_dwordx2 a[230:231], v251, s[64:67], s81 offen offset:32\n"
"v_mfma_f32_16x16x32_bf16 v[28:31], a[28:31], a[64:67], v[28:31]\n"
"v_mfma_f32_16x16x32_bf16 v[28:31], a[60:63], a[80:83], v[28:31]\n"
"buffer_load_dwordx4 v231, s[48:51], s91 offen offset:64 lds\n"
"v_mfma_f32_16x16x32_bf16 v[208:211], a[96:99], a[224:227], v[208:211]\n"
"v_mfma_f32_16x16x32_bf16 v[208:211], a[104:107], a[224:227], v[208:211]\n"
"v_mfma_f32_16x16x32_bf16 v[212:215], a[100:103], a[224:227], v[212:215]\n"
"v_mfma_f32_16x16x32_bf16 v[212:215], a[108:111], a[224:227], v[212:215]\n"
"s_waitcnt vmcnt(9) lgkmcnt(0)\n"
"s_waitcnt vmcnt(6)\n"
"v_cmp_ne_u32 vcc, s95, v249\n"
"s_cbranch_vccnz L_slow0\n"
"L_back0:\n"
"s_barrier\n"
"v_mfma_f32_16x16x32_bf16 v[0:3], a[112:115], a[176:179], v[0:3]\n"
"s_mov_b32 s92, s6\n"
"s_add_u32 m0, s87, 0\n"
"ds_read_b128 a[64:67], v194 offset:16384\n"
"v_mfma_f32_16x16x32_bf16 v[0:3], a[144:147], a[192:195], v[0:3]\n"
"ds_read_b128 a[80:83], v194 offset:32768\n"
"v_mfma_f32_16x16x32_bf16 v[32:35], a[112:115], a[180:183], v[32:35]\n"
"buffer_load_dwordx4 v232, s[56:59], s92 offen sc1 lds\n"
"v_mfma_f32_16x16x32_bf16 v[32:35], a[144:147], a[196:199], v[32:35]\n"
"ds_read_b128 a[68:71], v194 offset:17408\n"
"v_mfma_f32_16x16x32_bf16 v[64:67], a[112:115], a[184:187], v[64:67]\n"
"ds_read_b128 a[84:87], v194 offset:33792\n"
"v_mfma_f32_16x16x32_bf16 v[64:67], a[144:147], a[200:203], v[64:67]\n"
"ds_read_b128 a[72:75], v194 offset:18432\n"
"v_mfma_f32_16x16x32_bf16 v[96:99], a[112:115], a[188:191], v[96:99]\n"
"ds_read_b128 a[88:91], v194 offset:34816\n"
"v_mfma_f32_16x16x32_bf16 v[96:99], a[144:147], a[204:207], v[96:99]\n"
"buffer_load_dwordx4 v232, s[56:59], s92 offen offset:1024 sc1 lds\n"
"v_mfma_f32_16x16x32_bf16 v[100:103], a[116:119], a[188:191], v[100:103]\n"
"ds_read_b128 a[76:79], v194 offset:19456\n"
"v_mfma_f32_16x16x32_bf16 v[100:103], a[148:151], a[204:207], v[100:103]\n"
"ds_read_b128 a[92:95], v194 offset:35840\n"
"v_mfma_f32_16x16x32_bf16 v[68:71], a[116:119], a[184:187], v[68:71]\n"
"ds_read_b64_tr_b16 a[0:1], v196\n"
"v_mfma_f32_16x16x32_bf16 v[68:71], a[148:151], a[200:203], v[68:71]\n"
"buffer_load_dwordx4 v232, s[56:59], s92 offen offset:2048 sc1 lds\n"
"v_mfma_f32_16x16x32_bf16 v[36:39], a[116:119], a[180:183], v[36:39]\n"
"ds_read_b64_tr_b16 a[2:3], v196 offset:256\n"
"v_mfma_f32_16x16x32_bf16 v[36:39], a[148:151], a[196:199], v[36:39]\n"
"ds_read_b64_tr_b16 a[32:33], v196 offset:8192\n"
"v_mfma_f32_16x16x32_bf16 v[4:7], a[116:119], a[176:179], v[4:7]\n"
"ds_read_b64_tr_b16 a[34:35], v196 offset:8448\n"
"v_mfma_f32_16x16x32_bf16 v[4:7], a[148:151], a[192:195], v[4:7]\n"
"ds_read_b64_tr_b16 a[4:5], v196 offset:8\n"
"v_mfma_f32_16x16x32_bf16 v[8:11], a[120:123], a[176:179], v[8:11]\n"
"buffer_load_dwordx4 v232, s[56:59], s92 offen offset:3072 sc1 lds\n"
"v_mfma_f32_16x16x32_bf16 v[8:11], a[152:155], a[192:195], v[8:11]\n"
"s_add_u32 s94, s92, 0x1000\n"
"ds_read_b64_tr_b16 a[6:7], v196 offset:264\n"
"v_mfma_f32_16x16x32_bf16 v[40:43], a[120:123], a[180:183], v[40:43]\n"
"s_addk_i32 m0, 4096\n"
"ds_read_b64_tr_b16 a[36:37], v196 offset:8200\n"
"v_mfma_f32_16x16x32_bf16 v[40:43], a[152:155], a[196:199], v[40:43]\n"
"ds_read_b64_tr_b16 a[38:39], v196 offset:8456\n"
"v_mfma_f32_16x16x32_bf16 v[72:75], a[120:123], a[184:187], v[72:75]\n"
"ds_read_b64_tr_b16 a[8:9], v196 offset:2048\n"
"v_mfma_f32_16x16x32_bf16 v[72:75], a[152:155], a[200:203], v[72:75]\n"
"buffer_load_dwordx4 v232, s[56:59], s94 offen sc1 lds\n"
"v_mfma_f32_16x16x32_bf16 v[104:107], a[120:123], a[188:191], v[104:107]\n"
"ds_read_b64_tr_b16 a[10:11], v196 offset:2304\n"
"v_mfma_f32_16x16x32_bf16 v[104:107], a[152:155], a[204:207], v[104:107]\n"
"ds_read_b64_tr_b16 a[40:41], v196 offset:10240\n"
"v_mfma_f32_16x16x32_bf16 v[108:111], a[124:127], a[188:191], v[108:111]\n"
"ds_read_b64_tr_b16 a[42:43], v196 offset:10496\n"
"v_mfma_f32_16x16x32_bf16 v[108:111], a[156:159], a[204:207], v[108:111]\n"
"ds_read_b64_tr_b16 a[12:13], v196 offset:2056\n"
"v_mfma_f32_16x16x32_bf16 v[76:79], a[124:127], a[184:187], v[76:79]\n"
"buffer_load_dwordx4 v232, s[56:59], s94 offen offset:1024 sc1 lds\n"
"v_mfma_f32_16x16x32_bf16 v[76:79], a[156:159], a[200:203], v[76:79]\n"
"ds_read_b64_tr_b16 a[14:15], v196 offset:2312\n"
"v_and_b32 v254, 14, v224\n"
"v_mfma_f32_16x16x32_bf16 v[44:47], a[124:127], a[180:183], v[44:47]\n"
"ds_read_b64_tr_b16 a[44:45], v196 offset:10248\n"
"v_lshlrev_b32 v254, 3, v254\n"
"v_mfma_f32_16x16x32_bf16 v[44:47], a[156:159], a[196:199], v[44:47]\n"
"ds_read_b64_tr_b16 a[46:47], v196 offset:10504\n"
"v_and_or_b32 v254, v224, 1, v254\n"
"v_mfma_f32_16x16x32_bf16 v[12:15], a[124:127], a[176:179], v[12:15]\n"
"v_lshlrev_b32 v254, 3, v254\n"
"s_lshl_b32 s84, s10, 6\n"
"s_add_u32 s84, s84, 0x38000\n"
"v_mfma_f32_16x16x32_bf16 v[12:15], a[156:159], a[192:195], v[12:15]\n"
"buffer_load_dwordx2 v[176:177], v254, s[56:59], s84 offen sc1\n"
"v_mfma_f32_16x16x32_bf16 v[16:19], a[128:131], a[176:179], v[16:19]\n"
"ds_read_b64_tr_b16 a[16:17], v196 offset:4096\n"
"v_mfma_f32_16x16x32_bf16 v[16:19], a[160:163], a[192:195], v[16:19]\n"
"ds_read_b64_tr_b16 a[18:19], v196 offset:4352\n"
"v_mfma_f32_16x16x32_bf16 v[48:51], a[128:131], a[180:183], v[48:51]\n"
"ds_read_b64_tr_b16 a[48:49], v196 offset:12288\n"
"v_mfma_f32_16x16x32_bf16 v[48:51], a[160:163], a[196:199], v[48:51]\n"
"buffer_load_dwordx2 v[178:179], v254, s[56:59], s84 offen offset:1024 sc1\n"
"v_mfma_f32_16x16x32_bf16 v[80:83], a[128:131], a[184:187], v[80:83]\n"
"ds_read_b64_tr_b16 a[50:51], v196 offset:12544\n"
"v_mfma_f32_16x16x32_bf16 v[80:83], a[160:163], a[200:203], v[80:83]\n"
"ds_read_b64_tr_b16 a[20:21], v196 offset:4104\n"
"v_mfma_f32_16x16x32_bf16 v[112:115], a[128:131], a[188:191], v[112:115]\n"
"ds_read_b64_tr_b16 a[22:23], v196 offset:4360\n"
"v_mfma_f32_16x16x32_bf16 v[112:115], a[160:163], a[204:207], v[112:115]\n"
"ds_read_b64_tr_b16 a[52:53], v196 offset:12296\n"
"v_mfma_f32_16x16x32_bf16 v[116:119], a[132:135], a[188:191], v[116:119]\n"
"buffer_load_dwordx2 v[180:181], v254, s[56:59], s84 offen offset:2048 sc1\n"
"v_mfma_f32_16x16x32_bf16 v[116:119], a[164:167], a[204:207], v[116:119]\n"
"ds_read_b64_tr_b16 a[54:55], v196 offset:12552\n"
"v_mfma_f32_16x16x32_bf16 v[84:87], a[132:135], a[184:187], v[84:87]\n"
"ds_read_b64_tr_b16 a[24:25], v196 offset:6144\n"
"v_mfma_f32_16x16x32_bf16 v[84:87], a[164:167], a[200:203], v[84:87]\n"
"ds_read_b64_tr_b16 a[26:27], v196 offset:6400\n"
"v_mfma_f32_16x16x32_bf16 v[52:55], a[132:135], a[180:183], v[52:55]\n"
"ds_read_b64_tr_b16 a[56:57], v196 offset:14336\n"
"v_mfma_f32_16x16x32_bf16 v[52:55], a[164:167], a[196:199], v[52:55]\n"
"buffer_load_dwordx2 v[182:183], v254, s[56:59], s84 offen offset:3072 sc1\n"
"v_mfma_f32_16x16x32_bf16 v[20:23], a[132:135], a[176:179], v[20:23]\n"
"s_add_u32 s91, s91, 0x48000\n"
"ds_read_b64_tr_b16 a[58:59], v196 offset:14592\n"
"v_mfma_f32_16x16x32_bf16 v[20:23], a[164:167], a[192:195], v[20:23]\n"
"s_add_u32 m0, s85, 0x10000\n"
"ds_read_b64_tr_b16 a[28:29], v196 offset:6152\n"
"v_mfma_f32_16x16x32_bf16 v[24:27], a[136:139], a[176:179], v[24:27]\n"
"ds_read_b64_tr_b16 a[30:31], v196 offset:6408\n"
"v_mfma_f32_16x16x32_bf16 v[24:27], a[168:171], a[192:195], v[24:27]\n"
"ds_read_b64_tr_b16 a[60:61], v196 offset:14344\n"
"v_mfma_f32_16x16x32_bf16 v[56:59], a[136:139], a[180:183], v[56:59]\n"
"buffer_load_dwordx4 v231, s[48:51], s91 offen lds\n"
"v_mfma_f32_16x16x32_bf16 v[56:59], a[168:171], a[196:199], v[56:59]\n"
"s_addk_i32 m0, 1984\n"
"ds_read_b64_tr_b16 a[62:63], v196 offset:14600\n"
"v_mfma_f32_16x16x32_bf16 v[88:91], a[136:139], a[184:187], v[88:91]\n"
"ds_read_b64_tr_b16 a[96:97], v199\n"
"v_mfma_f32_16x16x32_bf16 v[88:91], a[168:171], a[200:203], v[88:91]\n"
"ds_read_b64_tr_b16 a[98:99], v199 offset:256\n"
"v_mfma_f32_16x16x32_bf16 v[120:123], a[136:139], a[188:191], v[120:123]\n"
"ds_read_b64_tr_b16 a[104:105], v199 offset:8192\n"
"v_mfma_f32_16x16x32_bf16 v[120:123], a[168:171], a[204:207], v[120:123]\n"
"buffer_load_dwordx4 v231, s[48:51], s91 offen offset:64 lds\n"
"v_mfma_f32_16x16x32_bf16 v[124:127], a[140:143], a[188:191], v[124:127]\n"
"s_add_u32 s91, s91, 0x48000\n"
"ds_read_b64_tr_b16 a[106:107], v199 offset:8448\n"
"v_mfma_f32_16x16x32_bf16 v[124:127], a[172:175], a[204:207], v[124:127]\n"
"s_addk_i32 m0, 6208\n"
"ds_read_b64_tr_b16 a[100:101], v199 offset:8\n"
"v_mfma_f32_16x16x32_bf16 v[92:95], a[140:143], a[184:187], v[92:95]\n"
"ds_read_b64_tr_b16 a[102:103], v199 offset:264\n"
"v_mfma_f32_16x16x32_bf16 v[92:95], a[172:175], a[200:203], v[92:95]\n"
"buffer_load_dwordx4 v231, s[48:51], s91 offen lds\n"
"v_mfma_f32_16x16x32_bf16 v[60:63], a[140:143], a[180:183], v[60:63]\n"
"s_addk_i32 m0, 1984\n"
"ds_read_b64_tr_b16 a[108:109], v199 offset:8200\n"
"v_mfma_f32_16x16x32_bf16 v[60:63], a[172:175], a[196:199], v[60:63]\n"
"ds_read_b64_tr_b16 a[110:111], v199 offset:8456\n"
"v_mfma_f32_16x16x32_bf16 v[28:31], a[140:143], a[176:179], v[28:31]\n"
"v_mfma_f32_16x16x32_bf16 v[28:31], a[172:175], a[192:195], v[28:31]\n"
"v_mfma_f32_16x16x32_bf16 v[208:211], a[208:211], a[224:227], v[208:211]\n"
"buffer_load_dwordx4 v231, s[48:51], s91 offen offset:64 lds\n"
"v_mfma_f32_16x16x32_bf16 v[208:211], a[216:219], a[224:227], v[208:211]\n"
"v_mfma_f32_16x16x32_bf16 v[212:215], a[212:215], a[224:227], v[212:215]\n"
"v_mfma_f32_16x16x32_bf16 v[212:215], a[220:223], a[224:227], v[212:215]\n"
"s_waitcnt lgkmcnt(0)\n"
"v_mfma_f32_16x16x32_bf16 v[208:211], a[96:99], a[224:227], v[208:211]\n"
"s_add_u32 s92, s92, 0x12000\n"
"v_and_b32 v185, 48, v224\n"
"v_mfma_f32_16x16x32_bf16 v[208:211], a[104:107], a[224:227], v[208:211]\n"
"s_add_u32 m0, s87, 0xa000\n"
"v_add_u32 v185, 0x24000, v185\n"
"v_mfma_f32_16x16x32_bf16 v[212:215], a[100:103], a[224:227], v[212:215]\n"
"s_lshl_b32 s81, s76, 7\n"
"v_mfma_f32_16x16x32_bf16 v[212:215], a[108:111], a[224:227], v[212:215]\n"
"v_add_u32 v184, s81, v185\n"
"v_mfma_f32_16x16x32_bf16 v[0:3], a[0:3], a[64:67], v[0:3]\n"
"buffer_load_dwordx4 v232, s[56:59], s92 offen sc1 lds\n"
"v_mfma_f32_16x16x32_bf16 v[0:3], a[32:35], a[80:83], v[0:3]\n"
"v_mfma_f32_16x16x32_bf16 v[4:7], a[4:7], a[64:67], v[4:7]\n"
"v_mfma_f32_16x16x32_bf16 v[4:7], a[36:39], a[80:83], v[4:7]\n"
"v_mfma_f32_16x16x32_bf16 v[8:11], a[8:11], a[64:67], v[8:11]\n"
"v_mfma_f32_16x16x32_bf16 v[8:11], a[40:43], a[80:83], v[8:11]\n"
"ds_write_b128 v184, v[208:211]\n"
"v_mfma_f32_16x16x32_bf16 v[12:15], a[12:15], a[64:67], v[12:15]\n"
"ds_write_b128 v184, v[212:215] offset:64\n"
"v_mfma_f32_16x16x32_bf16 v[12:15], a[44:47], a[80:83], v[12:15]\n"
"v_mfma_f32_16x16x32_bf16 v[16:19], a[16:19], a[64:67], v[16:19]\n"
"buffer_load_dwordx4 v232, s[56:59], s92 offen offset:1024 sc1 lds\n"
"v_mfma_f32_16x16x32_bf16 v[16:19], a[48:51], a[80:83], v[16:19]\n"
"v_mfma_f32_16x16x32_bf16 v[20:23], a[20:23], a[64:67], v[20:23]\n"
"s_waitcnt vmcnt(6) lgkmcnt(0)\n"
"s_barrier\n"
"v_mfma_f32_16x16x32_bf16 v[20:23], a[52:55], a[80:83], v[20:23]\n"
"ds_read_b128 v[192:195], v185\n"
"v_mfma_f32_16x16x32_bf16 v[24:27], a[24:27], a[64:67], v[24:27]\n"
"ds_read_b128 v[196:199], v185 offset:64\n"
"v_mfma_f32_16x16x32_bf16 v[24:27], a[56:59], a[80:83], v[24:27]\n"
"ds_read_b128 v[200:203], v185 offset:128\n"
"v_mfma_f32_16x16x32_bf16 v[28:31], a[28:31], a[64:67], v[28:31]\n"
"ds_read_b128 v[204:207], v185 offset:192\n"
"v_mfma_f32_16x16x32_bf16 v[28:31], a[60:63], a[80:83], v[28:31]\n"
"ds_read_b128 v[208:211], v185 offset:256\n"
"v_mfma_f32_16x16x32_bf16 v[60:63], a[28:31], a[68:71], v[60:63]\n"
"buffer_load_dwordx4 v232, s[56:59], s92 offen offset:2048 sc1 lds\n"
"v_mfma_f32_16x16x32_bf16 v[60:63], a[60:63], a[84:87], v[60:63]\n"
"ds_read_b128 v[212:215], v185 offset:320\n"
"v_mfma_f32_16x16x32_bf16 v[56:59], a[24:27], a[68:71], v[56:59]\n"
"ds_read_b128 v[216:219], v185 offset:384\n"
"v_mfma_f32_16x16x32_bf16 v[56:59], a[56:59], a[84:87], v[56:59]\n"
"ds_read_b128 v[220:223], v185 offset:448\n"
"v_mfma_f32_16x16x32_bf16 v[52:55], a[20:23], a[68:71], v[52:55]\n"
"ds_read_b64_tr_b16 a[192:193], v238 offset:24576\n"
"v_mfma_f32_16x16x32_bf16 v[52:55], a[52:55], a[84:87], v[52:55]\n"
"ds_read_b64_tr_b16 a[194:195], v238 offset:25600\n"
"v_mfma_f32_16x16x32_bf16 v[48:51], a[16:19], a[68:71], v[48:51]\n"
"s_waitcnt lgkmcnt(2)\n"
"ds_read_b64_tr_b16 a[200:201], v238 offset:32768\n"
"v_fmac_f32 v0, v192, v176\n"
"v_mfma_f32_16x16x32_bf16 v[48:51], a[48:51], a[84:87], v[48:51]\n"
"ds_read_b64_tr_b16 a[202:203], v238 offset:33792\n"
"v_fmac_f32 v1, v193, v176\n"
"v_fmac_f32 v2, v194, v176\n"
"v_mfma_f32_16x16x32_bf16 v[44:47], a[12:15], a[68:71], v[44:47]\n"
"ds_read_b64_tr_b16 a[196:197], v239 offset:24576\n"
"v_fmac_f32 v3, v195, v176\n"
"v_fmac_f32 v4, v196, v176\n"
"v_mfma_f32_16x16x32_bf16 v[44:47], a[44:47], a[84:87], v[44:47]\n"
"buffer_load_dwordx4 v232, s[56:59], s92 offen offset:3072 sc1 lds\n"
"v_fmac_f32 v5, v197, v176\n"
"v_mfma_f32_16x16x32_bf16 v[40:43], a[8:11], a[68:71], v[40:43]\n"
"s_add_u32 s94, s92, 0x1000\n"
"ds_read_b64_tr_b16 a[198:199], v239 offset:25600\n"
"v_fmac_f32 v6, v198, v176\n"
"v_fmac_f32 v7, v199, v176\n"
"v_mfma_f32_16x16x32_bf16 v[40:43], a[40:43], a[84:87], v[40:43]\n"
"s_addk_i32 m0, 4096\n"
"ds_read_b64_tr_b16 a[204:205], v239 offset:32768\n"
"v_fmac_f32 v8, v200, v176\n"
"v_fmac_f32 v9, v201, v176\n"
"v_mfma_f32_16x16x32_bf16 v[36:39], a[4:7], a[68:71], v[36:39]\n"
"ds_read_b64_tr_b16 a[206:207], v239 offset:33792\n"
"v_fmac_f32 v10, v202, v176\n"
"v_fmac_f32 v11, v203, v176\n"
"v_mfma_f32_16x16x32_bf16 v[36:39], a[36:39], a[84:87], v[36:39]\n"
"ds_read_b128 a[96:99], v235\n"
"v_fmac_f32 v12, v204, v176\n"
"v_fmac_f32 v13, v205, v176\n"
"v_mfma_f32_16x16x32_bf16 v[32:35], a[0:3], a[68:71], v[32:35]\n"
"v_fmac_f32 v14, v206, v176\n"
"v_fmac_f32 v15, v207, v176\n"
"v_fmac_f32 v16, v208, v176\n"
"v_mfma_f32_16x16x32_bf16 v[32:35], a[32:35], a[84:87], v[32:35]\n"
"ds_read_b128 a[144:147], v235 offset:1024\n"
"v_fmac_f32 v17, v209, v176\n"
"v_fmac_f32 v18, v210, v176\n"
"v_mfma_f32_16x16x32_bf16 v[64:67], a[0:3], a[72:75], v[64:67]\n"
"ds_read_b128 a[100:103], v235 offset:2048\n"
"v_fmac_f32 v19, v211, v176\n"
"v_fmac_f32 v20, v212, v176\n"
"v_mfma_f32_16x16x32_bf16 v[64:67], a[32:35], a[88:91], v[64:67]\n"
"buffer_load_dwordx4 v232, s[56:59], s94 offen sc1 lds\n"
"v_fmac_f32 v21, v213, v176\n"
"v_mfma_f32_16x16x32_bf16 v[68:71], a[4:7], a[72:75], v[68:71]\n"
"ds_read_b128 a[148:151], v235 offset:3072\n"
"v_fmac_f32 v22, v214, v176\n"
"v_fmac_f32 v23, v215, v176\n"
"v_mfma_f32_16x16x32_bf16 v[68:71], a[36:39], a[88:91], v[68:71]\n"
"ds_read_b128 a[104:107], v235 offset:4096\n"
"v_fmac_f32 v24, v216, v176\n"
"v_fmac_f32 v25, v217, v176\n"
"v_mfma_f32_16x16x32_bf16 v[72:75], a[8:11], a[72:75], v[72:75]\n"
"ds_read_b128 a[152:155], v235 offset:5120\n"
"v_fmac_f32 v26, v218, v176\n"
"v_fmac_f32 v27, v219, v176\n"
"v_mfma_f32_16x16x32_bf16 v[72:75], a[40:43], a[88:91], v[72:75]\n"
"ds_read_b128 a[108:111], v235 offset:6144\n"
"v_fmac_f32 v28, v220, v176\n"
"v_fmac_f32 v29, v221, v176\n"
"v_mfma_f32_16x16x32_bf16 v[76:79], a[12:15], a[72:75], v[76:79]\n"
"v_fmac_f32 v30, v222, v176\n"
"v_fmac_f32 v31, v223, v176\n"
"v_mul_f32 v0, v0, v177\n"
"v_mfma_f32_16x16x32_bf16 v[76:79], a[44:47], a[88:91], v[76:79]\n"
"ds_read_b128 a[156:159], v235 offset:7168\n"
"v_mul_f32 v1, v1, v177\n"
"v_mul_f32 v2, v2, v177\n"
"v_mfma_f32_16x16x32_bf16 v[80:83], a[16:19], a[72:75], v[80:83]\n"
"ds_read_b128 a[112:115], v235 offset:8192\n"
"v_mul_f32 v3, v3, v177\n"
"v_mul_f32 v4, v4, v177\n"
"v_mfma_f32_16x16x32_bf16 v[80:83], a[48:51], a[88:91], v[80:83]\n"
"ds_read_b128 a[160:163], v235 offset:9216\n"
"v_mul_f32 v5, v5, v177\n"
"v_mul_f32 v6, v6, v177\n"
"v_mfma_f32_16x16x32_bf16 v[84:87], a[20:23], a[72:75], v[84:87]\n"
"buffer_load_dwordx4 v232, s[56:59], s94 offen offset:1024 sc1 lds\n"
"v_mul_f32 v7, v7, v177\n"
"v_mfma_f32_16x16x32_bf16 v[84:87], a[52:55], a[88:91], v[84:87]\n"
"s_cmp_eq_u32 s77, 0\n"
"s_cselect_b32 s84, 0, 0x7ffffff0\n"
"v_or_b32 v249, s84, v234\n"
"v_mul_f32 v8, v8, v177\n"
"v_mfma_f32_16x16x32_bf16 v[88:91], a[24:27], a[72:75], v[88:91]\n"
"ds_read_b128 a[116:119], v235 offset:10240\n"
"v_mov_b32 v254, s95\n"
"v_mul_f32 v9, v9, v177\n"
"v_mfma_f32_16x16x32_bf16 v[88:91], a[56:59], a[88:91], v[88:91]\n"
"ds_read_b128 a[164:167], v235 offset:11264\n"
"v_mul_f32 v10, v10, v177\n"
"v_mul_f32 v11, v11, v177\n"
"v_mfma_f32_16x16x32_bf16 v[92:95], a[28:31], a[72:75], v[92:95]\n"
"ds_read_b128 a[120:123], v235 offset:12288\n"
"v_mul_f32 v12, v12, v177\n"
"v_mul_f32 v13, v13, v177\n"
"v_mfma_f32_16x16x32_bf16 v[92:95], a[60:63], a[88:91], v[92:95]\n"
"v_mul_f32 v14, v14, v177\n"
"v_mul_f32 v15, v15, v177\n"
"v_mul_f32 v16, v16, v177\n"
"v_mfma_f32_16x16x32_bf16 v[124:127], a[28:31], a[76:79], v[124:127]\n"
"ds_read_b128 a[168:171], v235 offset:13312\n"
"v_mul_f32 v17, v17, v177\n"
"v_mul_f32 v18, v18, v177\n"
"v_mfma_f32_16x16x32_bf16 v[124:127], a[60:63], a[92:95], v[124:127]\n"
"ds_read_b128 a[124:127], v235 offset:14336\n"
"v_mul_f32 v19, v19, v177\n"
"v_mul_f32 v20, v20, v177\n"
"v_mfma_f32_16x16x32_bf16 v[120:123], a[24:27], a[76:79], v[120:123]\n"
"buffer_store_dword v254, v249, s[56:59], 0 offen sc1\n"
"ds_read_b128 a[172:175], v235 offset:15360\n"
"v_mfma_f32_16x16x32_bf16 v[120:123], a[56:59], a[92:95], v[120:123]\n"
"s_mov_b32 s84, 0x783c00\n"
"v_mul_f32 v21, v21, v177\n"
"v_mul_f32 v22, v22, v177\n"
"v_mul_f32 v23, v23, v177\n"
"v_mfma_f32_16x16x32_bf16 v[116:119], a[20:23], a[76:79], v[116:119]\n"
"ds_read_b128 a[128:131], v235 offset:16384\n"
"v_mul_f32 v24, v24, v177\n"
"v_mul_f32 v25, v25, v177\n"
"v_mfma_f32_16x16x32_bf16 v[116:119], a[52:55], a[92:95], v[116:119]\n"
"ds_read_b128 a[176:179], v235 offset:17408\n"
"v_mul_f32 v26, v26, v177\n"
"v_mul_f32 v27, v27, v177\n"
"v_mfma_f32_16x16x32_bf16 v[112:115], a[16:19], a[76:79], v[112:115]\n"
"ds_read_b128 a[132:135], v235 offset:18432\n"
"v_mul_f32 v28, v28, v177\n"
"v_mul_f32 v29, v29, v177\n"
"v_mfma_f32_16x16x32_bf16 v[112:115], a[48:51], a[92:95], v[112:115]\n"
"v_mul_f32 v30, v30, v177\n"
"v_mul_f32 v31, v31, v177\n"
"v_cvt_pk_bf16_f32 v128, v0, v1\n"
"v_mfma_f32_16x16x32_bf16 v[108:111], a[12:15], a[76:79], v[108:111]\n"
"ds_read_b128 a[180:183], v235 offset:19456\n"
"v_cvt_pk_bf16_f32 v129, v2, v3\n"
"v_cvt_pk_bf16_f32 v130, v4, v5\n"
"v_mfma_f32_16x16x32_bf16 v[108:111], a[44:47], a[92:95], v[108:111]\n"
"ds_read_b128 a[136:139], v235 offset:20480\n"
"v_cvt_pk_bf16_f32 v131, v6, v7\n"
"v_cvt_pk_bf16_f32 v132, v8, v9\n"
"v_mfma_f32_16x16x32_bf16 v[104:107], a[8:11], a[76:79], v[104:107]\n"
"buffer_load_dword v250, v186, s[24:27], s84 offen\n"
"ds_read_b128 a[184:187], v235 offset:21504\n"
"v_mfma_f32_16x16x32_bf16 v[104:107], a[40:43], a[92:95], v[104:107]\n"
"v_cvt_pk_bf16_f32 v133, v10, v11\n"
"v_cvt_pk_bf16_f32 v134, v12, v13\n"
"v_cvt_pk_bf16_f32 v135, v14, v15\n"
"v_mfma_f32_16x16x32_bf16 v[100:103], a[4:7], a[76:79], v[100:103]\n"
"ds_read_b128 a[140:143], v235 offset:22528\n"
"v_cvt_pk_bf16_f32 v136, v16, v17\n"
"v_cvt_pk_bf16_f32 v137, v18, v19\n"
"v_mfma_f32_16x16x32_bf16 v[100:103], a[36:39], a[92:95], v[100:103]\n"
"ds_read_b128 a[188:191], v235 offset:23552\n"
"v_cvt_pk_bf16_f32 v138, v20, v21\n"
"v_cvt_pk_bf16_f32 v139, v22, v23\n"
"v_mfma_f32_16x16x32_bf16 v[96:99], a[0:3], a[76:79], v[96:99]\n"
"v_cvt_pk_bf16_f32 v140, v24, v25\n"
"v_cvt_pk_bf16_f32 v141, v26, v27\n"
"v_cvt_pk_bf16_f32 v142, v28, v29\n"
"v_mfma_f32_16x16x32_bf16 v[96:99], a[32:35], a[92:95], v[96:99]\n"
"v_cvt_pk_bf16_f32 v143, v30, v31\n"
"s_add_u32 s92, s92, 0x12000\n"
"s_add_u32 m0, s87, 0x14000\n"
"v_pk_fma_f32 v[32:33], v[192:193], v[178:179], v[32:33] op_sel_hi:[1,0,1]\n"
"buffer_load_dwordx4 v232, s[56:59], s92 offen sc1 lds\n"
"v_pk_fma_f32 v[34:35], v[194:195], v[178:179], v[34:35] op_sel_hi:[1,0,1]\n"
"v_pk_fma_f32 v[36:37], v[196:197], v[178:179], v[36:37] op_sel_hi:[1,0,1]\n"
"v_pk_fma_f32 v[38:39], v[198:199], v[178:179], v[38:39] op_sel_hi:[1,0,1]\n"
"v_pk_fma_f32 v[40:41], v[200:201], v[178:179], v[40:41] op_sel_hi:[1,0,1]\n"
"v_pk_fma_f32 v[42:43], v[202:203], v[178:179], v[42:43] op_sel_hi:[1,0,1]\n"
"v_pk_fma_f32 v[44:45], v[204:205], v[178:179], v[44:45] op_sel_hi:[1,0,1]\n"
"v_pk_fma_f32 v[46:47], v[206:207], v[178:179], v[46:47] op_sel_hi:[1,0,1]\n"
"v_pk_fma_f32 v[48:49], v[208:209], v[178:179], v[48:49] op_sel_hi:[1,0,1]\n"
"buffer_load_dwordx4 v232, s[56:59], s92 offen offset:1024 sc1 lds\n"
"v_pk_fma_f32 v[50:51], v[210:211], v[178:179], v[50:51] op_sel_hi:[1,0,1]\n"
"v_pk_fma_f32 v[52:53], v[212:213], v[178:179], v[52:53] op_sel_hi:[1,0,1]\n"
"v_pk_fma_f32 v[54:55], v[214:215], v[178:179], v[54:55] op_sel_hi:[1,0,1]\n"
"v_pk_fma_f32 v[56:57], v[216:217], v[178:179], v[56:57] op_sel_hi:[1,0,1]\n"
"v_pk_fma_f32 v[58:59], v[218:219], v[178:179], v[58:59] op_sel_hi:[1,0,1]\n"
"v_pk_fma_f32 v[60:61], v[220:221], v[178:179], v[60:61] op_sel_hi:[1,0,1]\n"
"v_pk_fma_f32 v[62:63], v[222:223], v[178:179], v[62:63] op_sel_hi:[1,0,1]\n"
"v_pk_mul_f32 v[32:33], v[32:33], v[178:179] op_sel:[0,1]\n"
"buffer_load_dwordx4 v232, s[56:59], s92 offen offset:2048 sc1 lds\n"
"v_pk_mul_f32 v[34:35], v[34:35], v[178:179] op_sel:[0,1]\n"
"v_pk_mul_f32 v[36:37], v[36:37], v[178:179] op_sel:[0,1]\n"
"v_pk_mul_f32 v[38:39], v[38:39], v[178:179] op_sel:[0,1]\n"
"v_pk_mul_f32 v[40:41], v[40:41], v[178:179] op_sel:[0,1]\n"
"v_pk_mul_f32 v[42:43], v[42:43], v[178:179] op_sel:[0,1]\n"
"v_pk_mul_f32 v[44:45], v[44:45], v[178:179] op_sel:[0,1]\n"
"v_pk_mul_f32 v[46:47], v[46:47], v[178:179] op_sel:[0,1]\n"
"v_pk_mul_f32 v[48:49], v[48:49], v[178:179] op_sel:[0,1]\n"
"buffer_load_dwordx4 v232, s[56:59], s92 offen offset:3072 sc1 lds\n"
"v_pk_mul_f32 v[50:51], v[50:51], v[178:179] op_sel:[0,1]\n"
"v_pk_mul_f32 v[52:53], v[52:53], v[178:179] op_sel:[0,1]\n"
"v_pk_mul_f32 v[54:55], v[54:55], v[178:179] op_sel:[0,1]\n"
"v_pk_mul_f32 v[56:57], v[56:57], v[178:179] op_sel:[0,1]\n"
"v_pk_mul_f32 v[58:59], v[58:59], v[178:179] op_sel:[0,1]\n"
"v_pk_mul_f32 v[60:61], v[60:61], v[178:179] op_sel:[0,1]\n"
"v_pk_mul_f32 v[62:63], v[62:63], v[178:179] op_sel:[0,1]\n"
"v_cvt_pk_bf16_f32 v144, v32, v33\n"
"s_add_u32 s94, s92, 0x1000\n"
"s_addk_i32 m0, 4096\n"
"v_cvt_pk_bf16_f32 v145, v34, v35\n"
"buffer_load_dwordx4 v232, s[56:59], s94 offen sc1 lds\n"
"v_cvt_pk_bf16_f32 v146, v36, v37\n"
"v_cvt_pk_bf16_f32 v147, v38, v39\n"
"v_cvt_pk_bf16_f32 v148, v40, v41\n"
"v_cvt_pk_bf16_f32 v149, v42, v43\n"
"v_cvt_pk_bf16_f32 v150, v44, v45\n"
"v_cvt_pk_bf16_f32 v151, v46, v47\n"
"v_cvt_pk_bf16_f32 v152, v48, v49\n"
"v_cvt_pk_bf16_f32 v153, v50, v51\n"
"buffer_load_dwordx4 v232, s[56:59], s94 offen offset:1024 sc1 lds\n"
"v_cvt_pk_bf16_f32 v154, v52, v53\n"
"v_cvt_pk_bf16_f32 v155, v54, v55\n"
"v_cvt_pk_bf16_f32 v156, v56, v57\n"
"v_cvt_pk_bf16_f32 v157, v58, v59\n"
"v_cvt_pk_bf16_f32 v158, v60, v61\n"
"v_cvt_pk_bf16_f32 v159, v62, v63\n"
"v_pk_fma_f32 v[64:65], v[192:193], v[180:181], v[64:65] op_sel_hi:[1,0,1]\n"
"v_pk_fma_f32 v[66:67], v[194:195], v[180:181], v[66:67] op_sel_hi:[1,0,1]\n"
"s_add_u32 s91, s91, 0x48000\n"
"s_add_u32 m0, s85, 0x1a000\n"
"v_pk_fma_f32 v[68:69], v[196:197], v[180:181], v[68:69] op_sel_hi:[1,0,1]\n"
"buffer_load_dwordx4 v231, s[48:51], s91 offen lds\n"
"v_pk_fma_f32 v[70:71], v[198:199], v[180:181], v[70:71] op_sel_hi:[1,0,1]\n"
"v_pk_fma_f32 v[72:73], v[200:201], v[180:181], v[72:73] op_sel_hi:[1,0,1]\n"
"v_pk_fma_f32 v[74:75], v[202:203], v[180:181], v[74:75] op_sel_hi:[1,0,1]\n"
"v_pk_fma_f32 v[76:77], v[204:205], v[180:181], v[76:77] op_sel_hi:[1,0,1]\n"
"v_pk_fma_f32 v[78:79], v[206:207], v[180:181], v[78:79] op_sel_hi:[1,0,1]\n"
"v_pk_fma_f32 v[80:81], v[208:209], v[180:181], v[80:81] op_sel_hi:[1,0,1]\n"
"v_pk_fma_f32 v[82:83], v[210:211], v[180:181], v[82:83] op_sel_hi:[1,0,1]\n"
"v_pk_fma_f32 v[84:85], v[212:213], v[180:181], v[84:85] op_sel_hi:[1,0,1]\n"
"s_addk_i32 m0, 1984\n"
"v_pk_fma_f32 v[86:87], v[214:215], v[180:181], v[86:87] op_sel_hi:[1,0,1]\n"
"buffer_load_dwordx4 v231, s[48:51], s91 offen offset:64 lds\n"
"v_pk_fma_f32 v[88:89], v[216:217], v[180:181], v[88:89] op_sel_hi:[1,0,1]\n"
"v_pk_fma_f32 v[90:91], v[218:219], v[180:181], v[90:91] op_sel_hi:[1,0,1]\n"
"v_pk_fma_f32 v[92:93], v[220:221], v[180:181], v[92:93] op_sel_hi:[1,0,1]\n"
"v_pk_fma_f32 v[94:95], v[222:223], v[180:181], v[94:95] op_sel_hi:[1,0,1]\n"
"v_pk_mul_f32 v[64:65], v[64:65], v[180:181] op_sel:[0,1]\n"
"v_pk_mul_f32 v[66:67], v[66:67], v[180:181] op_sel:[0,1]\n"
"v_pk_mul_f32 v[68:69], v[68:69], v[180:181] op_sel:[0,1]\n"
"v_pk_mul_f32 v[70:71], v[70:71], v[180:181] op_sel:[0,1]\n"
"s_add_u32 s91, s91, 0x48000\n"
"s_addk_i32 m0, 6208\n"
"v_pk_mul_f32 v[72:73], v[72:73], v[180:181] op_sel:[0,1]\n"
"buffer_load_dwordx4 v231, s[48:51], s91 offen lds\n"
"v_pk_mul_f32 v[74:75], v[74:75], v[180:181] op_sel:[0,1]\n"
"v_pk_mul_f32 v[76:77], v[76:77], v[180:181] op_sel:[0,1]\n"
"v_pk_mul_f32 v[78:79], v[78:79], v[180:181] op_sel:[0,1]\n"
"v_pk_mul_f32 v[80:81], v[80:81], v[180:181] op_sel:[0,1]\n"
"v_pk_mul_f32 v[82:83], v[82:83], v[180:181] op_sel:[0,1]\n"
"v_pk_mul_f32 v[84:85], v[84:85], v[180:181] op_sel:[0,1]\n"
"v_pk_mul_f32 v[86:87], v[86:87], v[180:181] op_sel:[0,1]\n"
"v_pk_mul_f32 v[88:89], v[88:89], v[180:181] op_sel:[0,1]\n"
"s_addk_i32 m0, 1984\n"
"v_pk_mul_f32 v[90:91], v[90:91], v[180:181] op_sel:[0,1]\n"
"buffer_load_dwordx4 v231, s[48:51], s91 offen offset:64 lds\n"
"v_pk_mul_f32 v[92:93], v[92:93], v[180:181] op_sel:[0,1]\n"
"v_pk_mul_f32 v[94:95], v[94:95], v[180:181] op_sel:[0,1]\n"
"v_cvt_pk_bf16_f32 v160, v64, v65\n"
"v_cvt_pk_bf16_f32 v161, v66, v67\n"
"v_cvt_pk_bf16_f32 v162, v68, v69\n"
"v_cvt_pk_bf16_f32 v163, v70, v71\n"
"v_cvt_pk_bf16_f32 v164, v72, v73\n"
"v_cvt_pk_bf16_f32 v165, v74, v75\n"
"v_cvt_pk_bf16_f32 v166, v76, v77\n"
"v_cvt_pk_bf16_f32 v167, v78, v79\n"
"v_cvt_pk_bf16_f32 v168, v80, v81\n"
"v_cvt_pk_bf16_f32 v169, v82, v83\n"
"v_cvt_pk_bf16_f32 v170, v84, v85\n"
"v_cvt_pk_bf16_f32 v171, v86, v87\n"
"v_cvt_pk_bf16_f32 v172, v88, v89\n"
"v_cvt_pk_bf16_f32 v173, v90, v91\n"
"v_cvt_pk_bf16_f32 v174, v92, v93\n"
"v_cvt_pk_bf16_f32 v175, v94, v95\n"
"v_accvgpr_read_b32 v8, a228\n"
"v_accvgpr_read_b32 v9, a229\n"
"v_accvgpr_read_b32 v10, a230\n"
"v_accvgpr_read_b32 v11, a231\n"
"v_pk_fma_f32 v[96:97], v[192:193], v[182:183], v[96:97] op_sel_hi:[1,0,1]\n"
"v_pk_fma_f32 v[98:99], v[194:195], v[182:183], v[98:99] op_sel_hi:[1,0,1]\n"
"v_pk_fma_f32 v[100:101], v[196:197], v[182:183], v[100:101] op_sel_hi:[1,0,1]\n"
"v_pk_fma_f32 v[102:103], v[198:199], v[182:183], v[102:103] op_sel_hi:[1,0,1]\n"
"v_pk_fma_f32 v[104:105], v[200:201], v[182:183], v[104:105] op_sel_hi:[1,0,1]\n"
"v_pk_fma_f32 v[106:107], v[202:203], v[182:183], v[106:107] op_sel_hi:[1,0,1]\n"
"v_pk_fma_f32 v[108:109], v[204:205], v[182:183], v[108:109] op_sel_hi:[1,0,1]\n"
"v_pk_fma_f32 v[110:111], v[206:207], v[182:183], v[110:111] op_sel_hi:[1,0,1]\n"
"v_pk_fma_f32 v[112:113], v[208:209], v[182:183], v[112:113] op_sel_hi:[1,0,1]\n"
"v_pk_fma_f32 v[114:115], v[210:211], v[182:183], v[114:115] op_sel_hi:[1,0,1]\n"
"v_pk_fma_f32 v[116:117], v[212:213], v[182:183], v[116:117] op_sel_hi:[1,0,1]\n"
"v_pk_fma_f32 v[118:119], v[214:215], v[182:183], v[118:119] op_sel_hi:[1,0,1]\n"
"v_pk_fma_f32 v[120:121], v[216:217], v[182:183], v[120:121] op_sel_hi:[1,0,1]\n"
"v_pk_fma_f32 v[122:123], v[218:219], v[182:183], v[122:123] op_sel_hi:[1,0,1]\n"
"v_pk_fma_f32 v[124:125], v[220:221], v[182:183], v[124:125] op_sel_hi:[1,0,1]\n"
"v_pk_fma_f32 v[126:127], v[222:223], v[182:183], v[126:127] op_sel_hi:[1,0,1]\n"
"v_pk_mul_f32 v[96:97], v[96:97], v[182:183] op_sel:[0,1]\n"
"v_pk_mul_f32 v[98:99], v[98:99], v[182:183] op_sel:[0,1]\n"
"v_pk_mul_f32 v[100:101], v[100:101], v[182:183] op_sel:[0,1]\n"
"v_pk_mul_f32 v[102:103], v[102:103], v[182:183] op_sel:[0,1]\n"
"v_pk_mul_f32 v[104:105], v[104:105], v[182:183] op_sel:[0,1]\n"
"v_pk_mul_f32 v[106:107], v[106:107], v[182:183] op_sel:[0,1]\n"
"v_pk_mul_f32 v[108:109], v[108:109], v[182:183] op_sel:[0,1]\n"
"v_pk_mul_f32 v[110:111], v[110:111], v[182:183] op_sel:[0,1]\n"
"v_pk_mul_f32 v[112:113], v[112:113], v[182:183] op_sel:[0,1]\n"
"v_pk_mul_f32 v[114:115], v[114:115], v[182:183] op_sel:[0,1]\n"
"v_pk_mul_f32 v[116:117], v[116:117], v[182:183] op_sel:[0,1]\n"
"v_pk_mul_f32 v[118:119], v[118:119], v[182:183] op_sel:[0,1]\n"
"v_pk_mul_f32 v[120:121], v[120:121], v[182:183] op_sel:[0,1]\n"
"v_pk_mul_f32 v[122:123], v[122:123], v[182:183] op_sel:[0,1]\n"
"v_pk_mul_f32 v[124:125], v[124:125], v[182:183] op_sel:[0,1]\n"
"v_pk_mul_f32 v[126:127], v[126:127], v[182:183] op_sel:[0,1]\n"
"v_cvt_pk_bf16_f32 v176, v96, v97\n"
"v_cvt_pk_bf16_f32 v177, v98, v99\n"
"v_cvt_pk_bf16_f32 v178, v100, v101\n"
"v_cvt_pk_bf16_f32 v179, v102, v103\n"
"v_cvt_pk_bf16_f32 v180, v104, v105\n"
"v_cvt_pk_bf16_f32 v181, v106, v107\n"
"v_cvt_pk_bf16_f32 v182, v108, v109\n"
"v_cvt_pk_bf16_f32 v183, v110, v111\n"
"v_cvt_pk_bf16_f32 v184, v112, v113\n"
"v_cvt_pk_bf16_f32 v185, v114, v115\n"
"v_cvt_pk_bf16_f32 v186, v116, v117\n"
"v_cvt_pk_bf16_f32 v187, v118, v119\n"
"v_cvt_pk_bf16_f32 v188, v120, v121\n"
"v_cvt_pk_bf16_f32 v189, v122, v123\n"
"v_cvt_pk_bf16_f32 v190, v124, v125\n"
"v_cvt_pk_bf16_f32 v191, v126, v127\n"
"s_cmp_eq_u32 s7, 0\n"
"s_cbranch_scc1 L_nob1\n"
"v_lshlrev_b32 v0, 16, v8\n"
"v_and_b32 v1, s14, v8\n"
"v_lshlrev_b32 v2, 16, v9\n"
"v_and_b32 v3, s14, v9\n"
"v_lshlrev_b32 v4, 16, v10\n"
"v_and_b32 v5, s14, v10\n"
"v_lshlrev_b32 v6, 16, v11\n"
"v_and_b32 v7, s14, v11\n"
"v_accvgpr_write_b32 a248, v0\n"
"v_accvgpr_write_b32 a249, v1\n"
"v_accvgpr_write_b32 a250, v2\n"
"v_accvgpr_write_b32 a251, v3\n"
"v_accvgpr_write_b32 a252, v4\n"
"v_accvgpr_write_b32 a253, v5\n"
"v_accvgpr_write_b32 a254, v6\n"
"v_accvgpr_write_b32 a255, v7\n"
"s_mov_b32 s13, 2\n"
"s_waitcnt vmcnt(12) lgkmcnt(0)\n"
"s_barrier\n"
"v_mfma_f32_16x16x32_bf16 a[208:211], a[192:195], a[96:99], a[248:251]\n"
"s_add_u32 s92, s92, 0x12000\n"
"s_add_u32 m0, s87, 0\n"
"ds_read_b64_tr_b16 v[32:33], v240\n"
"v_mfma_f32_16x16x32_bf16 a[208:211], a[200:203], a[144:147], a[208:211]\n"
"ds_read_b64_tr_b16 v[34:35], v240 offset:1024\n"
"v_mfma_f32_16x16x32_bf16 a[212:215], a[196:199], a[96:99], a[252:255]\n"
"buffer_load_dwordx4 v232, s[56:59], s92 offen sc1 lds\n"
"v_mfma_f32_16x16x32_bf16 a[212:215], a[204:207], a[144:147], a[212:215]\n"
"ds_read_b64_tr_b16 v[40:41], v240 offset:8192\n"
"v_mfma_f32_16x16x32_bf16 a[220:223], a[196:199], a[100:103], a[252:255]\n"
"ds_read_b64_tr_b16 v[42:43], v240 offset:9216\n"
"v_mfma_f32_16x16x32_bf16 a[220:223], a[204:207], a[148:151], a[220:223]\n"
"ds_read_b64_tr_b16 v[36:37], v241\n"
"v_mfma_f32_16x16x32_bf16 a[216:219], a[192:195], a[100:103], a[248:251]\n"
"ds_read_b64_tr_b16 v[38:39], v241 offset:1024\n"
"v_mfma_f32_16x16x32_bf16 a[216:219], a[200:203], a[148:151], a[216:219]\n"
"buffer_load_dwordx4 v232, s[56:59], s92 offen offset:1024 sc1 lds\n"
"v_mfma_f32_16x16x32_bf16 a[224:227], a[192:195], a[104:107], a[248:251]\n"
"ds_read_b64_tr_b16 v[44:45], v241 offset:8192\n"
"v_mfma_f32_16x16x32_bf16 a[224:227], a[200:203], a[152:155], a[224:227]\n"
"ds_read_b64_tr_b16 v[46:47], v241 offset:9216\n"
"v_mfma_f32_16x16x32_bf16 a[228:231], a[196:199], a[104:107], a[252:255]\n"
"ds_read_b128 a[0:3], v235 offset:40960\n"
"v_mfma_f32_16x16x32_bf16 a[228:231], a[204:207], a[152:155], a[228:231]\n"
"buffer_load_dwordx4 v232, s[56:59], s92 offen offset:2048 sc1 lds\n"
"v_mfma_f32_16x16x32_bf16 a[236:239], a[196:199], a[108:111], a[252:255]\n"
"ds_read_b128 a[48:51], v235 offset:41984\n"
"v_mfma_f32_16x16x32_bf16 a[236:239], a[204:207], a[156:159], a[236:239]\n"
"ds_read_b128 a[4:7], v235 offset:43008\n"
"v_mfma_f32_16x16x32_bf16 a[232:235], a[192:195], a[108:111], a[248:251]\n"
"ds_read_b128 a[52:55], v235 offset:44032\n"
"v_mfma_f32_16x16x32_bf16 a[232:235], a[200:203], a[156:159], a[232:235]\n"
"v_mfma_f32_16x16x32_bf16 a[240:243], a[192:195], a[112:115], a[248:251]\n"
"buffer_load_dwordx4 v232, s[56:59], s92 offen offset:3072 sc1 lds\n"
"v_mfma_f32_16x16x32_bf16 a[240:243], a[200:203], a[160:163], a[240:243]\n"
"s_add_u32 s94, s92, 0x1000\n"
"ds_read_b128 a[8:11], v235 offset:45056\n"
"v_mfma_f32_16x16x32_bf16 a[244:247], a[196:199], a[112:115], a[252:255]\n"
"s_addk_i32 m0, 4096\n"
"ds_read_b128 a[56:59], v235 offset:46080\n"
"v_mfma_f32_16x16x32_bf16 a[244:247], a[204:207], a[160:163], a[244:247]\n"
"ds_read_b128 a[12:15], v235 offset:47104\n"
"v_mfma_f32_16x16x32_bf16 a[252:255], a[196:199], a[116:119], a[252:255]\n"
"ds_read_b128 a[60:63], v235 offset:48128\n"
"v_mfma_f32_16x16x32_bf16 a[252:255], a[204:207], a[164:167], a[252:255]\n"
"buffer_load_dwordx4 v232, s[56:59], s94 offen sc1 lds\n"
"v_mfma_f32_16x16x32_bf16 a[248:251], a[192:195], a[116:119], a[248:251]\n"
"ds_read_b128 a[16:19], v235 offset:49152\n"
"v_mfma_f32_16x16x32_bf16 a[248:251], a[200:203], a[164:167], a[248:251]\n"
"ds_read_b128 a[64:67], v235 offset:50176\n"
"v_mfma_f32_16x16x32_bf16 v[64:67], a[192:195], a[120:123], 0\n"
"ds_read_b128 a[20:23], v235 offset:51200\n"
"v_mfma_f32_16x16x32_bf16 v[64:67], a[200:203], a[168:171], v[64:67]\n"
"buffer_load_dwordx4 v232, s[56:59], s94 offen offset:1024 sc1 lds\n"
"v_mfma_f32_16x16x32_bf16 v[68:71], a[196:199], a[120:123], 0\n"
"s_add_u32 s91, s91, 0x48000\n"
"ds_read_b128 a[68:71], v235 offset:52224\n"
"v_mfma_f32_16x16x32_bf16 v[68:71], a[204:207], a[168:171], v[68:71]\n"
"s_add_u32 m0, s85, 0x6000\n"
"ds_read_b128 a[24:27], v235 offset:53248\n"
"v_mfma_f32_16x16x32_bf16 v[76:79], a[196:199], a[124:127], 0\n"
"ds_read_b128 a[72:75], v235 offset:54272\n"
"v_mfma_f32_16x16x32_bf16 v[76:79], a[204:207], a[172:175], v[76:79]\n"
"ds_read_b128 a[28:31], v235 offset:55296\n"
"v_mfma_f32_16x16x32_bf16 v[72:75], a[192:195], a[124:127], 0\n"
"buffer_load_dwordx4 v231, s[48:51], s91 offen lds\n"
"v_mfma_f32_16x16x32_bf16 v[72:75], a[200:203], a[172:175], v[72:75]\n"
"s_addk_i32 m0, 1984\n"
"ds_read_b128 a[76:79], v235 offset:56320\n"
"v_mfma_f32_16x16x32_bf16 v[80:83], a[192:195], a[128:131], 0\n"
"ds_read_b128 a[32:35], v235 offset:57344\n"
"v_mfma_f32_16x16x32_bf16 v[80:83], a[200:203], a[176:179], v[80:83]\n"
"ds_read_b128 a[80:83], v235 offset:58368\n"
"v_mfma_f32_16x16x32_bf16 v[84:87], a[196:199], a[128:131], 0\n"
"v_mfma_f32_16x16x32_bf16 v[84:87], a[204:207], a[176:179], v[84:87]\n"
"buffer_load_dwordx4 v231, s[48:51], s91 offen offset:64 lds\n"
"v_mfma_f32_16x16x32_bf16 v[92:95], a[196:199], a[132:135], 0\n"
"s_add_u32 s91, s91, 0x48000\n"
"ds_read_b128 a[36:39], v235 offset:59392\n"
"v_mfma_f32_16x16x32_bf16 v[92:95], a[204:207], a[180:183], v[92:95]\n"
"s_addk_i32 m0, 6208\n"
"ds_read_b128 a[84:87], v235 offset:60416\n"
"v_mfma_f32_16x16x32_bf16 v[88:91], a[192:195], a[132:135], 0\n"
"ds_read_b128 a[40:43], v235 offset:61440\n"
"v_mfma_f32_16x16x32_bf16 v[88:91], a[200:203], a[180:183], v[88:91]\n"
"buffer_load_dwordx4 v231, s[48:51], s91 offen lds\n"
"v_mfma_f32_16x16x32_bf16 v[96:99], a[192:195], a[136:139], 0\n"
"s_addk_i32 m0, 1984\n"
"ds_read_b128 a[88:91], v235 offset:62464\n"
"v_mfma_f32_16x16x32_bf16 v[96:99], a[200:203], a[184:187], v[96:99]\n"
"ds_read_b128 a[44:47], v235 offset:63488\n"
"v_mfma_f32_16x16x32_bf16 v[100:103], a[196:199], a[136:139], 0\n"
"ds_read_b128 a[92:95], v235 offset:64512\n"
"v_mfma_f32_16x16x32_bf16 v[100:103], a[204:207], a[184:187], v[100:103]\n"
"v_mfma_f32_16x16x32_bf16 v[108:111], a[196:199], a[140:143], 0\n"
"buffer_load_dwordx4 v231, s[48:51], s91 offen offset:64 lds\n"
"v_mfma_f32_16x16x32_bf16 v[108:111], a[204:207], a[188:191], v[108:111]\n"
"v_mfma_f32_16x16x32_bf16 v[104:107], a[192:195], a[140:143], 0\n"
"v_mfma_f32_16x16x32_bf16 v[104:107], a[200:203], a[188:191], v[104:107]\n"
"s_waitcnt vmcnt(10) lgkmcnt(0)\n"
"s_barrier\n"
"L_b1:\n"
"v_mfma_f32_16x16x32_bf16 a[208:211], v[32:35], a[0:3], a[208:211]\n"
"s_add_u32 s92, s92, 0x12000\n"
"s_add_u32 m0, s87, 0xa000\n"
"ds_read_b64_tr_b16 a[192:193], v240 offset:40960\n"
"v_mfma_f32_16x16x32_bf16 a[208:211], v[40:43], a[48:51], a[208:211]\n"
"ds_read_b64_tr_b16 a[194:195], v240 offset:41984\n"
"v_mfma_f32_16x16x32_bf16 a[212:215], v[36:39], a[0:3], a[212:215]\n"
"buffer_load_dwordx4 v232, s[56:59], s92 offen sc1 lds\n"
"v_mfma_f32_16x16x32_bf16 a[212:215], v[44:47], a[48:51], a[212:215]\n"
"ds_read_b64_tr_b16 a[200:201], v240 offset:49152\n"
"v_mfma_f32_16x16x32_bf16 a[220:223], v[36:39], a[4:7], a[220:223]\n"
"ds_read_b64_tr_b16 a[202:203], v240 offset:50176\n"
"v_mfma_f32_16x16x32_bf16 a[220:223], v[44:47], a[52:55], a[220:223]\n"
"ds_read_b64_tr_b16 a[196:197], v241 offset:40960\n"
"v_mfma_f32_16x16x32_bf16 a[216:219], v[32:35], a[4:7], a[216:219]\n"
"ds_read_b64_tr_b16 a[198:199], v241 offset:41984\n"
"v_mfma_f32_16x16x32_bf16 a[216:219], v[40:43], a[52:55], a[216:219]\n"
"buffer_load_dwordx4 v232, s[56:59], s92 offen offset:1024 sc1 lds\n"
"v_mfma_f32_16x16x32_bf16 a[224:227], v[32:35], a[8:11], a[224:227]\n"
"ds_read_b64_tr_b16 a[204:205], v241 offset:49152\n"
"v_mfma_f32_16x16x32_bf16 a[224:227], v[40:43], a[56:59], a[224:227]\n"
"ds_read_b64_tr_b16 a[206:207], v241 offset:50176\n"
"v_mfma_f32_16x16x32_bf16 a[228:231], v[36:39], a[8:11], a[228:231]\n"
"ds_read_b128 a[96:99], v236 offset:16384\n"
"v_mfma_f32_16x16x32_bf16 a[228:231], v[44:47], a[56:59], a[228:231]\n"
"buffer_load_dwordx4 v232, s[56:59], s92 offen offset:2048 sc1 lds\n"
"v_mfma_f32_16x16x32_bf16 a[236:239], v[36:39], a[12:15], a[236:239]\n"
"ds_read_b128 a[144:147], v236 offset:17408\n"
"v_mfma_f32_16x16x32_bf16 a[236:239], v[44:47], a[60:63], a[236:239]\n"
"ds_read_b128 a[100:103], v236 offset:18432\n"
"v_mfma_f32_16x16x32_bf16 a[232:235], v[32:35], a[12:15], a[232:235]\n"
"ds_read_b128 a[148:151], v236 offset:19456\n"
"v_mfma_f32_16x16x32_bf16 a[232:235], v[40:43], a[60:63], a[232:235]\n"
"v_mfma_f32_16x16x32_bf16 a[240:243], v[32:35], a[16:19], a[240:243]\n"
"buffer_load_dwordx4 v232, s[56:59], s92 offen offset:3072 sc1 lds\n"
"v_mfma_f32_16x16x32_bf16 a[240:243], v[40:43], a[64:67], a[240:243]\n"
"s_add_u32 s94, s92, 0x1000\n"
"ds_read_b128 a[104:107], v236 offset:20480\n"
"v_mfma_f32_16x16x32_bf16 a[244:247], v[36:39], a[16:19], a[244:247]\n"
"s_addk_i32 m0, 4096\n"
"ds_read_b128 a[152:155], v236 offset:21504\n"
"v_mfma_f32_16x16x32_bf16 a[244:247], v[44:47], a[64:67], a[244:247]\n"
"ds_read_b128 a[108:111], v236 offset:22528\n"
"v_mfma_f32_16x16x32_bf16 a[252:255], v[36:39], a[20:23], a[252:255]\n"
"ds_read_b128 a[156:159], v236 offset:23552\n"
"v_mfma_f32_16x16x32_bf16 a[252:255], v[44:47], a[68:71], a[252:255]\n"
"buffer_load_dwordx4 v232, s[56:59], s94 offen sc1 lds\n"
"v_mfma_f32_16x16x32_bf16 a[248:251], v[32:35], a[20:23], a[248:251]\n"
"ds_read_b128 a[112:115], v236 offset:24576\n"
"v_mfma_f32_16x16x32_bf16 a[248:251], v[40:43], a[68:71], a[248:251]\n"
"ds_read_b128 a[160:163], v236 offset:25600\n"
"v_mfma_f32_16x16x32_bf16 v[64:67], v[32:35], a[24:27], v[64:67]\n"
"ds_read_b128 a[116:119], v236 offset:26624\n"
"v_mfma_f32_16x16x32_bf16 v[64:67], v[40:43], a[72:75], v[64:67]\n"
"buffer_load_dwordx4 v232, s[56:59], s94 offen offset:1024 sc1 lds\n"
"v_mfma_f32_16x16x32_bf16 v[68:71], v[36:39], a[24:27], v[68:71]\n"
"s_add_u32 s91, s91, 0x48000\n"
"ds_read_b128 a[164:167], v236 offset:27648\n"
"v_mfma_f32_16x16x32_bf16 v[68:71], v[44:47], a[72:75], v[68:71]\n"
"s_add_u32 m0, s85, 0x10000\n"
"ds_read_b128 a[120:123], v236 offset:28672\n"
"v_mfma_f32_16x16x32_bf16 v[76:79], v[36:39], a[28:31], v[76:79]\n"
"ds_read_b128 a[168:171], v236 offset:29696\n"
"v_mfma_f32_16x16x32_bf16 v[76:79], v[44:47], a[76:79], v[76:79]\n"
"ds_read_b128 a[124:127], v236 offset:30720\n"
"v_mfma_f32_16x16x32_bf16 v[72:75], v[32:35], a[28:31], v[72:75]\n"
"buffer_load_dwordx4 v231, s[48:51], s91 offen lds\n"
"v_mfma_f32_16x16x32_bf16 v[72:75], v[40:43], a[76:79], v[72:75]\n"
"s_addk_i32 m0, 1984\n"
"ds_read_b128 a[172:175], v236 offset:31744\n"
"v_mfma_f32_16x16x32_bf16 v[80:83], v[32:35], a[32:35], v[80:83]\n"
"ds_read_b128 a[128:131], v236 offset:32768\n"
"v_mfma_f32_16x16x32_bf16 v[80:83], v[40:43], a[80:83], v[80:83]\n"
"ds_read_b128 a[176:179], v236 offset:33792\n"
"v_mfma_f32_16x16x32_bf16 v[84:87], v[36:39], a[32:35], v[84:87]\n"
"v_mfma_f32_16x16x32_bf16 v[84:87], v[44:47], a[80:83], v[84:87]\n"
"buffer_load_dwordx4 v231, s[48:51], s91 offen offset:64 lds\n"
"v_mfma_f32_16x16x32_bf16 v[92:95], v[36:39], a[36:39], v[92:95]\n"
"s_add_u32 s91, s91, 0x48000\n"
"ds_read_b128 a[132:135], v236 offset:34816\n"
"v_mfma_f32_16x16x32_bf16 v[92:95], v[44:47], a[84:87], v[92:95]\n"
"s_addk_i32 m0, 6208\n"
"ds_read_b128 a[180:183], v236 offset:35840\n"
"v_mfma_f32_16x16x32_bf16 v[88:91], v[32:35], a[36:39], v[88:91]\n"
"ds_read_b128 a[136:139], v236 offset:36864\n"
"v_mfma_f32_16x16x32_bf16 v[88:91], v[40:43], a[84:87], v[88:91]\n"
"buffer_load_dwordx4 v231, s[48:51], s91 offen lds\n"
"v_mfma_f32_16x16x32_bf16 v[96:99], v[32:35], a[40:43], v[96:99]\n"
"s_addk_i32 m0, 1984\n"
"ds_read_b128 a[184:187], v236 offset:37888\n"
"v_mfma_f32_16x16x32_bf16 v[96:99], v[40:43], a[88:91], v[96:99]\n"
"ds_read_b128 a[140:143], v236 offset:38912\n"
"v_mfma_f32_16x16x32_bf16 v[100:103], v[36:39], a[40:43], v[100:103]\n"
"ds_read_b128 a[188:191], v236 offset:39936\n"
"v_mfma_f32_16x16x32_bf16 v[100:103], v[44:47], a[88:91], v[100:103]\n"
"v_mfma_f32_16x16x32_bf16 v[108:111], v[36:39], a[44:47], v[108:111]\n"
"buffer_load_dwordx4 v231, s[48:51], s91 offen offset:64 lds\n"
"v_mfma_f32_16x16x32_bf16 v[108:111], v[44:47], a[92:95], v[108:111]\n"
"v_mfma_f32_16x16x32_bf16 v[104:107], v[32:35], a[44:47], v[104:107]\n"
"v_mfma_f32_16x16x32_bf16 v[104:107], v[40:43], a[92:95], v[104:107]\n"
"s_waitcnt vmcnt(10) lgkmcnt(0)\n"
"s_barrier\n"
"v_mfma_f32_16x16x32_bf16 a[208:211], a[192:195], a[96:99], a[208:211]\n"
"s_add_u32 s92, s92, 0x12000\n"
"s_add_u32 m0, s87, 0x14000\n"
"ds_read_b64_tr_b16 v[32:33], v238 offset:24576\n"
"v_mfma_f32_16x16x32_bf16 a[208:211], a[200:203], a[144:147], a[208:211]\n"
"ds_read_b64_tr_b16 v[34:35], v238 offset:25600\n"
"v_mfma_f32_16x16x32_bf16 a[212:215], a[196:199], a[96:99], a[212:215]\n"
"buffer_load_dwordx4 v232, s[56:59], s92 offen sc1 lds\n"
"v_mfma_f32_16x16x32_bf16 a[212:215], a[204:207], a[144:147], a[212:215]\n"
"ds_read_b64_tr_b16 v[40:41], v238 offset:32768\n"
"v_mfma_f32_16x16x32_bf16 a[220:223], a[196:199], a[100:103], a[220:223]\n"
"ds_read_b64_tr_b16 v[42:43], v238 offset:33792\n"
"v_mfma_f32_16x16x32_bf16 a[220:223], a[204:207], a[148:151], a[220:223]\n"
"ds_read_b64_tr_b16 v[36:37], v239 offset:24576\n"
"v_mfma_f32_16x16x32_bf16 a[216:219], a[192:195], a[100:103], a[216:219]\n"
"ds_read_b64_tr_b16 v[38:39], v239 offset:25600\n"
"v_mfma_f32_16x16x32_bf16 a[216:219], a[200:203], a[148:151], a[216:219]\n"
"buffer_load_dwordx4 v232, s[56:59], s92 offen offset:1024 sc1 lds\n"
"v_mfma_f32_16x16x32_bf16 a[224:227], a[192:195], a[104:107], a[224:227]\n"
"ds_read_b64_tr_b16 v[44:45], v239 offset:32768\n"
"v_mfma_f32_16x16x32_bf16 a[224:227], a[200:203], a[152:155], a[224:227]\n"
"ds_read_b64_tr_b16 v[46:47], v239 offset:33792\n"
"v_mfma_f32_16x16x32_bf16 a[228:231], a[196:199], a[104:107], a[228:231]\n"
"ds_read_b128 a[0:3], v235\n"
"v_mfma_f32_16x16x32_bf16 a[228:231], a[204:207], a[152:155], a[228:231]\n"
"buffer_load_dwordx4 v232, s[56:59], s92 offen offset:2048 sc1 lds\n"
"v_mfma_f32_16x16x32_bf16 a[236:239], a[196:199], a[108:111], a[236:239]\n"
"ds_read_b128 a[48:51], v235 offset:1024\n"
"v_mfma_f32_16x16x32_bf16 a[236:239], a[204:207], a[156:159], a[236:239]\n"
"ds_read_b128 a[4:7], v235 offset:2048\n"
"v_mfma_f32_16x16x32_bf16 a[232:235], a[192:195], a[108:111], a[232:235]\n"
"ds_read_b128 a[52:55], v235 offset:3072\n"
"v_mfma_f32_16x16x32_bf16 a[232:235], a[200:203], a[156:159], a[232:235]\n"
"v_mfma_f32_16x16x32_bf16 a[240:243], a[192:195], a[112:115], a[240:243]\n"
"buffer_load_dwordx4 v232, s[56:59], s92 offen offset:3072 sc1 lds\n"
"v_mfma_f32_16x16x32_bf16 a[240:243], a[200:203], a[160:163], a[240:243]\n"
"s_add_u32 s94, s92, 0x1000\n"
"ds_read_b128 a[8:11], v235 offset:4096\n"
"v_mfma_f32_16x16x32_bf16 a[244:247], a[196:199], a[112:115], a[244:247]\n"
"s_addk_i32 m0, 4096\n"
"ds_read_b128 a[56:59], v235 offset:5120\n"
"v_mfma_f32_16x16x32_bf16 a[244:247], a[204:207], a[160:163], a[244:247]\n"
"ds_read_b128 a[12:15], v235 offset:6144\n"
"v_mfma_f32_16x16x32_bf16 a[252:255], a[196:199], a[116:119], a[252:255]\n"
"ds_read_b128 a[60:63], v235 offset:7168\n"
"v_mfma_f32_16x16x32_bf16 a[252:255], a[204:207], a[164:167], a[252:255]\n"
"buffer_load_dwordx4 v232, s[56:59], s94 offen sc1 lds\n"
"v_mfma_f32_16x16x32_bf16 a[248:251], a[192:195], a[116:119], a[248:251]\n"
"ds_read_b128 a[16:19], v235 offset:8192\n"
"v_mfma_f32_16x16x32_bf16 a[248:251], a[200:203], a[164:167], a[248:251]\n"
"ds_read_b128 a[64:67], v235 offset:9216\n"
"v_mfma_f32_16x16x32_bf16 v[64:67], a[192:195], a[120:123], v[64:67]\n"
"ds_read_b128 a[20:23], v235 offset:10240\n"
"v_mfma_f32_16x16x32_bf16 v[64:67], a[200:203], a[168:171], v[64:67]\n"
"buffer_load_dwordx4 v232, s[56:59], s94 offen offset:1024 sc1 lds\n"
"v_mfma_f32_16x16x32_bf16 v[68:71], a[196:199], a[120:123], v[68:71]\n"
"s_add_u32 s91, s91, 0x48000\n"
"ds_read_b128 a[68:71], v235 offset:11264\n"
"v_mfma_f32_16x16x32_bf16 v[68:71], a[204:207], a[168:171], v[68:71]\n"
"s_add_u32 m0, s85, 0x1a000\n"
"ds_read_b128 a[24:27], v235 offset:12288\n"
"v_mfma_f32_16x16x32_bf16 v[76:79], a[196:199], a[124:127], v[76:79]\n"
"ds_read_b128 a[72:75], v235 offset:13312\n"
"v_mfma_f32_16x16x32_bf16 v[76:79], a[204:207], a[172:175], v[76:79]\n"
"ds_read_b128 a[28:31], v235 offset:14336\n"
"v_mfma_f32_16x16x32_bf16 v[72:75], a[192:195], a[124:127], v[72:75]\n"
"buffer_load_dwordx4 v231, s[48:51], s91 offen lds\n"
"v_mfma_f32_16x16x32_bf16 v[72:75], a[200:203], a[172:175], v[72:75]\n"
"s_addk_i32 m0, 1984\n"
"ds_read_b128 a[76:79], v235 offset:15360\n"
"v_mfma_f32_16x16x32_bf16 v[80:83], a[192:195], a[128:131], v[80:83]\n"
"ds_read_b128 a[32:35], v235 offset:16384\n"
"v_mfma_f32_16x16x32_bf16 v[80:83], a[200:203], a[176:179], v[80:83]\n"
"ds_read_b128 a[80:83], v235 offset:17408\n"
"v_mfma_f32_16x16x32_bf16 v[84:87], a[196:199], a[128:131], v[84:87]\n"
"v_mfma_f32_16x16x32_bf16 v[84:87], a[204:207], a[176:179], v[84:87]\n"
"buffer_load_dwordx4 v231, s[48:51], s91 offen offset:64 lds\n"
"v_mfma_f32_16x16x32_bf16 v[92:95], a[196:199], a[132:135], v[92:95]\n"
"s_add_u32 s91, s91, 0x48000\n"
"ds_read_b128 a[36:39], v235 offset:18432\n"
"v_mfma_f32_16x16x32_bf16 v[92:95], a[204:207], a[180:183], v[92:95]\n"
"s_addk_i32 m0, 6208\n"
"ds_read_b128 a[84:87], v235 offset:19456\n"
"v_mfma_f32_16x16x32_bf16 v[88:91], a[192:195], a[132:135], v[88:91]\n"
"ds_read_b128 a[40:43], v235 offset:20480\n"
"v_mfma_f32_16x16x32_bf16 v[88:91], a[200:203], a[180:183], v[88:91]\n"
"buffer_load_dwordx4 v231, s[48:51], s91 offen lds\n"
"v_mfma_f32_16x16x32_bf16 v[96:99], a[192:195], a[136:139], v[96:99]\n"
"s_addk_i32 m0, 1984\n"
"ds_read_b128 a[88:91], v235 offset:21504\n"
"v_mfma_f32_16x16x32_bf16 v[96:99], a[200:203], a[184:187], v[96:99]\n"
"ds_read_b128 a[44:47], v235 offset:22528\n"
"v_mfma_f32_16x16x32_bf16 v[100:103], a[196:199], a[136:139], v[100:103]\n"
"ds_read_b128 a[92:95], v235 offset:23552\n"
"v_mfma_f32_16x16x32_bf16 v[100:103], a[204:207], a[184:187], v[100:103]\n"
"v_mfma_f32_16x16x32_bf16 v[108:111], a[196:199], a[140:143], v[108:111]\n"
"buffer_load_dwordx4 v231, s[48:51], s91 offen offset:64 lds\n"
"v_mfma_f32_16x16x32_bf16 v[108:111], a[204:207], a[188:191], v[108:111]\n"
"v_mfma_f32_16x16x32_bf16 v[104:107], a[192:195], a[140:143], v[104:107]\n"
"v_mfma_f32_16x16x32_bf16 v[104:107], a[200:203], a[188:191], v[104:107]\n"
"s_waitcnt vmcnt(10) lgkmcnt(0)\n"
"s_barrier\n"
"v_mfma_f32_16x16x32_bf16 a[208:211], v[32:35], a[0:3], a[208:211]\n"
"s_add_u32 s92, s92, 0x12000\n"
"s_add_u32 m0, s87, 0\n"
"ds_read_b64_tr_b16 a[192:193], v240\n"
"v_mfma_f32_16x16x32_bf16 a[208:211], v[40:43], a[48:51], a[208:211]\n"
"ds_read_b64_tr_b16 a[194:195], v240 offset:1024\n"
"v_mfma_f32_16x16x32_bf16 a[212:215], v[36:39], a[0:3], a[212:215]\n"
"buffer_load_dwordx4 v232, s[56:59], s92 offen sc1 lds\n"
"v_mfma_f32_16x16x32_bf16 a[212:215], v[44:47], a[48:51], a[212:215]\n"
"ds_read_b64_tr_b16 a[200:201], v240 offset:8192\n"
"v_mfma_f32_16x16x32_bf16 a[220:223], v[36:39], a[4:7], a[220:223]\n"
"ds_read_b64_tr_b16 a[202:203], v240 offset:9216\n"
"v_mfma_f32_16x16x32_bf16 a[220:223], v[44:47], a[52:55], a[220:223]\n"
"ds_read_b64_tr_b16 a[196:197], v241\n"
"v_mfma_f32_16x16x32_bf16 a[216:219], v[32:35], a[4:7], a[216:219]\n"
"ds_read_b64_tr_b16 a[198:199], v241 offset:1024\n"
"v_mfma_f32_16x16x32_bf16 a[216:219], v[40:43], a[52:55], a[216:219]\n"
"buffer_load_dwordx4 v232, s[56:59], s92 offen offset:1024 sc1 lds\n"
"v_mfma_f32_16x16x32_bf16 a[224:227], v[32:35], a[8:11], a[224:227]\n"
"ds_read_b64_tr_b16 a[204:205], v241 offset:8192\n"
"v_mfma_f32_16x16x32_bf16 a[224:227], v[40:43], a[56:59], a[224:227]\n"
"ds_read_b64_tr_b16 a[206:207], v241 offset:9216\n"
"v_mfma_f32_16x16x32_bf16 a[228:231], v[36:39], a[8:11], a[228:231]\n"
"ds_read_b128 a[96:99], v235 offset:40960\n"
"v_mfma_f32_16x16x32_bf16 a[228:231], v[44:47], a[56:59], a[228:231]\n"
"buffer_load_dwordx4 v232, s[56:59], s92 offen offset:2048 sc1 lds\n"
"v_mfma_f32_16x16x32_bf16 a[236:239], v[36:39], a[12:15], a[236:239]\n"
"ds_read_b128 a[144:147], v235 offset:41984\n"
"v_mfma_f32_16x16x32_bf16 a[236:239], v[44:47], a[60:63], a[236:239]\n"
"ds_read_b128 a[100:103], v235 offset:43008\n"
"v_mfma_f32_16x16x32_bf16 a[232:235], v[32:35], a[12:15], a[232:235]\n"
"ds_read_b128 a[148:151], v235 offset:44032\n"
"v_mfma_f32_16x16x32_bf16 a[232:235], v[40:43], a[60:63], a[232:235]\n"
"v_mfma_f32_16x16x32_bf16 a[240:243], v[32:35], a[16:19], a[240:243]\n"
"buffer_load_dwordx4 v232, s[56:59], s92 offen offset:3072 sc1 lds\n"
"v_mfma_f32_16x16x32_bf16 a[240:243], v[40:43], a[64:67], a[240:243]\n"
"s_add_u32 s94, s92, 0x1000\n"
"ds_read_b128 a[104:107], v235 offset:45056\n"
"v_mfma_f32_16x16x32_bf16 a[244:247], v[36:39], a[16:19], a[244:247]\n"
"s_addk_i32 m0, 4096\n"
"ds_read_b128 a[152:155], v235 offset:46080\n"
"v_mfma_f32_16x16x32_bf16 a[244:247], v[44:47], a[64:67], a[244:247]\n"
"ds_read_b128 a[108:111], v235 offset:47104\n"
"v_mfma_f32_16x16x32_bf16 a[252:255], v[36:39], a[20:23], a[252:255]\n"
"ds_read_b128 a[156:159], v235 offset:48128\n"
"v_mfma_f32_16x16x32_bf16 a[252:255], v[44:47], a[68:71], a[252:255]\n"
"buffer_load_dwordx4 v232, s[56:59], s94 offen sc1 lds\n"
"v_mfma_f32_16x16x32_bf16 a[248:251], v[32:35], a[20:23], a[248:251]\n"
"ds_read_b128 a[112:115], v235 offset:49152\n"
"v_mfma_f32_16x16x32_bf16 a[248:251], v[40:43], a[68:71], a[248:251]\n"
"ds_read_b128 a[160:163], v235 offset:50176\n"
"v_mfma_f32_16x16x32_bf16 v[64:67], v[32:35], a[24:27], v[64:67]\n"
"ds_read_b128 a[116:119], v235 offset:51200\n"
"v_mfma_f32_16x16x32_bf16 v[64:67], v[40:43], a[72:75], v[64:67]\n"
"buffer_load_dwordx4 v232, s[56:59], s94 offen offset:1024 sc1 lds\n"
"v_mfma_f32_16x16x32_bf16 v[68:71], v[36:39], a[24:27], v[68:71]\n"
"s_add_u32 s91, s91, 0x48000\n"
"ds_read_b128 a[164:167], v235 offset:52224\n"
"v_mfma_f32_16x16x32_bf16 v[68:71], v[44:47], a[72:75], v[68:71]\n"
"s_add_u32 m0, s85, 0x6000\n"
"ds_read_b128 a[120:123], v235 offset:53248\n"
"v_mfma_f32_16x16x32_bf16 v[76:79], v[36:39], a[28:31], v[76:79]\n"
"ds_read_b128 a[168:171], v235 offset:54272\n"
"v_mfma_f32_16x16x32_bf16 v[76:79], v[44:47], a[76:79], v[76:79]\n"
"ds_read_b128 a[124:127], v235 offset:55296\n"
"v_mfma_f32_16x16x32_bf16 v[72:75], v[32:35], a[28:31], v[72:75]\n"
"buffer_load_dwordx4 v231, s[48:51], s91 offen lds\n"
"v_mfma_f32_16x16x32_bf16 v[72:75], v[40:43], a[76:79], v[72:75]\n"
"s_addk_i32 m0, 1984\n"
"ds_read_b128 a[172:175], v235 offset:56320\n"
"v_mfma_f32_16x16x32_bf16 v[80:83], v[32:35], a[32:35], v[80:83]\n"
"ds_read_b128 a[128:131], v235 offset:57344\n"
"v_mfma_f32_16x16x32_bf16 v[80:83], v[40:43], a[80:83], v[80:83]\n"
"ds_read_b128 a[176:179], v235 offset:58368\n"
"v_mfma_f32_16x16x32_bf16 v[84:87], v[36:39], a[32:35], v[84:87]\n"
"v_mfma_f32_16x16x32_bf16 v[84:87], v[44:47], a[80:83], v[84:87]\n"
"buffer_load_dwordx4 v231, s[48:51], s91 offen offset:64 lds\n"
"v_mfma_f32_16x16x32_bf16 v[92:95], v[36:39], a[36:39], v[92:95]\n"
"s_add_u32 s91, s91, 0x48000\n"
"ds_read_b128 a[132:135], v235 offset:59392\n"
"v_mfma_f32_16x16x32_bf16 v[92:95], v[44:47], a[84:87], v[92:95]\n"
"s_addk_i32 m0, 6208\n"
"ds_read_b128 a[180:183], v235 offset:60416\n"
"v_mfma_f32_16x16x32_bf16 v[88:91], v[32:35], a[36:39], v[88:91]\n"
"ds_read_b128 a[136:139], v235 offset:61440\n"
"v_mfma_f32_16x16x32_bf16 v[88:91], v[40:43], a[84:87], v[88:91]\n"
"buffer_load_dwordx4 v231, s[48:51], s91 offen lds\n"
"v_mfma_f32_16x16x32_bf16 v[96:99], v[32:35], a[40:43], v[96:99]\n"
"s_addk_i32 m0, 1984\n"
"ds_read_b128 a[184:187], v235 offset:62464\n"
"v_mfma_f32_16x16x32_bf16 v[96:99], v[40:43], a[88:91], v[96:99]\n"
"ds_read_b128 a[140:143], v235 offset:63488\n"
"v_mfma_f32_16x16x32_bf16 v[100:103], v[36:39], a[40:43], v[100:103]\n"
"ds_read_b128 a[188:191], v235 offset:64512\n"
"v_mfma_f32_16x16x32_bf16 v[100:103], v[44:47], a[88:91], v[100:103]\n"
"v_mfma_f32_16x16x32_bf16 v[108:111], v[36:39], a[44:47], v[108:111]\n"
"buffer_load_dwordx4 v231, s[48:51], s91 offen offset:64 lds\n"
"v_mfma_f32_16x16x32_bf16 v[108:111], v[44:47], a[92:95], v[108:111]\n"
"v_mfma_f32_16x16x32_bf16 v[104:107], v[32:35], a[44:47], v[104:107]\n"
"v_mfma_f32_16x16x32_bf16 v[104:107], v[40:43], a[92:95], v[104:107]\n"
"s_waitcnt vmcnt(10) lgkmcnt(0)\n"
"s_barrier\n"
"v_mfma_f32_16x16x32_bf16 a[208:211], a[192:195], a[96:99], a[208:211]\n"
"s_add_u32 s92, s92, 0x12000\n"
"s_add_u32 m0, s87, 0xa000\n"
"ds_read_b64_tr_b16 v[32:33], v240 offset:40960\n"
"v_mfma_f32_16x16x32_bf16 a[208:211], a[200:203], a[144:147], a[208:211]\n"
"ds_read_b64_tr_b16 v[34:35], v240 offset:41984\n"
"v_mfma_f32_16x16x32_bf16 a[212:215], a[196:199], a[96:99], a[212:215]\n"
"buffer_load_dwordx4 v232, s[56:59], s92 offen sc1 lds\n"
"v_mfma_f32_16x16x32_bf16 a[212:215], a[204:207], a[144:147], a[212:215]\n"
"ds_read_b64_tr_b16 v[40:41], v240 offset:49152\n"
"v_mfma_f32_16x16x32_bf16 a[220:223], a[196:199], a[100:103], a[220:223]\n"
"ds_read_b64_tr_b16 v[42:43], v240 offset:50176\n"
"v_mfma_f32_16x16x32_bf16 a[220:223], a[204:207], a[148:151], a[220:223]\n"
"ds_read_b64_tr_b16 v[36:37], v241 offset:40960\n"
"v_mfma_f32_16x16x32_bf16 a[216:219], a[192:195], a[100:103], a[216:219]\n"
"ds_read_b64_tr_b16 v[38:39], v241 offset:41984\n"
"v_mfma_f32_16x16x32_bf16 a[216:219], a[200:203], a[148:151], a[216:219]\n"
"buffer_load_dwordx4 v232, s[56:59], s92 offen offset:1024 sc1 lds\n"
"v_mfma_f32_16x16x32_bf16 a[224:227], a[192:195], a[104:107], a[224:227]\n"
"ds_read_b64_tr_b16 v[44:45], v241 offset:49152\n"
"v_mfma_f32_16x16x32_bf16 a[224:227], a[200:203], a[152:155], a[224:227]\n"
"ds_read_b64_tr_b16 v[46:47], v241 offset:50176\n"
"v_mfma_f32_16x16x32_bf16 a[228:231], a[196:199], a[104:107], a[228:231]\n"
"ds_read_b128 a[0:3], v236 offset:16384\n"
"v_mfma_f32_16x16x32_bf16 a[228:231], a[204:207], a[152:155], a[228:231]\n"
"buffer_load_dwordx4 v232, s[56:59], s92 offen offset:2048 sc1 lds\n"
"v_mfma_f32_16x16x32_bf16 a[236:239], a[196:199], a[108:111], a[236:239]\n"
"ds_read_b128 a[48:51], v236 offset:17408\n"
"v_mfma_f32_16x16x32_bf16 a[236:239], a[204:207], a[156:159], a[236:239]\n"
"ds_read_b128 a[4:7], v236 offset:18432\n"
"v_mfma_f32_16x16x32_bf16 a[232:235], a[192:195], a[108:111], a[232:235]\n"
"ds_read_b128 a[52:55], v236 offset:19456\n"
"v_mfma_f32_16x16x32_bf16 a[232:235], a[200:203], a[156:159], a[232:235]\n"
"v_mfma_f32_16x16x32_bf16 a[240:243], a[192:195], a[112:115], a[240:243]\n"
"buffer_load_dwordx4 v232, s[56:59], s92 offen offset:3072 sc1 lds\n"
"v_mfma_f32_16x16x32_bf16 a[240:243], a[200:203], a[160:163], a[240:243]\n"
"s_add_u32 s94, s92, 0x1000\n"
"ds_read_b128 a[8:11], v236 offset:20480\n"
"v_mfma_f32_16x16x32_bf16 a[244:247], a[196:199], a[112:115], a[244:247]\n"
"s_addk_i32 m0, 4096\n"
"ds_read_b128 a[56:59], v236 offset:21504\n"
"v_mfma_f32_16x16x32_bf16 a[244:247], a[204:207], a[160:163], a[244:247]\n"
"ds_read_b128 a[12:15], v236 offset:22528\n"
"v_mfma_f32_16x16x32_bf16 a[252:255], a[196:199], a[116:119], a[252:255]\n"
"ds_read_b128 a[60:63], v236 offset:23552\n"
"v_mfma_f32_16x16x32_bf16 a[252:255], a[204:207], a[164:167], a[252:255]\n"
"buffer_load_dwordx4 v232, s[56:59], s94 offen sc1 lds\n"
"v_mfma_f32_16x16x32_bf16 a[248:251], a[192:195], a[116:119], a[248:251]\n"
"ds_read_b128 a[16:19], v236 offset:24576\n"
"v_mfma_f32_16x16x32_bf16 a[248:251], a[200:203], a[164:167], a[248:251]\n"
"ds_read_b128 a[64:67], v236 offset:25600\n"
"v_mfma_f32_16x16x32_bf16 v[64:67], a[192:195], a[120:123], v[64:67]\n"
"ds_read_b128 a[20:23], v236 offset:26624\n"
"v_mfma_f32_16x16x32_bf16 v[64:67], a[200:203], a[168:171], v[64:67]\n"
"buffer_load_dwordx4 v232, s[56:59], s94 offen offset:1024 sc1 lds\n"
"v_mfma_f32_16x16x32_bf16 v[68:71], a[196:199], a[120:123], v[68:71]\n"
"s_add_u32 s91, s91, 0x48000\n"
"ds_read_b128 a[68:71], v236 offset:27648\n"
"v_mfma_f32_16x16x32_bf16 v[68:71], a[204:207], a[168:171], v[68:71]\n"
"s_add_u32 m0, s85, 0x10000\n"
"ds_read_b128 a[24:27], v236 offset:28672\n"
"v_mfma_f32_16x16x32_bf16 v[76:79], a[196:199], a[124:127], v[76:79]\n"
"ds_read_b128 a[72:75], v236 offset:29696\n"
"v_mfma_f32_16x16x32_bf16 v[76:79], a[204:207], a[172:175], v[76:79]\n"
"ds_read_b128 a[28:31], v236 offset:30720\n"
"v_mfma_f32_16x16x32_bf16 v[72:75], a[192:195], a[124:127], v[72:75]\n"
"buffer_load_dwordx4 v231, s[48:51], s91 offen lds\n"
"v_mfma_f32_16x16x32_bf16 v[72:75], a[200:203], a[172:175], v[72:75]\n"
"s_addk_i32 m0, 1984\n"
"ds_read_b128 a[76:79], v236 offset:31744\n"
"v_mfma_f32_16x16x32_bf16 v[80:83], a[192:195], a[128:131], v[80:83]\n"
"ds_read_b128 a[32:35], v236 offset:32768\n"
"v_mfma_f32_16x16x32_bf16 v[80:83], a[200:203], a[176:179], v[80:83]\n"
"ds_read_b128 a[80:83], v236 offset:33792\n"
"v_mfma_f32_16x16x32_bf16 v[84:87], a[196:199], a[128:131], v[84:87]\n"
"v_mfma_f32_16x16x32_bf16 v[84:87], a[204:207], a[176:179], v[84:87]\n"
"buffer_load_dwordx4 v231, s[48:51], s91 offen offset:64 lds\n"
"v_mfma_f32_16x16x32_bf16 v[92:95], a[196:199], a[132:135], v[92:95]\n"
"s_add_u32 s91, s91, 0x48000\n"
"ds_read_b128 a[36:39], v236 offset:34816\n"
"v_mfma_f32_16x16x32_bf16 v[92:95], a[204:207], a[180:183], v[92:95]\n"
"s_addk_i32 m0, 6208\n"
"ds_read_b128 a[84:87], v236 offset:35840\n"
"v_mfma_f32_16x16x32_bf16 v[88:91], a[192:195], a[132:135], v[88:91]\n"
"ds_read_b128 a[40:43], v236 offset:36864\n"
"v_mfma_f32_16x16x32_bf16 v[88:91], a[200:203], a[180:183], v[88:91]\n"
"buffer_load_dwordx4 v231, s[48:51], s91 offen lds\n"
"v_mfma_f32_16x16x32_bf16 v[96:99], a[192:195], a[136:139], v[96:99]\n"
"s_addk_i32 m0, 1984\n"
"ds_read_b128 a[88:91], v236 offset:37888\n"
"v_mfma_f32_16x16x32_bf16 v[96:99], a[200:203], a[184:187], v[96:99]\n"
"ds_read_b128 a[44:47], v236 offset:38912\n"
"v_mfma_f32_16x16x32_bf16 v[100:103], a[196:199], a[136:139], v[100:103]\n"
"ds_read_b128 a[92:95], v236 offset:39936\n"
"v_mfma_f32_16x16x32_bf16 v[100:103], a[204:207], a[184:187], v[100:103]\n"
"v_mfma_f32_16x16x32_bf16 v[108:111], a[196:199], a[140:143], v[108:111]\n"
"buffer_load_dwordx4 v231, s[48:51], s91 offen offset:64 lds\n"
"v_mfma_f32_16x16x32_bf16 v[108:111], a[204:207], a[188:191], v[108:111]\n"
"v_mfma_f32_16x16x32_bf16 v[104:107], a[192:195], a[140:143], v[104:107]\n"
"v_mfma_f32_16x16x32_bf16 v[104:107], a[200:203], a[188:191], v[104:107]\n"
"s_waitcnt vmcnt(10) lgkmcnt(0)\n"
"s_barrier\n"
"v_mfma_f32_16x16x32_bf16 a[208:211], v[32:35], a[0:3], a[208:211]\n"
"s_add_u32 s92, s92, 0x12000\n"
"s_add_u32 m0, s87, 0x14000\n"
"ds_read_b64_tr_b16 a[192:193], v238 offset:24576\n"
"v_mfma_f32_16x16x32_bf16 a[208:211], v[40:43], a[48:51], a[208:211]\n"
"ds_read_b64_tr_b16 a[194:195], v238 offset:25600\n"
"v_mfma_f32_16x16x32_bf16 a[212:215], v[36:39], a[0:3], a[212:215]\n"
"buffer_load_dwordx4 v232, s[56:59], s92 offen sc1 lds\n"
"v_mfma_f32_16x16x32_bf16 a[212:215], v[44:47], a[48:51], a[212:215]\n"
"ds_read_b64_tr_b16 a[200:201], v238 offset:32768\n"
"v_mfma_f32_16x16x32_bf16 a[220:223], v[36:39], a[4:7], a[220:223]\n"
"ds_read_b64_tr_b16 a[202:203], v238 offset:33792\n"
"v_mfma_f32_16x16x32_bf16 a[220:223], v[44:47], a[52:55], a[220:223]\n"
"ds_read_b64_tr_b16 a[196:197], v239 offset:24576\n"
"v_mfma_f32_16x16x32_bf16 a[216:219], v[32:35], a[4:7], a[216:219]\n"
"ds_read_b64_tr_b16 a[198:199], v239 offset:25600\n"
"v_mfma_f32_16x16x32_bf16 a[216:219], v[40:43], a[52:55], a[216:219]\n"
"buffer_load_dwordx4 v232, s[56:59], s92 offen offset:1024 sc1 lds\n"
"v_mfma_f32_16x16x32_bf16 a[224:227], v[32:35], a[8:11], a[224:227]\n"
"ds_read_b64_tr_b16 a[204:205], v239 offset:32768\n"
"v_mfma_f32_16x16x32_bf16 a[224:227], v[40:43], a[56:59], a[224:227]\n"
"ds_read_b64_tr_b16 a[206:207], v239 offset:33792\n"
"v_mfma_f32_16x16x32_bf16 a[228:231], v[36:39], a[8:11], a[228:231]\n"
"ds_read_b128 a[96:99], v235\n"
"v_mfma_f32_16x16x32_bf16 a[228:231], v[44:47], a[56:59], a[228:231]\n"
"buffer_load_dwordx4 v232, s[56:59], s92 offen offset:2048 sc1 lds\n"
"v_mfma_f32_16x16x32_bf16 a[236:239], v[36:39], a[12:15], a[236:239]\n"
"ds_read_b128 a[144:147], v235 offset:1024\n"
"v_mfma_f32_16x16x32_bf16 a[236:239], v[44:47], a[60:63], a[236:239]\n"
"ds_read_b128 a[100:103], v235 offset:2048\n"
"v_mfma_f32_16x16x32_bf16 a[232:235], v[32:35], a[12:15], a[232:235]\n"
"ds_read_b128 a[148:151], v235 offset:3072\n"
"v_mfma_f32_16x16x32_bf16 a[232:235], v[40:43], a[60:63], a[232:235]\n"
"v_mfma_f32_16x16x32_bf16 a[240:243], v[32:35], a[16:19], a[240:243]\n"
"buffer_load_dwordx4 v232, s[56:59], s92 offen offset:3072 sc1 lds\n"
"v_mfma_f32_16x16x32_bf16 a[240:243], v[40:43], a[64:67], a[240:243]\n"
"s_add_u32 s94, s92, 0x1000\n"
"ds_read_b128 a[104:107], v235 offset:4096\n"
"v_mfma_f32_16x16x32_bf16 a[244:247], v[36:39], a[16:19], a[244:247]\n"
"s_addk_i32 m0, 4096\n"
"ds_read_b128 a[152:155], v235 offset:5120\n"
"v_mfma_f32_16x16x32_bf16 a[244:247], v[44:47], a[64:67], a[244:247]\n"
"ds_read_b128 a[108:111], v235 offset:6144\n"
"v_mfma_f32_16x16x32_bf16 a[252:255], v[36:39], a[20:23], a[252:255]\n"
"ds_read_b128 a[156:159], v235 offset:7168\n"
"v_mfma_f32_16x16x32_bf16 a[252:255], v[44:47], a[68:71], a[252:255]\n"
"buffer_load_dwordx4 v232, s[56:59], s94 offen sc1 lds\n"
"v_mfma_f32_16x16x32_bf16 a[248:251], v[32:35], a[20:23], a[248:251]\n"
"ds_read_b128 a[112:115], v235 offset:8192\n"
"v_mfma_f32_16x16x32_bf16 a[248:251], v[40:43], a[68:71], a[248:251]\n"
"ds_read_b128 a[160:163], v235 offset:9216\n"
"v_mfma_f32_16x16x32_bf16 v[64:67], v[32:35], a[24:27], v[64:67]\n"
"ds_read_b128 a[116:119], v235 offset:10240\n"
"v_mfma_f32_16x16x32_bf16 v[64:67], v[40:43], a[72:75], v[64:67]\n"
"buffer_load_dwordx4 v232, s[56:59], s94 offen offset:1024 sc1 lds\n"
"v_mfma_f32_16x16x32_bf16 v[68:71], v[36:39], a[24:27], v[68:71]\n"
"s_add_u32 s91, s91, 0x48000\n"
"ds_read_b128 a[164:167], v235 offset:11264\n"
"v_mfma_f32_16x16x32_bf16 v[68:71], v[44:47], a[72:75], v[68:71]\n"
"s_add_u32 m0, s85, 0x1a000\n"
"ds_read_b128 a[120:123], v235 offset:12288\n"
"v_mfma_f32_16x16x32_bf16 v[76:79], v[36:39], a[28:31], v[76:79]\n"
"ds_read_b128 a[168:171], v235 offset:13312\n"
"v_mfma_f32_16x16x32_bf16 v[76:79], v[44:47], a[76:79], v[76:79]\n"
"ds_read_b128 a[124:127], v235 offset:14336\n"
"v_mfma_f32_16x16x32_bf16 v[72:75], v[32:35], a[28:31], v[72:75]\n"
"buffer_load_dwordx4 v231, s[48:51], s91 offen lds\n"
"v_mfma_f32_16x16x32_bf16 v[72:75], v[40:43], a[76:79], v[72:75]\n"
"s_addk_i32 m0, 1984\n"
"ds_read_b128 a[172:175], v235 offset:15360\n"
"v_mfma_f32_16x16x32_bf16 v[80:83], v[32:35], a[32:35], v[80:83]\n"
"ds_read_b128 a[128:131], v235 offset:16384\n"
"v_mfma_f32_16x16x32_bf16 v[80:83], v[40:43], a[80:83], v[80:83]\n"
"ds_read_b128 a[176:179], v235 offset:17408\n"
"v_mfma_f32_16x16x32_bf16 v[84:87], v[36:39], a[32:35], v[84:87]\n"
"v_mfma_f32_16x16x32_bf16 v[84:87], v[44:47], a[80:83], v[84:87]\n"
"buffer_load_dwordx4 v231, s[48:51], s91 offen offset:64 lds\n"
"v_mfma_f32_16x16x32_bf16 v[92:95], v[36:39], a[36:39], v[92:95]\n"
"s_add_u32 s91, s91, 0x48000\n"
"ds_read_b128 a[132:135], v235 offset:18432\n"
"v_mfma_f32_16x16x32_bf16 v[92:95], v[44:47], a[84:87], v[92:95]\n"
"s_addk_i32 m0, 6208\n"
"ds_read_b128 a[180:183], v235 offset:19456\n"
"v_mfma_f32_16x16x32_bf16 v[88:91], v[32:35], a[36:39], v[88:91]\n"
"ds_read_b128 a[136:139], v235 offset:20480\n"
"v_mfma_f32_16x16x32_bf16 v[88:91], v[40:43], a[84:87], v[88:91]\n"
"buffer_load_dwordx4 v231, s[48:51], s91 offen lds\n"
"v_mfma_f32_16x16x32_bf16 v[96:99], v[32:35], a[40:43], v[96:99]\n"
"s_addk_i32 m0, 1984\n"
"ds_read_b128 a[184:187], v235 offset:21504\n"
"v_mfma_f32_16x16x32_bf16 v[96:99], v[40:43], a[88:91], v[96:99]\n"
"ds_read_b128 a[140:143], v235 offset:22528\n"
"v_mfma_f32_16x16x32_bf16 v[100:103], v[36:39], a[40:43], v[100:103]\n"
"ds_read_b128 a[188:191], v235 offset:23552\n"
"v_mfma_f32_16x16x32_bf16 v[100:103], v[44:47], a[88:91], v[100:103]\n"
"v_mfma_f32_16x16x32_bf16 v[108:111], v[36:39], a[44:47], v[108:111]\n"
"buffer_load_dwordx4 v231, s[48:51], s91 offen offset:64 lds\n"
"v_mfma_f32_16x16x32_bf16 v[108:111], v[44:47], a[92:95], v[108:111]\n"
"v_mfma_f32_16x16x32_bf16 v[104:107], v[32:35], a[44:47], v[104:107]\n"
"v_mfma_f32_16x16x32_bf16 v[104:107], v[40:43], a[92:95], v[104:107]\n"
"s_waitcnt vmcnt(10) lgkmcnt(0)\n"
"s_barrier\n"
"v_mfma_f32_16x16x32_bf16 a[208:211], a[192:195], a[96:99], a[208:211]\n"
"s_add_u32 s92, s92, 0x12000\n"
"s_add_u32 m0, s87, 0\n"
"ds_read_b64_tr_b16 v[32:33], v240\n"
"v_mfma_f32_16x16x32_bf16 a[208:211], a[200:203], a[144:147], a[208:211]\n"
"ds_read_b64_tr_b16 v[34:35], v240 offset:1024\n"
"v_mfma_f32_16x16x32_bf16 a[212:215], a[196:199], a[96:99], a[212:215]\n"
"buffer_load_dwordx4 v232, s[56:59], s92 offen sc1 lds\n"
"v_mfma_f32_16x16x32_bf16 a[212:215], a[204:207], a[144:147], a[212:215]\n"
"ds_read_b64_tr_b16 v[40:41], v240 offset:8192\n"
"v_mfma_f32_16x16x32_bf16 a[220:223], a[196:199], a[100:103], a[220:223]\n"
"ds_read_b64_tr_b16 v[42:43], v240 offset:9216\n"
"v_mfma_f32_16x16x32_bf16 a[220:223], a[204:207], a[148:151], a[220:223]\n"
"ds_read_b64_tr_b16 v[36:37], v241\n"
"v_mfma_f32_16x16x32_bf16 a[216:219], a[192:195], a[100:103], a[216:219]\n"
"ds_read_b64_tr_b16 v[38:39], v241 offset:1024\n"
"v_mfma_f32_16x16x32_bf16 a[216:219], a[200:203], a[148:151], a[216:219]\n"
"buffer_load_dwordx4 v232, s[56:59], s92 offen offset:1024 sc1 lds\n"
"v_mfma_f32_16x16x32_bf16 a[224:227], a[192:195], a[104:107], a[224:227]\n"
"ds_read_b64_tr_b16 v[44:45], v241 offset:8192\n"
"v_mfma_f32_16x16x32_bf16 a[224:227], a[200:203], a[152:155], a[224:227]\n"
"ds_read_b64_tr_b16 v[46:47], v241 offset:9216\n"
"v_mfma_f32_16x16x32_bf16 a[228:231], a[196:199], a[104:107], a[228:231]\n"
"ds_read_b128 a[0:3], v235 offset:40960\n"
"v_mfma_f32_16x16x32_bf16 a[228:231], a[204:207], a[152:155], a[228:231]\n"
"buffer_load_dwordx4 v232, s[56:59], s92 offen offset:2048 sc1 lds\n"
"v_mfma_f32_16x16x32_bf16 a[236:239], a[196:199], a[108:111], a[236:239]\n"
"ds_read_b128 a[48:51], v235 offset:41984\n"
"v_mfma_f32_16x16x32_bf16 a[236:239], a[204:207], a[156:159], a[236:239]\n"
"ds_read_b128 a[4:7], v235 offset:43008\n"
"v_mfma_f32_16x16x32_bf16 a[232:235], a[192:195], a[108:111], a[232:235]\n"
"ds_read_b128 a[52:55], v235 offset:44032\n"
"v_mfma_f32_16x16x32_bf16 a[232:235], a[200:203], a[156:159], a[232:235]\n"
"v_mfma_f32_16x16x32_bf16 a[240:243], a[192:195], a[112:115], a[240:243]\n"
"buffer_load_dwordx4 v232, s[56:59], s92 offen offset:3072 sc1 lds\n"
"v_mfma_f32_16x16x32_bf16 a[240:243], a[200:203], a[160:163], a[240:243]\n"
"s_add_u32 s94, s92, 0x1000\n"
"ds_read_b128 a[8:11], v235 offset:45056\n"
"v_mfma_f32_16x16x32_bf16 a[244:247], a[196:199], a[112:115], a[244:247]\n"
"s_addk_i32 m0, 4096\n"
"ds_read_b128 a[56:59], v235 offset:46080\n"
"v_mfma_f32_16x16x32_bf16 a[244:247], a[204:207], a[160:163], a[244:247]\n"
"ds_read_b128 a[12:15], v235 offset:47104\n"
"v_mfma_f32_16x16x32_bf16 a[252:255], a[196:199], a[116:119], a[252:255]\n"
"ds_read_b128 a[60:63], v235 offset:48128\n"
"v_mfma_f32_16x16x32_bf16 a[252:255], a[204:207], a[164:167], a[252:255]\n"
"buffer_load_dwordx4 v232, s[56:59], s94 offen sc1 lds\n"
"v_mfma_f32_16x16x32_bf16 a[248:251], a[192:195], a[116:119], a[248:251]\n"
"ds_read_b128 a[16:19], v235 offset:49152\n"
"v_mfma_f32_16x16x32_bf16 a[248:251], a[200:203], a[164:167], a[248:251]\n"
"ds_read_b128 a[64:67], v235 offset:50176\n"
"v_mfma_f32_16x16x32_bf16 v[64:67], a[192:195], a[120:123], v[64:67]\n"
"ds_read_b128 a[20:23], v235 offset:51200\n"
"v_mfma_f32_16x16x32_bf16 v[64:67], a[200:203], a[168:171], v[64:67]\n"
"buffer_load_dwordx4 v232, s[56:59], s94 offen offset:1024 sc1 lds\n"
"v_mfma_f32_16x16x32_bf16 v[68:71], a[196:199], a[120:123], v[68:71]\n"
"s_add_u32 s91, s91, 0x48000\n"
"ds_read_b128 a[68:71], v235 offset:52224\n"
"v_mfma_f32_16x16x32_bf16 v[68:71], a[204:207], a[168:171], v[68:71]\n"
"s_add_u32 m0, s85, 0x6000\n"
"ds_read_b128 a[24:27], v235 offset:53248\n"
"v_mfma_f32_16x16x32_bf16 v[76:79], a[196:199], a[124:127], v[76:79]\n"
"ds_read_b128 a[72:75], v235 offset:54272\n"
"v_mfma_f32_16x16x32_bf16 v[76:79], a[204:207], a[172:175], v[76:79]\n"
"ds_read_b128 a[28:31], v235 offset:55296\n"
"v_mfma_f32_16x16x32_bf16 v[72:75], a[192:195], a[124:127], v[72:75]\n"
"buffer_load_dwordx4 v231, s[48:51], s91 offen lds\n"
"v_mfma_f32_16x16x32_bf16 v[72:75], a[200:203], a[172:175], v[72:75]\n"
"s_addk_i32 m0, 1984\n"
"ds_read_b128 a[76:79], v235 offset:56320\n"
"v_mfma_f32_16x16x32_bf16 v[80:83], a[192:195], a[128:131], v[80:83]\n"
"ds_read_b128 a[32:35], v235 offset:57344\n"
"v_mfma_f32_16x16x32_bf16 v[80:83], a[200:203], a[176:179], v[80:83]\n"
"ds_read_b128 a[80:83], v235 offset:58368\n"
"v_mfma_f32_16x16x32_bf16 v[84:87], a[196:199], a[128:131], v[84:87]\n"
"v_mfma_f32_16x16x32_bf16 v[84:87], a[204:207], a[176:179], v[84:87]\n"
"buffer_load_dwordx4 v231, s[48:51], s91 offen offset:64 lds\n"
"v_mfma_f32_16x16x32_bf16 v[92:95], a[196:199], a[132:135], v[92:95]\n"
"s_add_u32 s91, s91, 0x48000\n"
"ds_read_b128 a[36:39], v235 offset:59392\n"
"v_mfma_f32_16x16x32_bf16 v[92:95], a[204:207], a[180:183], v[92:95]\n"
"s_addk_i32 m0, 6208\n"
"ds_read_b128 a[84:87], v235 offset:60416\n"
"v_mfma_f32_16x16x32_bf16 v[88:91], a[192:195], a[132:135], v[88:91]\n"
"ds_read_b128 a[40:43], v235 offset:61440\n"
"v_mfma_f32_16x16x32_bf16 v[88:91], a[200:203], a[180:183], v[88:91]\n"
"buffer_load_dwordx4 v231, s[48:51], s91 offen lds\n"
"v_mfma_f32_16x16x32_bf16 v[96:99], a[192:195], a[136:139], v[96:99]\n"
"s_addk_i32 m0, 1984\n"
"ds_read_b128 a[88:91], v235 offset:62464\n"
"v_mfma_f32_16x16x32_bf16 v[96:99], a[200:203], a[184:187], v[96:99]\n"
"s_sub_u32 s13, s13, 1\n"
"ds_read_b128 a[44:47], v235 offset:63488\n"
"v_mfma_f32_16x16x32_bf16 v[100:103], a[196:199], a[136:139], v[100:103]\n"
"ds_read_b128 a[92:95], v235 offset:64512\n"
"v_mfma_f32_16x16x32_bf16 v[100:103], a[204:207], a[184:187], v[100:103]\n"
"v_mfma_f32_16x16x32_bf16 v[108:111], a[196:199], a[140:143], v[108:111]\n"
"buffer_load_dwordx4 v231, s[48:51], s91 offen offset:64 lds\n"
"v_mfma_f32_16x16x32_bf16 v[108:111], a[204:207], a[188:191], v[108:111]\n"
"v_mfma_f32_16x16x32_bf16 v[104:107], a[192:195], a[140:143], v[104:107]\n"
"s_cmp_lg_u32 s13, 0\n"
"v_mfma_f32_16x16x32_bf16 v[104:107], a[200:203], a[188:191], v[104:107]\n"
"s_waitcnt vmcnt(10) lgkmcnt(0)\n"
"s_barrier\n"
"s_cbranch_scc1 L_b1\n"
"v_mfma_f32_16x16x32_bf16 a[208:211], v[32:35], a[0:3], a[208:211]\n"
"s_mov_b32 s90, 0\n"
"s_add_u32 m0, s86, 0x20000\n"
"ds_read_b64_tr_b16 a[192:193], v240 offset:40960\n"
"v_mfma_f32_16x16x32_bf16 a[208:211], v[40:43], a[48:51], a[208:211]\n"
"ds_read_b64_tr_b16 a[194:195], v240 offset:41984\n"
"v_lshrrev_b32 v251, 2, v224\n"
"v_mfma_f32_16x16x32_bf16 a[212:215], v[36:39], a[0:3], a[212:215]\n"
"buffer_load_dwordx4 v244, s[40:43], s90 offen lds\n"
"v_mfma_f32_16x16x32_bf16 a[212:215], v[44:47], a[48:51], a[212:215]\n"
"s_addk_i32 m0, 4032\n"
"ds_read_b64_tr_b16 a[200:201], v240 offset:49152\n"
"v_mul_u32_u24 v251, 0x480, v251\n"
"v_mfma_f32_16x16x32_bf16 a[220:223], v[36:39], a[4:7], a[220:223]\n"
"ds_read_b64_tr_b16 a[202:203], v240 offset:50176\n"
"v_and_b32 v252, 3, v224\n"
"v_mfma_f32_16x16x32_bf16 a[220:223], v[44:47], a[52:55], a[220:223]\n"
"ds_read_b64_tr_b16 a[196:197], v241 offset:40960\n"
"v_bfe_u32 v253, v224, 4, 1\n"
"v_mfma_f32_16x16x32_bf16 a[216:219], v[32:35], a[4:7], a[216:219]\n"
"ds_read_b64_tr_b16 a[198:199], v241 offset:41984\n"
"v_lshlrev_b32 v253, 1, v253\n"
"v_mfma_f32_16x16x32_bf16 a[216:219], v[40:43], a[52:55], a[216:219]\n"
"v_xor_b32 v252, v252, v253\n"
"v_lshl_add_u32 v251, v252, 4, v251\n"
"v_mfma_f32_16x16x32_bf16 a[224:227], v[32:35], a[8:11], a[224:227]\n"
"s_and_b32 s82, s76, 1\n"
"buffer_load_dwordx4 v244, s[40:43], s90 offen offset:64 lds\n"
"v_mfma_f32_16x16x32_bf16 a[224:227], v[40:43], a[56:59], a[224:227]\n"
"s_addk_i32 m0, 4032\n"
"ds_read_b64_tr_b16 a[204:205], v241 offset:49152\n"
"v_mfma_f32_16x16x32_bf16 a[228:231], v[36:39], a[8:11], a[228:231]\n"
"s_mul_i32 s82, s82, 0x4800\n"
"ds_read_b64_tr_b16 a[206:207], v241 offset:50176\n"
"v_mfma_f32_16x16x32_bf16 a[228:231], v[44:47], a[56:59], a[228:231]\n"
"s_lshr_b32 s83, s76, 1\n"
"ds_read_b128 a[96:99], v236 offset:16384\n"
"v_mfma_f32_16x16x32_bf16 a[236:239], v[36:39], a[12:15], a[236:239]\n"
"s_lshl_b32 s83, s83, 6\n"
"ds_read_b128 a[144:147], v236 offset:17408\n"
"v_mfma_f32_16x16x32_bf16 a[236:239], v[44:47], a[60:63], a[236:239]\n"
"s_add_u32 s82, s82, s83\n"
"ds_read_b128 a[100:103], v236 offset:18432\n"
"v_add_u32 v230, s82, v251\n"
"v_mfma_f32_16x16x32_bf16 a[232:235], v[32:35], a[12:15], a[232:235]\n"
"buffer_load_dwordx4 v244, s[40:43], s90 offen offset:128 lds\n"
"v_lshrrev_b32 v251, 2, v224\n"
"v_mfma_f32_16x16x32_bf16 a[232:235], v[40:43], a[60:63], a[232:235]\n"
"s_addk_i32 m0, 4032\n"
"ds_read_b128 a[148:151], v236 offset:19456\n"
"v_mul_u32_u24 v251, 0x1000, v251\n"
"v_mfma_f32_16x16x32_bf16 a[240:243], v[32:35], a[16:19], a[240:243]\n"
"ds_read_b128 a[104:107], v236 offset:20480\n"
"v_and_b32 v252, 3, v224\n"
"v_mfma_f32_16x16x32_bf16 a[240:243], v[40:43], a[64:67], a[240:243]\n"
"ds_read_b128 a[152:155], v236 offset:21504\n"
"v_bfe_u32 v253, v224, 4, 1\n"
"v_mfma_f32_16x16x32_bf16 a[244:247], v[36:39], a[16:19], a[244:247]\n"
"ds_read_b128 a[108:111], v236 offset:22528\n"
"v_lshlrev_b32 v253, 1, v253\n"
"v_mfma_f32_16x16x32_bf16 a[244:247], v[44:47], a[64:67], a[244:247]\n"
"v_xor_b32 v252, v252, v253\n"
"v_lshl_add_u32 v251, v252, 4, v251\n"
"v_mfma_f32_16x16x32_bf16 a[252:255], v[36:39], a[20:23], a[252:255]\n"
"s_lshr_b32 s82, s76, 1\n"
"buffer_load_dwordx4 v244, s[40:43], s90 offen offset:192 lds\n"
"v_mfma_f32_16x16x32_bf16 a[252:255], v[44:47], a[68:71], a[252:255]\n"
"s_addk_i32 m0, 4288\n"
"ds_read_b128 a[156:159], v236 offset:23552\n"
"v_mfma_f32_16x16x32_bf16 a[248:251], v[32:35], a[20:23], a[248:251]\n"
"s_mul_i32 s82, s82, 0x10000\n"
"ds_read_b128 a[112:115], v236 offset:24576\n"
"v_mfma_f32_16x16x32_bf16 a[248:251], v[40:43], a[68:71], a[248:251]\n"
"s_and_b32 s83, s76, 1\n"
"ds_read_b128 a[160:163], v236 offset:25600\n"
"v_mfma_f32_16x16x32_bf16 v[64:67], v[32:35], a[24:27], v[64:67]\n"
"s_lshl_b32 s83, s83, 7\n"
"ds_read_b128 a[116:119], v236 offset:26624\n"
"v_mfma_f32_16x16x32_bf16 v[64:67], v[40:43], a[72:75], v[64:67]\n"
"s_add_u32 s82, s82, s83\n"
"ds_read_b128 a[164:167], v236 offset:27648\n"
"v_add_u32 v225, s82, v251\n"
"v_mfma_f32_16x16x32_bf16 v[68:71], v[36:39], a[24:27], v[68:71]\n"
"s_lshl_b32 s83, s80, 2\n"
"buffer_load_dwordx4 v245, s[44:47], s90 offen lds\n"
"v_mfma_f32_16x16x32_bf16 v[68:71], v[44:47], a[72:75], v[68:71]\n"
"s_addk_i32 m0, 4032\n"
"ds_read_b128 a[120:123], v236 offset:28672\n"
"v_mfma_f32_16x16x32_bf16 v[76:79], v[36:39], a[28:31], v[76:79]\n"
"s_add_u32 s83, s83, s76\n"
"ds_read_b128 a[168:171], v236 offset:29696\n"
"v_mfma_f32_16x16x32_bf16 v[76:79], v[44:47], a[76:79], v[76:79]\n"
"s_mul_i32 s81, s83, 12\n"
"ds_read_b128 a[124:127], v236 offset:30720\n"
"v_add_u32 v251, s81, v224\n"
"v_lshrrev_b32 v252, 4, v251\n"
"v_mfma_f32_16x16x32_bf16 v[72:75], v[32:35], a[28:31], v[72:75]\n"
"v_lshlrev_b32 v252, 12, v252\n"
"v_and_b32 v251, 15, v251\n"
"v_mfma_f32_16x16x32_bf16 v[72:75], v[40:43], a[76:79], v[72:75]\n"
"ds_read_b128 a[172:175], v236 offset:31744\n"
"v_lshl_add_u32 v219, v251, 7, v252\n"
"v_mfma_f32_16x16x32_bf16 v[80:83], v[32:35], a[32:35], v[80:83]\n"
"buffer_load_dwordx4 v245, s[44:47], s90 offen offset:64 lds\n"
"v_mov_b32 v252, 0x7ffffff0\n"
"v_mfma_f32_16x16x32_bf16 v[80:83], v[40:43], a[80:83], v[80:83]\n"
"s_addk_i32 m0, 4032\n"
"ds_read_b128 a[128:131], v236 offset:32768\n"
"v_mfma_f32_16x16x32_bf16 v[84:87], v[36:39], a[32:35], v[84:87]\n"
"s_mul_i32 s81, s83, 0x2880\n"
"ds_read_b128 a[176:179], v236 offset:33792\n"
"v_lshl_add_u32 v222, v224, 7, s81\n"
"v_mfma_f32_16x16x32_bf16 v[84:87], v[44:47], a[80:83], v[84:87]\n"
"ds_read_b128 a[132:135], v236 offset:34816\n"
"v_cmp_gt_u32 vcc, 12, v224\n"
"s_nop 1\n"
"v_cndmask_b32 v219, v252, v219, vcc\n"
"v_mfma_f32_16x16x32_bf16 v[92:95], v[36:39], a[36:39], v[92:95]\n"
"ds_read_b128 a[180:183], v236 offset:35840\n"
"v_cmp_gt_u32 vcc, 41, v224\n"
"s_nop 1\n"
"v_cndmask_b32 v222, v252, v222, vcc\n"
"v_mfma_f32_16x16x32_bf16 v[92:95], v[44:47], a[84:87], v[92:95]\n"
"s_and_b32 s81, s76, 1\n"
"ds_read_b128 a[136:139], v236 offset:36864\n"
"v_mfma_f32_16x16x32_bf16 v[88:91], v[32:35], a[36:39], v[88:91]\n"
"s_lshl_b32 s81, s81, 5\n"
"buffer_load_dwordx4 v245, s[44:47], s90 offen offset:128 lds\n"
"v_xor_b32 v251, s81, v247\n"
"v_mfma_f32_16x16x32_bf16 v[88:91], v[40:43], a[84:87], v[88:91]\n"
"s_addk_i32 m0, 4032\n"
"ds_read_b128 a[184:187], v236 offset:37888\n"
"v_mfma_f32_16x16x32_bf16 v[96:99], v[32:35], a[40:43], v[96:99]\n"
"s_lshr_b32 s82, s76, 1\n"
"ds_read_b128 a[140:143], v236 offset:38912\n"
"v_mfma_f32_16x16x32_bf16 v[96:99], v[40:43], a[88:91], v[96:99]\n"
"s_lshl_b32 s82, s82, 11\n"
"ds_read_b128 a[188:191], v236 offset:39936\n"
"v_mfma_f32_16x16x32_bf16 v[100:103], v[36:39], a[40:43], v[100:103]\n"
"s_add_u32 s82, s82, 0xffff5000\n"
"v_add_u32 v216, s82, v251\n"
"s_cmp_lt_u32 s76, 2\n"
"s_cselect_b32 s82, 0, 0x7ffffff0\n"
"v_mfma_f32_16x16x32_bf16 v[100:103], v[44:47], a[88:91], v[100:103]\n"
"v_or_b32 v217, s82, v235\n"
"v_mfma_f32_16x16x32_bf16 v[108:111], v[36:39], a[44:47], v[108:111]\n"
"s_cmp_eq_u32 s7, 1\n"
"s_cselect_b32 s82, 0, 0x7ffffff0\n"
"v_or_b32 v220, s82, v235\n"
"v_mfma_f32_16x16x32_bf16 v[108:111], v[44:47], a[92:95], v[108:111]\n"
"s_lshl_b32 s11, s79, 3\n"
"buffer_load_dwordx4 v245, s[44:47], s90 offen offset:192 lds\n"
"v_mfma_f32_16x16x32_bf16 v[104:107], v[32:35], a[44:47], v[104:107]\n"
"s_lshr_b32 s81, s80, 2\n"
"v_mfma_f32_16x16x32_bf16 v[104:107], v[40:43], a[92:95], v[104:107]\n"
"s_waitcnt vmcnt(8) lgkmcnt(0)\n"
"s_barrier\n"
"v_mfma_f32_16x16x32_bf16 a[208:211], a[192:195], a[96:99], a[208:211]\n"
"s_add_u32 s11, s11, s81\n"
"ds_read_b64_tr_b16 v[32:33], v238 offset:24576\n"
"v_mfma_f32_16x16x32_bf16 a[208:211], a[200:203], a[144:147], a[208:211]\n"
"s_and_b32 s12, s80, 3\n"
"ds_read_b64_tr_b16 v[34:35], v238 offset:25600\n"
"v_mfma_f32_16x16x32_bf16 a[212:215], a[196:199], a[96:99], a[212:215]\n"
"s_lshl_b32 s81, s11, 3\n"
"ds_read_b64_tr_b16 v[40:41], v238 offset:32768\n"
"v_mfma_f32_16x16x32_bf16 a[212:215], a[204:207], a[144:147], a[212:215]\n"
"s_lshl_b32 s82, s76, 1\n"
"v_mfma_f32_16x16x32_bf16 a[220:223], a[196:199], a[100:103], a[220:223]\n"
"s_add_u32 s81, s81, s82\n"
"ds_read_b64_tr_b16 v[42:43], v238 offset:33792\n"
"v_mfma_f32_16x16x32_bf16 a[220:223], a[204:207], a[148:151], a[220:223]\n"
"s_lshl_b32 s81, s81, 17\n"
"ds_read_b64_tr_b16 v[36:37], v239 offset:24576\n"
"v_mfma_f32_16x16x32_bf16 a[216:219], a[192:195], a[100:103], a[216:219]\n"
"s_lshl_b32 s82, s78, 15\n"
"ds_read_b64_tr_b16 v[38:39], v239 offset:25600\n"
"v_mfma_f32_16x16x32_bf16 a[216:219], a[200:203], a[148:151], a[216:219]\n"
"s_add_u32 s81, s81, s82\n"
"v_mfma_f32_16x16x32_bf16 a[224:227], a[192:195], a[104:107], a[224:227]\n"
"s_add_u32 s81, s81, 0xc28000\n"
"ds_read_b64_tr_b16 v[44:45], v239 offset:32768\n"
"v_add_u32 v226, s81, v235\n"
"v_add_u32 v227, 0x20000, v226\n"
"v_mfma_f32_16x16x32_bf16 a[224:227], a[200:203], a[152:155], a[224:227]\n"
"s_lshl_b32 s81, s11, 2\n"
"ds_read_b64_tr_b16 v[46:47], v239 offset:33792\n"
"v_mfma_f32_16x16x32_bf16 a[228:231], a[196:199], a[104:107], a[228:231]\n"
"s_add_u32 s81, s81, s12\n"
"ds_read_b128 a[0:3], v235\n"
"v_mfma_f32_16x16x32_bf16 a[228:231], a[204:207], a[152:155], a[228:231]\n"
"s_lshl_b32 s81, s81, 2\n"
"v_mfma_f32_16x16x32_bf16 a[236:239], a[196:199], a[108:111], a[236:239]\n"
"s_add_u32 s82, s81, s76\n"
"ds_read_b128 a[48:51], v235 offset:1024\n"
"v_mfma_f32_16x16x32_bf16 a[236:239], a[204:207], a[156:159], a[236:239]\n"
"s_lshl_b32 s82, s82, 2\n"
"ds_read_b128 a[4:7], v235 offset:2048\n"
"v_mfma_f32_16x16x32_bf16 a[232:235], a[192:195], a[108:111], a[232:235]\n"
"s_add_u32 s82, s82, s78\n"
"ds_read_b128 a[52:55], v235 offset:3072\n"
"v_mfma_f32_16x16x32_bf16 a[232:235], a[200:203], a[156:159], a[232:235]\n"
"s_mul_i32 s2, s82, 0x2400\n"
"v_mfma_f32_16x16x32_bf16 a[240:243], a[192:195], a[112:115], a[240:243]\n"
"s_add_u32 s82, s81, s78\n"
"ds_read_b128 a[8:11], v235 offset:4096\n"
"v_mfma_f32_16x16x32_bf16 a[240:243], a[200:203], a[160:163], a[240:243]\n"
"s_mul_i32 s82, s82, 0x9000\n"
"ds_read_b128 a[56:59], v235 offset:5120\n"
"v_mfma_f32_16x16x32_bf16 a[244:247], a[196:199], a[112:115], a[244:247]\n"
"s_add_u32 s83, s78, 1\n"
"ds_read_b128 a[12:15], v235 offset:6144\n"
"v_mfma_f32_16x16x32_bf16 a[244:247], a[204:207], a[160:163], a[244:247]\n"
"s_and_b32 s83, s83, 3\n"
"v_mfma_f32_16x16x32_bf16 a[252:255], a[196:199], a[116:119], a[252:255]\n"
"s_mul_i32 s83, s83, 0x2400\n"
"ds_read_b128 a[60:63], v235 offset:7168\n"
"v_mfma_f32_16x16x32_bf16 a[252:255], a[204:207], a[164:167], a[252:255]\n"
"s_add_u32 s3, s82, s83\n"
"ds_read_b128 a[16:19], v235 offset:8192\n"
"v_mfma_f32_16x16x32_bf16 a[248:251], a[192:195], a[116:119], a[248:251]\n"
"s_add_u32 s83, s78, 2\n"
"ds_read_b128 a[64:67], v235 offset:9216\n"
"v_mfma_f32_16x16x32_bf16 a[248:251], a[200:203], a[164:167], a[248:251]\n"
"s_and_b32 s83, s83, 3\n"
"v_mfma_f32_16x16x32_bf16 v[64:67], a[192:195], a[120:123], v[64:67]\n"
"s_mul_i32 s83, s83, 0x2400\n"
"ds_read_b128 a[20:23], v235 offset:10240\n"
"v_mfma_f32_16x16x32_bf16 v[64:67], a[200:203], a[168:171], v[64:67]\n"
"s_add_u32 s17, s82, s83\n"
"ds_read_b128 a[68:71], v235 offset:11264\n"
"v_mfma_f32_16x16x32_bf16 v[68:71], a[196:199], a[120:123], v[68:71]\n"
"s_add_u32 s83, s78, 3\n"
"v_mfma_f32_16x16x32_bf16 v[68:71], a[204:207], a[168:171], v[68:71]\n"
"s_and_b32 s83, s83, 3\n"
"ds_read_b128 a[24:27], v235 offset:12288\n"
"v_mfma_f32_16x16x32_bf16 v[76:79], a[196:199], a[124:127], v[76:79]\n"
"s_mul_i32 s83, s83, 0x2400\n"
"ds_read_b128 a[72:75], v235 offset:13312\n"
"v_mfma_f32_16x16x32_bf16 v[76:79], a[204:207], a[172:175], v[76:79]\n"
"s_add_u32 s15, s82, s83\n"
"ds_read_b128 a[28:31], v235 offset:14336\n"
"s_cmp_eq_u32 s76, s78\n"
"s_cselect_b32 s82, 0x7ffffff0, 0\n"
"v_mfma_f32_16x16x32_bf16 v[72:75], a[192:195], a[124:127], v[72:75]\n"
"v_or_b32 v218, s82, v235\n"
"v_mfma_f32_16x16x32_bf16 v[72:75], a[200:203], a[172:175], v[72:75]\n"
"s_mul_i32 s82, s12, 0x4800\n"
"ds_read_b128 a[76:79], v235 offset:15360\n"
"v_mfma_f32_16x16x32_bf16 v[80:83], a[192:195], a[128:131], v[80:83]\n"
"s_mul_i32 s81, s78, 0x144000\n"
"ds_read_b128 a[32:35], v235 offset:16384\n"
"v_mfma_f32_16x16x32_bf16 v[80:83], a[200:203], a[176:179], v[80:83]\n"
"s_add_u32 s81, s81, s82\n"
"ds_read_b128 a[80:83], v235 offset:17408\n"
"v_mfma_f32_16x16x32_bf16 v[84:87], a[196:199], a[128:131], v[84:87]\n"
"s_add_u32 s4, s81, 0x718000\n"
"v_mfma_f32_16x16x32_bf16 v[84:87], a[204:207], a[176:179], v[84:87]\n"
"s_mul_i32 s81, s78, 0x120000\n"
"ds_read_b128 a[36:39], v235 offset:18432\n"
"v_mfma_f32_16x16x32_bf16 v[92:95], a[196:199], a[132:135], v[92:95]\n"
"s_add_u32 s81, s81, s82\n"
"ds_read_b128 a[84:87], v235 offset:19456\n"
"v_mfma_f32_16x16x32_bf16 v[92:95], a[204:207], a[180:183], v[92:95]\n"
"s_add_u32 s5, s81, 0x298000\n"
"ds_read_b128 a[40:43], v235 offset:20480\n"
"v_mfma_f32_16x16x32_bf16 v[88:91], a[192:195], a[132:135], v[88:91]\n"
"s_lshl_b32 s81, s11, 7\n"
"v_mfma_f32_16x16x32_bf16 v[88:91], a[200:203], a[180:183], v[88:91]\n"
"s_lshl_b32 s82, s78, 5\n"
"ds_read_b128 a[88:91], v235 offset:21504\n"
"v_mfma_f32_16x16x32_bf16 v[96:99], a[192:195], a[136:139], v[96:99]\n"
"s_add_u32 s81, s81, s82\n"
"ds_read_b128 a[44:47], v235 offset:22528\n"
"v_mfma_f32_16x16x32_bf16 v[96:99], a[200:203], a[184:187], v[96:99]\n"
"s_mul_i32 s81, s81, 0x480\n"
"ds_read_b128 a[92:95], v235 offset:23552\n"
"v_and_b32 v251, 31, v224\n"
"v_mul_u32_u24 v251, 0x480, v251\n"
"v_mfma_f32_16x16x32_bf16 v[100:103], a[196:199], a[136:139], v[100:103]\n"
"v_lshrrev_b32 v252, 5, v224\n"
"v_lshl_add_u32 v251, v252, 4, v251\n"
"v_mfma_f32_16x16x32_bf16 v[100:103], a[204:207], a[184:187], v[100:103]\n"
"s_mul_i32 s82, s12, 0x120\n"
"v_mfma_f32_16x16x32_bf16 v[108:111], a[196:199], a[140:143], v[108:111]\n"
"s_lshl_b32 s83, s76, 5\n"
"v_mfma_f32_16x16x32_bf16 v[108:111], a[204:207], a[188:191], v[108:111]\n"
"s_add_u32 s82, s82, s83\n"
"v_mfma_f32_16x16x32_bf16 v[104:107], a[192:195], a[140:143], v[104:107]\n"
"s_add_u32 s82, s82, s81\n"
"v_add_u32 v228, s82, v251\n"
"v_and_b32 v251, 15, v224\n"
"v_mul_u32_u24 v251, 0x480, v251\n"
"v_mfma_f32_16x16x32_bf16 v[104:107], a[200:203], a[188:191], v[104:107]\n"
"s_waitcnt vmcnt(4) lgkmcnt(0)\n"
"s_barrier\n"
"v_lshrrev_b32 v252, 4, v224\n"
"v_lshl_add_u32 v251, v252, 3, v251\n"
"v_mfma_f32_16x16x32_bf16 v[64:67], v[32:35], a[24:27], v[64:67]\n"
"s_mul_i32 s82, s12, 0x120\n"
"ds_read_b128 a[144:147], v246 offset:32768\n"
"v_mfma_f32_16x16x32_bf16 v[64:67], v[40:43], a[72:75], v[64:67]\n"
"s_add_u32 s81, s81, s82\n"
"v_mfma_f32_16x16x32_bf16 v[68:71], v[36:39], a[24:27], v[68:71]\n"
"s_lshr_b32 s82, s76, 1\n"
"ds_read_b128 a[148:151], v246 offset:36864\n"
"v_mfma_f32_16x16x32_bf16 v[68:71], v[44:47], a[72:75], v[68:71]\n"
"s_mul_i32 s82, s82, 0x4800\n"
"v_mfma_f32_16x16x32_bf16 v[76:79], v[36:39], a[28:31], v[76:79]\n"
"s_add_u32 s81, s81, s82\n"
"ds_read_b128 a[152:155], v246 offset:40960\n"
"v_mfma_f32_16x16x32_bf16 v[76:79], v[44:47], a[76:79], v[76:79]\n"
"s_and_b32 s82, s76, 1\n"
"s_lshl_b32 s82, s82, 2\n"
"s_add_u32 s81, s81, s82\n"
"s_add_u32 s81, s81, 0x100\n"
"v_mfma_f32_16x16x32_bf16 v[72:75], v[32:35], a[28:31], v[72:75]\n"
"v_add_u32 v229, s81, v251\n"
"v_mfma_f32_16x16x32_bf16 v[72:75], v[40:43], a[76:79], v[72:75]\n"
"ds_read_b128 a[156:159], v246 offset:45056\n"
"v_mfma_f32_16x16x32_bf16 v[80:83], v[32:35], a[32:35], v[80:83]\n"
"v_mfma_f32_16x16x32_bf16 v[80:83], v[40:43], a[80:83], v[80:83]\n"
"s_add_u32 s99, s21, 0x2e2c000\n"
"ds_read_b128 a[160:163], v246 offset:33792\n"
"v_mfma_f32_16x16x32_bf16 v[84:87], v[36:39], a[32:35], v[84:87]\n"
"v_cvt_pk_bf16_f32 v192, v64, v65\n"
"v_mfma_f32_16x16x32_bf16 v[84:87], v[44:47], a[80:83], v[84:87]\n"
"v_cvt_pk_bf16_f32 v193, v66, v67\n"
"v_mfma_f32_16x16x32_bf16 v[92:95], v[36:39], a[36:39], v[92:95]\n"
"ds_read_b128 a[164:167], v246 offset:37888\n"
"v_cvt_pk_bf16_f32 v194, v68, v69\n"
"v_mfma_f32_16x16x32_bf16 v[92:95], v[44:47], a[84:87], v[92:95]\n"
"v_cvt_pk_bf16_f32 v195, v70, v71\n"
"v_mfma_f32_16x16x32_bf16 v[88:91], v[32:35], a[36:39], v[88:91]\n"
"ds_read_b128 a[168:171], v246 offset:41984\n"
"v_mfma_f32_16x16x32_bf16 v[88:91], v[40:43], a[84:87], v[88:91]\n"
"buffer_store_dwordx4 v[192:195], v220, s[56:59], s99 offen sc1\n"
"v_mfma_f32_16x16x32_bf16 v[96:99], v[32:35], a[40:43], v[96:99]\n"
"ds_read_b128 a[172:175], v246 offset:46080\n"
"v_cvt_pk_bf16_f32 v196, v72, v73\n"
"v_mfma_f32_16x16x32_bf16 v[96:99], v[40:43], a[88:91], v[96:99]\n"
"v_cvt_pk_bf16_f32 v197, v74, v75\n"
"v_mfma_f32_16x16x32_bf16 v[100:103], v[36:39], a[40:43], v[100:103]\n"
"v_cvt_pk_bf16_f32 v198, v76, v77\n"
"v_mfma_f32_16x16x32_bf16 v[100:103], v[44:47], a[88:91], v[100:103]\n"
"v_cvt_pk_bf16_f32 v199, v78, v79\n"
"v_mfma_f32_16x16x32_bf16 v[108:111], v[36:39], a[44:47], v[108:111]\n"
"buffer_store_dwordx4 v[196:199], v220, s[56:59], s99 offen offset:1024 sc1\n"
"v_mfma_f32_16x16x32_bf16 v[108:111], v[44:47], a[92:95], v[108:111]\n"
"v_mfma_f32_16x16x32_bf16 v[104:107], v[32:35], a[44:47], v[104:107]\n"
"v_cvt_pk_bf16_f32 v200, v80, v81\n"
"v_mfma_f32_16x16x32_bf16 v[104:107], v[40:43], a[92:95], v[104:107]\n"
"v_cvt_pk_bf16_f32 v201, v82, v83\n"
"v_mfma_f32_16x16x32_bf16 a[208:211], v[32:35], a[0:3], a[208:211]\n"
"v_cvt_pk_bf16_f32 v202, v84, v85\n"
"v_mfma_f32_16x16x32_bf16 a[208:211], v[40:43], a[48:51], a[208:211]\n"
"v_cvt_pk_bf16_f32 v203, v86, v87\n"
"v_mfma_f32_16x16x32_bf16 a[212:215], v[36:39], a[0:3], a[212:215]\n"
"buffer_store_dwordx4 v[200:203], v220, s[56:59], s99 offen offset:2048 sc1\n"
"v_mfma_f32_16x16x32_bf16 a[212:215], v[44:47], a[48:51], a[212:215]\n"
"v_mfma_f32_16x16x32_bf16 a[220:223], v[36:39], a[4:7], a[220:223]\n"
"v_cvt_pk_bf16_f32 v192, v88, v89\n"
"v_mfma_f32_16x16x32_bf16 a[220:223], v[44:47], a[52:55], a[220:223]\n"
"v_cvt_pk_bf16_f32 v193, v90, v91\n"
"v_mfma_f32_16x16x32_bf16 a[216:219], v[32:35], a[4:7], a[216:219]\n"
"v_cvt_pk_bf16_f32 v194, v92, v93\n"
"v_mfma_f32_16x16x32_bf16 a[216:219], v[40:43], a[52:55], a[216:219]\n"
"v_cvt_pk_bf16_f32 v195, v94, v95\n"
"v_mfma_f32_16x16x32_bf16 a[224:227], v[32:35], a[8:11], a[224:227]\n"
"buffer_store_dwordx4 v[192:195], v220, s[56:59], s99 offen offset:3072 sc1\n"
"v_mfma_f32_16x16x32_bf16 a[224:227], v[40:43], a[56:59], a[224:227]\n"
"v_cvt_pk_bf16_f32 v196, v96, v97\n"
"v_mfma_f32_16x16x32_bf16 a[228:231], v[36:39], a[8:11], a[228:231]\n"
"v_mfma_f32_16x16x32_bf16 a[228:231], v[44:47], a[56:59], a[228:231]\n"
"v_cvt_pk_bf16_f32 v197, v98, v99\n"
"v_mfma_f32_16x16x32_bf16 a[236:239], v[36:39], a[12:15], a[236:239]\n"
"v_cvt_pk_bf16_f32 v198, v100, v101\n"
"v_mfma_f32_16x16x32_bf16 a[236:239], v[44:47], a[60:63], a[236:239]\n"
"v_cvt_pk_bf16_f32 v199, v102, v103\n"
"v_mfma_f32_16x16x32_bf16 a[232:235], v[32:35], a[12:15], a[232:235]\n"
"s_add_u32 s99, s99, 0x1000\n"
"v_mfma_f32_16x16x32_bf16 a[232:235], v[40:43], a[60:63], a[232:235]\n"
"buffer_store_dwordx4 v[196:199], v220, s[56:59], s99 offen sc1\n"
"v_mfma_f32_16x16x32_bf16 a[240:243], v[32:35], a[16:19], a[240:243]\n"
"v_mfma_f32_16x16x32_bf16 a[240:243], v[40:43], a[64:67], a[240:243]\n"
"v_cvt_pk_bf16_f32 v200, v104, v105\n"
"v_mfma_f32_16x16x32_bf16 a[244:247], v[36:39], a[16:19], a[244:247]\n"
"v_cvt_pk_bf16_f32 v201, v106, v107\n"
"v_mfma_f32_16x16x32_bf16 a[244:247], v[44:47], a[64:67], a[244:247]\n"
"v_cvt_pk_bf16_f32 v202, v108, v109\n"
"v_mfma_f32_16x16x32_bf16 a[252:255], v[36:39], a[20:23], a[252:255]\n"
"v_cvt_pk_bf16_f32 v203, v110, v111\n"
"v_mfma_f32_16x16x32_bf16 a[252:255], v[44:47], a[68:71], a[252:255]\n"
"buffer_store_dwordx4 v[200:203], v220, s[56:59], s99 offen offset:1024 sc1\n"
"v_mfma_f32_16x16x32_bf16 a[248:251], v[32:35], a[20:23], a[248:251]\n"
"v_mfma_f32_16x16x32_bf16 a[248:251], v[40:43], a[68:71], a[248:251]\n"
"L_qk0:\n"
"s_waitcnt vmcnt(6) lgkmcnt(0)\n"
"s_barrier\n"
"v_mfma_f32_16x16x32_bf16 v[0:3], a[144:147], v[128:131], 0\n"
"s_mov_b32 s90, 0x4000\n"
"s_add_u32 m0, s86, 0x9000\n"
"ds_read_b128 a[176:179], v246 offset:34816\n"
"v_mfma_f32_16x16x32_bf16 v[0:3], a[148:151], v[132:135], v[0:3]\n"
"buffer_load_dwordx4 v244, s[40:43], s90 offen lds\n"
"v_mfma_f32_16x16x32_bf16 v[0:3], a[152:155], v[136:139], v[0:3]\n"
"s_addk_i32 m0, 4032\n"
"ds_read_b128 a[180:183], v246 offset:38912\n"
"v_subrev_u32 v231, 0xf000, v246\n"
"v_mfma_f32_16x16x32_bf16 v[0:3], a[156:159], v[140:143], v[0:3]\n"
"ds_read_b128 a[184:187], v246 offset:43008\n"
"v_subrev_u32 v232, 0xf000, v247\n"
"v_mfma_f32_16x16x32_bf16 v[4:7], a[160:163], v[128:131], 0\n"
"ds_read_b128 a[188:191], v246 offset:47104\n"
"v_mfma_f32_16x16x32_bf16 v[4:7], a[164:167], v[132:135], v[4:7]\n"
"buffer_load_dwordx4 v244, s[40:43], s90 offen offset:64 lds\n"
"v_mfma_f32_16x16x32_bf16 v[4:7], a[168:171], v[136:139], v[4:7]\n"
"s_addk_i32 m0, 4032\n"
"ds_read_b128 a[192:195], v246 offset:35840\n"
"v_subrev_u32 v252, 0xf000, v248\n"
"v_mfma_f32_16x16x32_bf16 v[4:7], a[172:175], v[140:143], v[4:7]\n"
"ds_read_b128 a[196:199], v246 offset:39936\n"
"v_mfma_f32_16x16x32_bf16 v[8:11], a[144:147], v[144:147], 0\n"
"ds_read_b128 a[200:203], v246 offset:44032\n"
"v_mfma_f32_16x16x32_bf16 v[8:11], a[148:151], v[148:151], v[8:11]\n"
"buffer_load_dwordx4 v244, s[40:43], s90 offen offset:128 lds\n"
"v_mfma_f32_16x16x32_bf16 v[8:11], a[152:155], v[152:155], v[8:11]\n"
"s_addk_i32 m0, 4032\n"
"ds_read_b128 a[204:207], v246 offset:48128\n"
"v_mfma_f32_16x16x32_bf16 v[8:11], a[156:159], v[156:159], v[8:11]\n"
"v_mfma_f32_16x16x32_bf16 v[12:15], a[160:163], v[144:147], 0\n"
"v_max3_f32 v212, v0, v1, v2\n"
"v_max3_f32 v208, v3, v4, v5\n"
"v_mfma_f32_16x16x32_bf16 v[12:15], a[164:167], v[148:151], v[12:15]\n"
"buffer_load_dwordx4 v244, s[40:43], s90 offen offset:192 lds\n"
"v_max3_f32 v212, v212, v6, v7\n"
"v_mfma_f32_16x16x32_bf16 v[12:15], a[168:171], v[152:155], v[12:15]\n"
"s_addk_i32 m0, 4288\n"
"v_max_f32 v208, v208, v212\n"
"v_mov_b32 v212, v208\n"
"v_mfma_f32_16x16x32_bf16 v[12:15], a[172:175], v[156:159], v[12:15]\n"
"s_nop 0\n"
"v_permlane32_swap_b32 v208, v212\n"
"v_max_f32 v208, v208, v212\n"
"v_mfma_f32_16x16x32_bf16 v[16:19], a[144:147], v[160:163], 0\n"
"v_mov_b32 v212, v208\n"
"s_nop 1\n"
"v_permlane16_swap_b32 v208, v212\n"
"v_mfma_f32_16x16x32_bf16 v[16:19], a[148:151], v[164:167], v[16:19]\n"
"buffer_load_dwordx4 v245, s[44:47], s90 offen lds\n"
"v_max_f32 v208, v208, v212\n"
"v_mfma_f32_16x16x32_bf16 v[16:19], a[152:155], v[168:171], v[16:19]\n"
"s_addk_i32 m0, 4032\n"
"v_sub_f32 v192, 0, v208\n"
"v_sub_f32 v193, 0, v208\n"
"v_sub_f32 v194, 0, v208\n"
"v_mfma_f32_16x16x32_bf16 v[16:19], a[156:159], v[172:175], v[16:19]\n"
"v_sub_f32 v195, 0, v208\n"
"v_add_f32 v0, v0, v192\n"
"v_mfma_f32_16x16x32_bf16 v[20:23], a[160:163], v[160:163], 0\n"
"v_add_f32 v1, v1, v192\n"
"v_add_f32 v2, v2, v192\n"
"v_mfma_f32_16x16x32_bf16 v[20:23], a[164:167], v[164:167], v[20:23]\n"
"buffer_load_dwordx4 v245, s[44:47], s90 offen offset:64 lds\n"
"v_add_f32 v3, v3, v192\n"
"v_mfma_f32_16x16x32_bf16 v[20:23], a[168:171], v[168:171], v[20:23]\n"
"s_addk_i32 m0, 4032\n"
"v_add_f32 v4, v4, v192\n"
"v_add_f32 v5, v5, v192\n"
"v_add_f32 v6, v6, v192\n"
"v_mfma_f32_16x16x32_bf16 v[20:23], a[172:175], v[172:175], v[20:23]\n"
"v_add_f32 v7, v7, v192\n"
"v_max3_f32 v213, v8, v9, v10\n"
"v_max3_f32 v209, v11, v12, v13\n"
"v_mfma_f32_16x16x32_bf16 v[24:27], a[144:147], v[176:179], 0\n"
"v_max3_f32 v213, v213, v14, v15\n"
"v_max_f32 v209, v209, v213\n"
"v_mov_b32 v213, v209\n"
"v_mfma_f32_16x16x32_bf16 v[24:27], a[148:151], v[180:183], v[24:27]\n"
"buffer_load_dwordx4 v245, s[44:47], s90 offen offset:128 lds\n"
"v_mfma_f32_16x16x32_bf16 v[24:27], a[152:155], v[184:187], v[24:27]\n"
"s_addk_i32 m0, 4032\n"
"v_permlane32_swap_b32 v209, v213\n"
"v_max_f32 v209, v209, v213\n"
"v_mfma_f32_16x16x32_bf16 v[24:27], a[156:159], v[188:191], v[24:27]\n"
"v_mov_b32 v213, v209\n"
"s_nop 1\n"
"v_permlane16_swap_b32 v209, v213\n"
"v_mfma_f32_16x16x32_bf16 v[28:31], a[160:163], v[176:179], 0\n"
"v_max_f32 v209, v209, v213\n"
"v_sub_f32 v196, 0, v209\n"
"v_mfma_f32_16x16x32_bf16 v[28:31], a[164:167], v[180:183], v[28:31]\n"
"buffer_load_dwordx4 v245, s[44:47], s90 offen offset:192 lds\n"
"v_sub_f32 v197, 0, v209\n"
"v_mfma_f32_16x16x32_bf16 v[28:31], a[168:171], v[184:187], v[28:31]\n"
"v_sub_f32 v198, 0, v209\n"
"v_sub_f32 v199, 0, v209\n"
"v_mfma_f32_16x16x32_bf16 v[28:31], a[172:175], v[188:191], v[28:31]\n"
"v_pk_add_f32 v[8:9], v[8:9], v[196:197]\n"
"v_pk_add_f32 v[10:11], v[10:11], v[196:197]\n"
"v_pk_add_f32 v[12:13], v[12:13], v[196:197]\n"
"v_pk_add_f32 v[14:15], v[14:15], v[196:197]\n"
"v_max3_f32 v214, v16, v17, v18\n"
"v_max3_f32 v215, v24, v25, v26\n"
"v_max3_f32 v210, v19, v20, v21\n"
"s_nop 0\n"
"v_max3_f32 v211, v27, v28, v29\n"
"v_max3_f32 v214, v214, v22, v23\n"
"v_max3_f32 v215, v215, v30, v31\n"
"v_max_f32 v210, v210, v214\n"
"v_max_f32 v211, v211, v215\n"
"v_mov_b32 v214, v210\n"
"v_mov_b32 v215, v211\n"
"s_nop 0\n"
"v_permlane32_swap_b32 v210, v214\n"
"s_nop 0\n"
"v_permlane32_swap_b32 v211, v215\n"
"v_max_f32 v210, v210, v214\n"
"v_max_f32 v211, v211, v215\n"
"v_mov_b32 v214, v210\n"
"v_mov_b32 v215, v211\n"
"s_nop 0\n"
"v_permlane16_swap_b32 v210, v214\n"
"s_nop 0\n"
"v_permlane16_swap_b32 v211, v215\n"
"v_max_f32 v210, v210, v214\n"
"v_max_f32 v211, v211, v215\n"
"v_sub_f32 v200, 0, v210\n"
"v_sub_f32 v204, 0, v211\n"
"v_sub_f32 v201, 0, v210\n"
"v_sub_f32 v205, 0, v211\n"
"v_sub_f32 v202, 0, v210\n"
"v_sub_f32 v206, 0, v211\n"
"v_sub_f32 v203, 0, v210\n"
"v_sub_f32 v207, 0, v211\n"
"v_pk_add_f32 v[16:17], v[16:17], v[200:201]\n"
"v_pk_add_f32 v[18:19], v[18:19], v[200:201]\n"
"v_pk_add_f32 v[20:21], v[20:21], v[200:201]\n"
"v_pk_add_f32 v[22:23], v[22:23], v[200:201]\n"
"v_pk_add_f32 v[24:25], v[24:25], v[204:205]\n"
"v_pk_add_f32 v[26:27], v[26:27], v[204:205]\n"
"v_pk_add_f32 v[28:29], v[28:29], v[204:205]\n"
"v_pk_add_f32 v[30:31], v[30:31], v[204:205]\n"
"v_mov_b32 v212, s96\n"
"v_mov_b32 v213, s96\n"
"v_mov_b32 v214, s96\n"
"v_mov_b32 v215, s96\n"
"L_attn:\n"
"s_waitcnt lgkmcnt(0)\n"
"v_mfma_f32_16x16x32_bf16 v[32:35], a[176:179], v[128:131], v[192:195]\n"
"s_mov_b32 s90, 0x8000\n"
"ds_read_b64_tr_b16 v[96:97], v247 offset:34816\n"
"v_mfma_f32_16x16x32_bf16 v[32:35], a[180:183], v[132:135], v[32:35]\n"
"ds_read_b64_tr_b16 v[98:99], v247 offset:35840\n"
"v_mfma_f32_16x16x32_bf16 v[32:35], a[184:187], v[136:139], v[32:35]\n"
"ds_read_b64_tr_b16 v[100:101], v248 offset:34816\n"
"v_mfma_f32_16x16x32_bf16 v[32:35], a[188:191], v[140:143], v[32:35]\n"
"s_add_u32 m0, s86, 0x18000\n"
"ds_read_b64_tr_b16 v[102:103], v248 offset:35840\n"
"v_mfma_f32_16x16x32_bf16 v[36:39], a[192:195], v[128:131], v[192:195]\n"
"v_exp_f32 v0, v0\n"
"v_mfma_f32_16x16x32_bf16 v[36:39], a[196:199], v[132:135], v[36:39]\n"
"ds_read_b64_tr_b16 v[104:105], v247 offset:38912\n"
"v_mfma_f32_16x16x32_bf16 v[36:39], a[200:203], v[136:139], v[36:39]\n"
"ds_read_b64_tr_b16 v[106:107], v247 offset:39936\n"
"v_mfma_f32_16x16x32_bf16 v[36:39], a[204:207], v[140:143], v[36:39]\n"
"v_exp_f32 v1, v1\n"
"v_mfma_f32_16x16x32_bf16 v[40:43], a[176:179], v[144:147], v[196:199]\n"
"v_exp_f32 v2, v2\n"
"v_mfma_f32_16x16x32_bf16 v[40:43], a[180:183], v[148:151], v[40:43]\n"
"buffer_load_dwordx4 v244, s[40:43], s90 offen lds\n"
"v_mfma_f32_16x16x32_bf16 v[40:43], a[184:187], v[152:155], v[40:43]\n"
"s_addk_i32 m0, 4032\n"
"v_exp_f32 v3, v3\n"
"v_mfma_f32_16x16x32_bf16 v[40:43], a[188:191], v[156:159], v[40:43]\n"
"v_exp_f32 v4, v4\n"
"v_mfma_f32_16x16x32_bf16 v[44:47], a[192:195], v[144:147], v[196:199]\n"
"v_exp_f32 v5, v5\n"
"v_mfma_f32_16x16x32_bf16 v[44:47], a[196:199], v[148:151], v[44:47]\n"
"v_exp_f32 v6, v6\n"
"v_mfma_f32_16x16x32_bf16 v[44:47], a[200:203], v[152:155], v[44:47]\n"
"v_exp_f32 v7, v7\n"
"v_mfma_f32_16x16x32_bf16 v[44:47], a[204:207], v[156:159], v[44:47]\n"
"v_exp_f32 v8, v8\n"
"v_mfma_f32_16x16x32_bf16 v[48:51], a[176:179], v[160:163], v[200:203]\n"
"buffer_load_dwordx4 v244, s[40:43], s90 offen offset:64 lds\n"
"v_mfma_f32_16x16x32_bf16 v[48:51], a[180:183], v[164:167], v[48:51]\n"
"s_addk_i32 m0, 4032\n"
"v_exp_f32 v9, v9\n"
"v_mfma_f32_16x16x32_bf16 v[48:51], a[184:187], v[168:171], v[48:51]\n"
"ds_read_b64_tr_b16 v[108:109], v248 offset:38912\n"
"v_cvt_pk_bf16_f32 v64, v0, v1\n"
"v_mfma_f32_16x16x32_bf16 v[48:51], a[188:191], v[172:175], v[48:51]\n"
"v_exp_f32 v10, v10\n"
"v_mfma_f32_16x16x32_bf16 v[52:55], a[192:195], v[160:163], v[200:203]\n"
"ds_read_b64_tr_b16 v[110:111], v248 offset:39936\n"
"v_cvt_pk_bf16_f32 v65, v2, v3\n"
"v_mfma_f32_16x16x32_bf16 v[52:55], a[196:199], v[164:167], v[52:55]\n"
"v_exp_f32 v11, v11\n"
"v_mfma_f32_16x16x32_bf16 v[52:55], a[200:203], v[168:171], v[52:55]\n"
"ds_read_b64_tr_b16 v[112:113], v247 offset:43008\n"
"v_cvt_pk_bf16_f32 v66, v4, v5\n"
"v_mfma_f32_16x16x32_bf16 v[52:55], a[204:207], v[172:175], v[52:55]\n"
"buffer_load_dwordx4 v244, s[40:43], s90 offen offset:128 lds\n"
"v_mfma_f32_16x16x32_bf16 v[56:59], a[176:179], v[176:179], v[204:207]\n"
"s_addk_i32 m0, 4032\n"
"ds_read_b64_tr_b16 v[114:115], v247 offset:44032\n"
"ds_read_b64_tr_b16 v[116:117], v248 offset:43008\n"
"ds_read_b64_tr_b16 v[118:119], v248 offset:44032\n"
"ds_read_b64_tr_b16 v[120:121], v247 offset:47104\n"
"ds_read_b64_tr_b16 v[122:123], v247 offset:48128\n"
"ds_read_b64_tr_b16 v[124:125], v248 offset:47104\n"
"ds_read_b64_tr_b16 v[126:127], v248 offset:48128\n"
"v_mfma_f32_16x16x32_bf16 v[56:59], a[180:183], v[180:183], v[56:59]\n"
"v_exp_f32 v12, v12\n"
"v_mfma_f32_16x16x32_bf16 v[56:59], a[184:187], v[184:187], v[56:59]\n"
"v_exp_f32 v13, v13\n"
"v_mfma_f32_16x16x32_bf16 v[56:59], a[188:191], v[188:191], v[56:59]\n"
"v_cvt_pk_bf16_f32 v67, v6, v7\n"
"v_cvt_pk_bf16_f32 v68, v8, v9\n"
"v_mfma_f32_16x16x32_bf16 v[60:63], a[192:195], v[176:179], v[204:207]\n"
"v_exp_f32 v14, v14\n"
"v_mfma_f32_16x16x32_bf16 v[60:63], a[196:199], v[180:183], v[60:63]\n"
"buffer_load_dwordx4 v244, s[40:43], s90 offen offset:192 lds\n"
"v_mfma_f32_16x16x32_bf16 v[60:63], a[200:203], v[184:187], v[60:63]\n"
"s_addk_i32 m0, 4288\n"
"v_exp_f32 v15, v15\n"
"v_mfma_f32_16x16x32_bf16 v[60:63], a[204:207], v[188:191], v[60:63]\n"
"s_waitcnt vmcnt(4) lgkmcnt(0)\n"
"s_barrier\n"
"v_cvt_pk_bf16_f32 v69, v10, v11\n"
"v_mfma_f32_16x16x32_bf16 a[0:3], v[96:99], v[64:67], 0\n"
"v_exp_f32 v16, v16\n"
"v_mfma_f32_16x16x32_bf16 a[16:19], v[100:103], v[64:67], 0\n"
"v_exp_f32 v17, v17\n"
"v_mfma_f32_16x16x32_bf16 a[32:35], v[104:107], v[64:67], 0\n"
"v_exp_f32 v18, v18\n"
"v_mfma_f32_16x16x32_bf16 a[48:51], v[108:111], v[64:67], 0\n"
"v_exp_f32 v19, v19\n"
"v_mfma_f32_16x16x32_bf16 a[64:67], v[112:115], v[64:67], 0\n"
"buffer_load_dwordx4 v245, s[44:47], s90 offen lds\n"
"v_mfma_f32_16x16x32_bf16 a[80:83], v[116:119], v[64:67], 0\n"
"s_addk_i32 m0, 4032\n"
"ds_read_b128 a[144:147], v231\n"
"v_cvt_pk_bf16_f32 v70, v12, v13\n"
"v_mfma_f32_16x16x32_bf16 a[96:99], v[120:123], v[64:67], 0\n"
"ds_read_b128 a[148:151], v231 offset:4096\n"
"v_cvt_pk_bf16_f32 v71, v14, v15\n"
"v_mfma_f32_16x16x32_bf16 a[112:115], v[124:127], v[64:67], 0\n"
"v_exp_f32 v20, v20\n"
"v_mfma_f32_16x16x32_bf16 a[128:131], v[212:215], v[64:67], 0\n"
"v_exp_f32 v21, v21\n"
"v_mfma_f32_16x16x32_bf16 a[132:135], v[212:215], v[68:71], 0\n"
"v_exp_f32 v22, v22\n"
"v_mfma_f32_16x16x32_bf16 a[116:119], v[124:127], v[68:71], 0\n"
"ds_read_b128 a[152:155], v231 offset:8192\n"
"v_cvt_pk_bf16_f32 v72, v16, v17\n"
"v_mfma_f32_16x16x32_bf16 a[100:103], v[120:123], v[68:71], 0\n"
"buffer_load_dwordx4 v245, s[44:47], s90 offen offset:64 lds\n"
"v_mfma_f32_16x16x32_bf16 a[84:87], v[116:119], v[68:71], 0\n"
"s_addk_i32 m0, 4032\n"
"v_exp_f32 v23, v23\n"
"v_mfma_f32_16x16x32_bf16 a[68:71], v[112:115], v[68:71], 0\n"
"ds_read_b128 a[156:159], v231 offset:12288\n"
"v_cvt_pk_bf16_f32 v73, v18, v19\n"
"v_mfma_f32_16x16x32_bf16 a[52:55], v[108:111], v[68:71], 0\n"
"v_exp_f32 v24, v24\n"
"v_mfma_f32_16x16x32_bf16 a[36:39], v[104:107], v[68:71], 0\n"
"v_cvt_pk_bf16_f32 v74, v20, v21\n"
"v_cvt_pk_bf16_f32 v75, v22, v23\n"
"v_mfma_f32_16x16x32_bf16 a[20:23], v[100:103], v[68:71], 0\n"
"v_exp_f32 v25, v25\n"
"v_mfma_f32_16x16x32_bf16 a[4:7], v[96:99], v[68:71], 0\n"
"buffer_load_dwordx4 v245, s[44:47], s90 offen offset:128 lds\n"
"v_mfma_f32_16x16x32_bf16 a[8:11], v[96:99], v[72:75], 0\n"
"s_addk_i32 m0, 4032\n"
"v_exp_f32 v26, v26\n"
"v_mfma_f32_16x16x32_bf16 a[24:27], v[100:103], v[72:75], 0\n"
"v_exp_f32 v27, v27\n"
"v_mfma_f32_16x16x32_bf16 a[40:43], v[104:107], v[72:75], 0\n"
"v_exp_f32 v28, v28\n"
"v_mfma_f32_16x16x32_bf16 a[56:59], v[108:111], v[72:75], 0\n"
"ds_read_b128 a[160:163], v231 offset:1024\n"
"v_cvt_pk_bf16_f32 v76, v24, v25\n"
"v_mfma_f32_16x16x32_bf16 a[72:75], v[112:115], v[72:75], 0\n"
"v_exp_f32 v29, v29\n"
"v_mfma_f32_16x16x32_bf16 a[88:91], v[116:119], v[72:75], 0\n"
"v_exp_f32 v30, v30\n"
"v_exp_f32 v31, v31\n"
"v_exp_f32 v32, v32\n"
"v_mfma_f32_16x16x32_bf16 a[104:107], v[120:123], v[72:75], 0\n"
"v_exp_f32 v33, v33\n"
"v_cvt_pk_bf16_f32 v77, v26, v27\n"
"v_cvt_pk_bf16_f32 v78, v28, v29\n"
"v_cvt_pk_bf16_f32 v79, v30, v31\n"
"v_mfma_f32_16x16x32_bf16 a[120:123], v[124:127], v[72:75], 0\n"
"buffer_load_dwordx4 v245, s[44:47], s90 offen offset:192 lds\n"
"v_mfma_f32_16x16x32_bf16 a[136:139], v[212:215], v[72:75], 0\n"
"s_mov_b32 s90, 0xc000\n"
"v_exp_f32 v34, v34\n"
"v_mfma_f32_16x16x32_bf16 a[140:143], v[212:215], v[76:79], 0\n"
"s_addk_i32 m0, 4288\n"
"s_nop 0\n"
"buffer_load_dwordx4 v244, s[40:43], s90 offen lds\n"
"v_mfma_f32_16x16x32_bf16 a[124:127], v[124:127], v[76:79], 0\n"
"s_addk_i32 m0, 4032\n"
"v_exp_f32 v35, v35\n"
"v_mfma_f32_16x16x32_bf16 a[108:111], v[120:123], v[76:79], 0\n"
"buffer_load_dwordx4 v244, s[40:43], s90 offen offset:64 lds\n"
"v_mfma_f32_16x16x32_bf16 a[92:95], v[116:119], v[76:79], 0\n"
"s_addk_i32 m0, 4032\n"
"v_exp_f32 v36, v36\n"
"v_mfma_f32_16x16x32_bf16 a[76:79], v[112:115], v[76:79], 0\n"
"buffer_load_dwordx4 v244, s[40:43], s90 offen offset:128 lds\n"
"v_mfma_f32_16x16x32_bf16 a[60:63], v[108:111], v[76:79], 0\n"
"s_addk_i32 m0, 4032\n"
"ds_read_b128 a[164:167], v231 offset:5120\n"
"ds_read_b128 a[168:171], v231 offset:9216\n"
"ds_read_b128 a[172:175], v231 offset:13312\n"
"v_exp_f32 v37, v37\n"
"v_mfma_f32_16x16x32_bf16 a[44:47], v[104:107], v[76:79], 0\n"
"buffer_load_dwordx4 v244, s[40:43], s90 offen offset:192 lds\n"
"v_mfma_f32_16x16x32_bf16 a[28:31], v[100:103], v[76:79], 0\n"
"s_addk_i32 m0, 4288\n"
"v_exp_f32 v38, v38\n"
"v_mfma_f32_16x16x32_bf16 a[12:15], v[96:99], v[76:79], 0\n"
"s_waitcnt lgkmcnt(6)\n"
"buffer_load_dwordx4 v245, s[44:47], s90 offen lds\n"
"v_mfma_f32_16x16x32_bf16 v[0:3], a[144:147], v[128:131], v[192:195]\n"
"s_addk_i32 m0, 4032\n"
"v_exp_f32 v39, v39\n"
"v_mfma_f32_16x16x32_bf16 v[0:3], a[148:151], v[132:135], v[0:3]\n"
"s_waitcnt lgkmcnt(4)\n"
"buffer_load_dwordx4 v245, s[44:47], s90 offen offset:64 lds\n"
"v_mfma_f32_16x16x32_bf16 v[0:3], a[152:155], v[136:139], v[0:3]\n"
"s_addk_i32 m0, 4032\n"
"v_exp_f32 v40, v40\n"
"v_mfma_f32_16x16x32_bf16 v[0:3], a[156:159], v[140:143], v[0:3]\n"
"s_waitcnt lgkmcnt(2)\n"
"v_exp_f32 v41, v41\n"
"v_mfma_f32_16x16x32_bf16 v[4:7], a[160:163], v[128:131], v[192:195]\n"
"ds_read_b64_tr_b16 v[96:97], v232\n"
"v_cvt_pk_bf16_f32 v80, v32, v33\n"
"v_mfma_f32_16x16x32_bf16 v[4:7], a[164:167], v[132:135], v[4:7]\n"
"s_waitcnt lgkmcnt(1)\n"
"v_exp_f32 v42, v42\n"
"v_mfma_f32_16x16x32_bf16 v[4:7], a[168:171], v[136:139], v[4:7]\n"
"v_exp_f32 v43, v43\n"
"v_mfma_f32_16x16x32_bf16 v[4:7], a[172:175], v[140:143], v[4:7]\n"
"buffer_load_dwordx4 v245, s[44:47], s90 offen offset:128 lds\n"
"v_mfma_f32_16x16x32_bf16 v[8:11], a[144:147], v[144:147], v[196:199]\n"
"s_addk_i32 m0, 4032\n"
"ds_read_b64_tr_b16 v[98:99], v232 offset:1024\n"
"v_cvt_pk_bf16_f32 v81, v34, v35\n"
"v_mfma_f32_16x16x32_bf16 v[8:11], a[148:151], v[148:151], v[8:11]\n"
"v_exp_f32 v44, v44\n"
"v_mfma_f32_16x16x32_bf16 v[8:11], a[152:155], v[152:155], v[8:11]\n"
"v_exp_f32 v45, v45\n"
"v_mfma_f32_16x16x32_bf16 v[8:11], a[156:159], v[156:159], v[8:11]\n"
"ds_read_b64_tr_b16 v[100:101], v252\n"
"v_cvt_pk_bf16_f32 v82, v36, v37\n"
"v_mfma_f32_16x16x32_bf16 v[12:15], a[160:163], v[144:147], v[196:199]\n"
"v_exp_f32 v46, v46\n"
"v_mfma_f32_16x16x32_bf16 v[12:15], a[164:167], v[148:151], v[12:15]\n"
"buffer_load_dwordx4 v245, s[44:47], s90 offen offset:192 lds\n"
"v_mfma_f32_16x16x32_bf16 v[12:15], a[168:171], v[152:155], v[12:15]\n"
"s_add_u32 m0, s86, 0x11000\n"
"v_exp_f32 v47, v47\n"
"v_mfma_f32_16x16x32_bf16 v[12:15], a[172:175], v[156:159], v[12:15]\n"
"ds_read_b64_tr_b16 v[102:103], v252 offset:1024\n"
"v_cvt_pk_bf16_f32 v83, v38, v39\n"
"v_mfma_f32_16x16x32_bf16 v[16:19], a[144:147], v[160:163], v[200:203]\n"
"v_exp_f32 v48, v48\n"
"v_mfma_f32_16x16x32_bf16 v[16:19], a[148:151], v[164:167], v[16:19]\n"
"ds_read_b64_tr_b16 v[104:105], v232 offset:4096\n"
"v_cvt_pk_bf16_f32 v84, v40, v41\n"
"v_mfma_f32_16x16x32_bf16 v[16:19], a[152:155], v[168:171], v[16:19]\n"
"buffer_load_dwordx4 v230, s[72:75], 0 offen lds\n"
"v_mfma_f32_16x16x32_bf16 v[16:19], a[156:159], v[172:175], v[16:19]\n"
"s_addk_i32 m0, 3968\n"
"v_exp_f32 v49, v49\n"
"v_mfma_f32_16x16x32_bf16 v[20:23], a[160:163], v[160:163], v[200:203]\n"
"v_exp_f32 v50, v50\n"
"v_mfma_f32_16x16x32_bf16 v[20:23], a[164:167], v[164:167], v[20:23]\n"
"ds_read_b64_tr_b16 v[106:107], v232 offset:5120\n"
"v_cvt_pk_bf16_f32 v85, v42, v43\n"
"v_mfma_f32_16x16x32_bf16 v[20:23], a[168:171], v[168:171], v[20:23]\n"
"v_exp_f32 v51, v51\n"
"v_mfma_f32_16x16x32_bf16 v[20:23], a[172:175], v[172:175], v[20:23]\n"
"ds_read_b64_tr_b16 v[108:109], v252 offset:4096\n"
"v_cvt_pk_bf16_f32 v86, v44, v45\n"
"v_mfma_f32_16x16x32_bf16 v[24:27], a[144:147], v[176:179], v[204:207]\n"
"buffer_load_dwordx4 v230, s[72:75], 0 offen offset:128 lds\n"
"v_mfma_f32_16x16x32_bf16 v[24:27], a[148:151], v[180:183], v[24:27]\n"
"s_addk_i32 m0, 3968\n"
"ds_read_b64_tr_b16 v[110:111], v252 offset:5120\n"
"v_cvt_pk_bf16_f32 v87, v46, v47\n"
"v_mfma_f32_16x16x32_bf16 v[24:27], a[152:155], v[184:187], v[24:27]\n"
"v_exp_f32 v52, v52\n"
"v_mfma_f32_16x16x32_bf16 v[24:27], a[156:159], v[188:191], v[24:27]\n"
"ds_read_b64_tr_b16 v[112:113], v232 offset:8192\n"
"v_cvt_pk_bf16_f32 v88, v48, v49\n"
"v_mfma_f32_16x16x32_bf16 v[28:31], a[160:163], v[176:179], v[204:207]\n"
"ds_read_b64_tr_b16 v[114:115], v232 offset:9216\n"
"ds_read_b64_tr_b16 v[116:117], v252 offset:8192\n"
"ds_read_b64_tr_b16 v[118:119], v252 offset:9216\n"
"ds_read_b64_tr_b16 v[120:121], v232 offset:12288\n"
"ds_read_b64_tr_b16 v[122:123], v232 offset:13312\n"
"ds_read_b64_tr_b16 v[124:125], v252 offset:12288\n"
"ds_read_b64_tr_b16 v[126:127], v252 offset:13312\n"
"v_mfma_f32_16x16x32_bf16 v[28:31], a[164:167], v[180:183], v[28:31]\n"
"buffer_load_dwordx4 v230, s[72:75], 0 offen offset:256 lds\n"
"v_mfma_f32_16x16x32_bf16 v[28:31], a[168:171], v[184:187], v[28:31]\n"
"s_addk_i32 m0, 3968\n"
"v_exp_f32 v53, v53\n"
"v_mfma_f32_16x16x32_bf16 v[28:31], a[172:175], v[188:191], v[28:31]\n"
"s_waitcnt lgkmcnt(12)\n"
"v_exp_f32 v54, v54\n"
"v_mfma_f32_16x16x32_bf16 a[0:3], v[96:99], v[80:83], a[0:3]\n"
"v_exp_f32 v55, v55\n"
"v_mfma_f32_16x16x32_bf16 a[16:19], v[100:103], v[80:83], a[16:19]\n"
"s_waitcnt lgkmcnt(8)\n"
"v_exp_f32 v56, v56\n"
"v_mfma_f32_16x16x32_bf16 a[32:35], v[104:107], v[80:83], a[32:35]\n"
"v_exp_f32 v57, v57\n"
"v_mfma_f32_16x16x32_bf16 a[48:51], v[108:111], v[80:83], a[48:51]\n"
"s_waitcnt lgkmcnt(4)\n"
"buffer_load_dwordx4 v230, s[72:75], 0 offen offset:384 lds\n"
"v_mfma_f32_16x16x32_bf16 a[64:67], v[112:115], v[80:83], a[64:67]\n"
"s_addk_i32 m0, 3968\n"
"ds_read_b128 a[144:147], v231 offset:2048\n"
"v_cvt_pk_bf16_f32 v89, v50, v51\n"
"v_mfma_f32_16x16x32_bf16 a[80:83], v[116:119], v[80:83], a[80:83]\n"
"s_waitcnt lgkmcnt(1)\n"
"v_exp_f32 v58, v58\n"
"v_mfma_f32_16x16x32_bf16 a[96:99], v[120:123], v[80:83], a[96:99]\n"
"v_exp_f32 v59, v59\n"
"v_mfma_f32_16x16x32_bf16 a[112:115], v[124:127], v[80:83], a[112:115]\n"
"v_exp_f32 v60, v60\n"
"v_mfma_f32_16x16x32_bf16 a[128:131], v[212:215], v[80:83], a[128:131]\n"
"v_exp_f32 v61, v61\n"
"v_mfma_f32_16x16x32_bf16 a[132:135], v[212:215], v[84:87], a[132:135]\n"
"buffer_load_dwordx4 v230, s[72:75], 0 offen offset:512 lds\n"
"v_mfma_f32_16x16x32_bf16 a[116:119], v[124:127], v[84:87], a[116:119]\n"
"ds_read_b128 a[148:151], v231 offset:6144\n"
"v_cvt_pk_bf16_f32 v90, v52, v53\n"
"v_mfma_f32_16x16x32_bf16 a[100:103], v[120:123], v[84:87], a[100:103]\n"
"v_exp_f32 v62, v62\n"
"v_mfma_f32_16x16x32_bf16 a[84:87], v[116:119], v[84:87], a[84:87]\n"
"ds_read_b128 a[152:155], v231 offset:10240\n"
"v_cvt_pk_bf16_f32 v91, v54, v55\n"
"v_mfma_f32_16x16x32_bf16 a[68:71], v[112:115], v[84:87], a[68:71]\n"
"v_exp_f32 v63, v63\n"
"v_exp_f32 v0, v0\n"
"v_mfma_f32_16x16x32_bf16 a[52:55], v[108:111], v[84:87], a[52:55]\n"
"v_exp_f32 v1, v1\n"
"v_mfma_f32_16x16x32_bf16 a[36:39], v[104:107], v[84:87], a[36:39]\n"
"ds_read_b128 a[156:159], v231 offset:14336\n"
"v_cvt_pk_bf16_f32 v92, v56, v57\n"
"v_mfma_f32_16x16x32_bf16 a[20:23], v[100:103], v[84:87], a[20:23]\n"
"v_exp_f32 v2, v2\n"
"v_mfma_f32_16x16x32_bf16 a[4:7], v[96:99], v[84:87], a[4:7]\n"
"ds_read_b128 a[160:163], v231 offset:3072\n"
"v_cvt_pk_bf16_f32 v93, v58, v59\n"
"v_mfma_f32_16x16x32_bf16 a[8:11], v[96:99], v[88:91], a[8:11]\n"
"v_exp_f32 v3, v3\n"
"v_mfma_f32_16x16x32_bf16 a[24:27], v[100:103], v[88:91], a[24:27]\n"
"ds_read_b128 a[164:167], v231 offset:7168\n"
"v_cvt_pk_bf16_f32 v94, v60, v61\n"
"v_mfma_f32_16x16x32_bf16 a[40:43], v[104:107], v[88:91], a[40:43]\n"
"v_exp_f32 v4, v4\n"
"v_mfma_f32_16x16x32_bf16 a[56:59], v[108:111], v[88:91], a[56:59]\n"
"ds_read_b128 a[168:171], v231 offset:11264\n"
"v_cvt_pk_bf16_f32 v95, v62, v63\n"
"v_mfma_f32_16x16x32_bf16 a[72:75], v[112:115], v[88:91], a[72:75]\n"
"v_exp_f32 v5, v5\n"
"v_mfma_f32_16x16x32_bf16 a[88:91], v[116:119], v[88:91], a[88:91]\n"
"ds_read_b128 a[172:175], v231 offset:15360\n"
"v_cvt_pk_bf16_f32 v64, v0, v1\n"
"v_mfma_f32_16x16x32_bf16 a[104:107], v[120:123], v[88:91], a[104:107]\n"
"v_exp_f32 v6, v6\n"
"v_mfma_f32_16x16x32_bf16 a[120:123], v[124:127], v[88:91], a[120:123]\n"
"v_exp_f32 v7, v7\n"
"v_mfma_f32_16x16x32_bf16 a[136:139], v[212:215], v[88:91], a[136:139]\n"
"v_cvt_pk_bf16_f32 v65, v2, v3\n"
"v_mfma_f32_16x16x32_bf16 a[140:143], v[212:215], v[92:95], a[140:143]\n"
"v_exp_f32 v8, v8\n"
"v_mfma_f32_16x16x32_bf16 a[124:127], v[124:127], v[92:95], a[124:127]\n"
"v_cvt_pk_bf16_f32 v66, v4, v5\n"
"v_mfma_f32_16x16x32_bf16 a[108:111], v[120:123], v[92:95], a[108:111]\n"
"v_exp_f32 v9, v9\n"
"v_mfma_f32_16x16x32_bf16 a[92:95], v[116:119], v[92:95], a[92:95]\n"
"v_exp_f32 v10, v10\n"
"v_mfma_f32_16x16x32_bf16 a[76:79], v[112:115], v[92:95], a[76:79]\n"
"s_waitcnt vmcnt(29)\n"
"s_barrier\n"
"v_cvt_pk_bf16_f32 v67, v6, v7\n"
"v_mfma_f32_16x16x32_bf16 a[60:63], v[108:111], v[92:95], a[60:63]\n"
"s_add_u32 s84, s8, 0x30000\n"
"v_exp_f32 v11, v11\n"
"v_mfma_f32_16x16x32_bf16 a[44:47], v[104:107], v[92:95], a[44:47]\n"
"v_exp_f32 v12, v12\n"
"v_mfma_f32_16x16x32_bf16 a[28:31], v[100:103], v[92:95], a[28:31]\n"
"v_cvt_pk_bf16_f32 v68, v8, v9\n"
"v_mfma_f32_16x16x32_bf16 a[12:15], v[96:99], v[92:95], a[12:15]\n"
"s_waitcnt lgkmcnt(6)\n"
"v_exp_f32 v13, v13\n"
"v_mfma_f32_16x16x32_bf16 v[32:35], a[144:147], v[128:131], v[192:195]\n"
"ds_read_b64_tr_b16 v[96:97], v232 offset:2048\n"
"v_cvt_pk_bf16_f32 v69, v10, v11\n"
"v_mfma_f32_16x16x32_bf16 v[32:35], a[148:151], v[132:135], v[32:35]\n"
"s_waitcnt lgkmcnt(5)\n"
"v_exp_f32 v14, v14\n"
"v_mfma_f32_16x16x32_bf16 v[32:35], a[152:155], v[136:139], v[32:35]\n"
"ds_read_b64_tr_b16 v[98:99], v232 offset:3072\n"
"v_mov_b32 v254, s95\n"
"v_mfma_f32_16x16x32_bf16 v[32:35], a[156:159], v[140:143], v[32:35]\n"
"s_waitcnt lgkmcnt(4)\n"
"buffer_store_dword v254, v234, s[56:59], s84 offen sc1\n"
"v_mfma_f32_16x16x32_bf16 v[36:39], a[160:163], v[128:131], v[192:195]\n"
"v_exp_f32 v15, v15\n"
"v_mfma_f32_16x16x32_bf16 v[36:39], a[164:167], v[132:135], v[36:39]\n"
"s_waitcnt lgkmcnt(2)\n"
"ds_read_b64_tr_b16 v[100:101], v252 offset:2048\n"
"v_cvt_pk_bf16_f32 v70, v12, v13\n"
"v_mfma_f32_16x16x32_bf16 v[36:39], a[168:171], v[136:139], v[36:39]\n"
"v_exp_f32 v16, v16\n"
"v_mfma_f32_16x16x32_bf16 v[36:39], a[172:175], v[140:143], v[36:39]\n"
"ds_read_b64_tr_b16 v[102:103], v252 offset:3072\n"
"v_cvt_pk_bf16_f32 v71, v14, v15\n"
"v_mfma_f32_16x16x32_bf16 v[40:43], a[144:147], v[144:147], v[196:199]\n"
"v_exp_f32 v17, v17\n"
"v_mfma_f32_16x16x32_bf16 v[40:43], a[148:151], v[148:151], v[40:43]\n"
"ds_read_b64_tr_b16 v[104:105], v232 offset:6144\n"
"v_cvt_pk_bf16_f32 v72, v16, v17\n"
"v_mfma_f32_16x16x32_bf16 v[40:43], a[152:155], v[152:155], v[40:43]\n"
"ds_read_b64_tr_b16 v[106:107], v232 offset:7168\n"
"v_mfma_f32_16x16x32_bf16 v[40:43], a[156:159], v[156:159], v[40:43]\n"
"v_exp_f32 v18, v18\n"
"v_mfma_f32_16x16x32_bf16 v[44:47], a[160:163], v[144:147], v[196:199]\n"
"ds_read_b64_tr_b16 v[108:109], v252 offset:6144\n"
"v_mfma_f32_16x16x32_bf16 v[44:47], a[164:167], v[148:151], v[44:47]\n"
"ds_read_b64_tr_b16 v[110:111], v252 offset:7168\n"
"v_mfma_f32_16x16x32_bf16 v[44:47], a[168:171], v[152:155], v[44:47]\n"
"v_exp_f32 v19, v19\n"
"v_mfma_f32_16x16x32_bf16 v[44:47], a[172:175], v[156:159], v[44:47]\n"
"ds_read_b64_tr_b16 v[112:113], v232 offset:10240\n"
"v_cvt_pk_bf16_f32 v73, v18, v19\n"
"v_mfma_f32_16x16x32_bf16 v[48:51], a[144:147], v[160:163], v[200:203]\n"
"v_exp_f32 v20, v20\n"
"v_mfma_f32_16x16x32_bf16 v[48:51], a[148:151], v[164:167], v[48:51]\n"
"ds_read_b64_tr_b16 v[114:115], v232 offset:11264\n"
"ds_read_b64_tr_b16 v[116:117], v252 offset:10240\n"
"v_mfma_f32_16x16x32_bf16 v[48:51], a[152:155], v[168:171], v[48:51]\n"
"v_exp_f32 v21, v21\n"
"v_mfma_f32_16x16x32_bf16 v[48:51], a[156:159], v[172:175], v[48:51]\n"
"ds_read_b64_tr_b16 v[118:119], v252 offset:11264\n"
"v_cvt_pk_bf16_f32 v74, v20, v21\n"
"v_mfma_f32_16x16x32_bf16 v[52:55], a[160:163], v[160:163], v[200:203]\n"
"v_exp_f32 v22, v22\n"
"v_mfma_f32_16x16x32_bf16 v[52:55], a[164:167], v[164:167], v[52:55]\n"
"ds_read_b64_tr_b16 v[120:121], v232 offset:14336\n"
"ds_read_b64_tr_b16 v[122:123], v232 offset:15360\n"
"v_mfma_f32_16x16x32_bf16 v[52:55], a[168:171], v[168:171], v[52:55]\n"
"v_exp_f32 v23, v23\n"
"v_mfma_f32_16x16x32_bf16 v[52:55], a[172:175], v[172:175], v[52:55]\n"
"ds_read_b64_tr_b16 v[124:125], v252 offset:14336\n"
"v_cvt_pk_bf16_f32 v75, v22, v23\n"
"v_mfma_f32_16x16x32_bf16 v[56:59], a[144:147], v[176:179], v[204:207]\n"
"ds_read_b64_tr_b16 v[126:127], v252 offset:15360\n"
"v_mfma_f32_16x16x32_bf16 v[56:59], a[148:151], v[180:183], v[56:59]\n"
"v_exp_f32 v24, v24\n"
"v_mfma_f32_16x16x32_bf16 v[56:59], a[152:155], v[184:187], v[56:59]\n"
"v_exp_f32 v25, v25\n"
"v_mfma_f32_16x16x32_bf16 v[56:59], a[156:159], v[188:191], v[56:59]\n"
"v_exp_f32 v26, v26\n"
"v_mfma_f32_16x16x32_bf16 v[60:63], a[160:163], v[176:179], v[204:207]\n"
"v_exp_f32 v27, v27\n"
"v_mfma_f32_16x16x32_bf16 v[60:63], a[164:167], v[180:183], v[60:63]\n"
"v_cvt_pk_bf16_f32 v76, v24, v25\n"
"v_mfma_f32_16x16x32_bf16 v[60:63], a[168:171], v[184:187], v[60:63]\n"
"v_exp_f32 v28, v28\n"
"v_mfma_f32_16x16x32_bf16 v[60:63], a[172:175], v[188:191], v[60:63]\n"
"s_waitcnt vmcnt(14) lgkmcnt(0)\n"
"s_barrier\n"
"v_cvt_pk_bf16_f32 v77, v26, v27\n"
"v_mfma_f32_16x16x32_bf16 a[0:3], v[96:99], v[64:67], a[0:3]\n"
"v_exp_f32 v29, v29\n"
"v_mfma_f32_16x16x32_bf16 a[16:19], v[100:103], v[64:67], a[16:19]\n"
"s_xor_b32 s84, s8, 0x1000\n"
"v_exp_f32 v30, v30\n"
"v_mfma_f32_16x16x32_bf16 a[32:35], v[104:107], v[64:67], a[32:35]\n"
"ds_read_b128 a[144:147], v246\n"
"v_cvt_pk_bf16_f32 v78, v28, v29\n"
"v_mfma_f32_16x16x32_bf16 a[48:51], v[108:111], v[64:67], a[48:51]\n"
"v_exp_f32 v31, v31\n"
"v_mfma_f32_16x16x32_bf16 a[64:67], v[112:115], v[64:67], a[64:67]\n"
"v_exp_f32 v32, v32\n"
"v_mfma_f32_16x16x32_bf16 a[80:83], v[116:119], v[64:67], a[80:83]\n"
"ds_read_b128 a[148:151], v246 offset:4096\n"
"v_cvt_pk_bf16_f32 v79, v30, v31\n"
"v_mfma_f32_16x16x32_bf16 a[96:99], v[120:123], v[64:67], a[96:99]\n"
"v_mov_b32 v254, 0\n"
"v_mfma_f32_16x16x32_bf16 a[112:115], v[124:127], v[64:67], a[112:115]\n"
"s_add_u32 s84, s84, 0x30000\n"
"v_exp_f32 v33, v33\n"
"v_mfma_f32_16x16x32_bf16 a[128:131], v[212:215], v[64:67], a[128:131]\n"
"ds_read_b128 a[152:155], v246 offset:8192\n"
"v_cvt_pk_bf16_f32 v80, v32, v33\n"
"v_mfma_f32_16x16x32_bf16 a[132:135], v[212:215], v[68:71], a[132:135]\n"
"v_exp_f32 v34, v34\n"
"v_mfma_f32_16x16x32_bf16 a[116:119], v[124:127], v[68:71], a[116:119]\n"
"v_exp_f32 v35, v35\n"
"v_mfma_f32_16x16x32_bf16 a[100:103], v[120:123], v[68:71], a[100:103]\n"
"ds_read_b128 a[156:159], v246 offset:12288\n"
"v_cvt_pk_bf16_f32 v81, v34, v35\n"
"v_mfma_f32_16x16x32_bf16 a[84:87], v[116:119], v[68:71], a[84:87]\n"
"buffer_load_dword v249, v254, s[56:59], s84 offen sc1\n"
"v_mfma_f32_16x16x32_bf16 a[68:71], v[112:115], v[68:71], a[68:71]\n"
"s_mov_b32 s90, 0x10000\n"
"v_exp_f32 v36, v36\n"
"v_mfma_f32_16x16x32_bf16 a[52:55], v[108:111], v[68:71], a[52:55]\n"
"s_add_u32 m0, s86, 0x9000\n"
"v_exp_f32 v37, v37\n"
"v_mfma_f32_16x16x32_bf16 a[36:39], v[104:107], v[68:71], a[36:39]\n"
"ds_read_b128 a[160:163], v246 offset:1024\n"
"v_cvt_pk_bf16_f32 v82, v36, v37\n"
"v_mfma_f32_16x16x32_bf16 a[20:23], v[100:103], v[68:71], a[20:23]\n"
"v_exp_f32 v38, v38\n"
"v_mfma_f32_16x16x32_bf16 a[4:7], v[96:99], v[68:71], a[4:7]\n"
"ds_read_b128 a[164:167], v246 offset:5120\n"
"v_mfma_f32_16x16x32_bf16 a[8:11], v[96:99], v[72:75], a[8:11]\n"
"v_exp_f32 v39, v39\n"
"v_mfma_f32_16x16x32_bf16 a[24:27], v[100:103], v[72:75], a[24:27]\n"
"v_cvt_pk_bf16_f32 v83, v38, v39\n"
"v_mfma_f32_16x16x32_bf16 a[40:43], v[104:107], v[72:75], a[40:43]\n"
"buffer_load_dwordx4 v244, s[40:43], s90 offen lds\n"
"v_mfma_f32_16x16x32_bf16 a[56:59], v[108:111], v[72:75], a[56:59]\n"
"s_addk_i32 m0, 4032\n"
"ds_read_b128 a[168:171], v246 offset:9216\n"
"v_mfma_f32_16x16x32_bf16 a[72:75], v[112:115], v[72:75], a[72:75]\n"
"v_exp_f32 v40, v40\n"
"v_mfma_f32_16x16x32_bf16 a[88:91], v[116:119], v[72:75], a[88:91]\n"
"v_exp_f32 v41, v41\n"
"v_mfma_f32_16x16x32_bf16 a[104:107], v[120:123], v[72:75], a[104:107]\n"
"ds_read_b128 a[172:175], v246 offset:13312\n"
"v_cvt_pk_bf16_f32 v84, v40, v41\n"
"v_mfma_f32_16x16x32_bf16 a[120:123], v[124:127], v[72:75], a[120:123]\n"
"buffer_load_dwordx4 v244, s[40:43], s90 offen offset:64 lds\n"
"v_mfma_f32_16x16x32_bf16 a[136:139], v[212:215], v[72:75], a[136:139]\n"
"s_addk_i32 m0, 4032\n"
"v_exp_f32 v42, v42\n"
"v_mfma_f32_16x16x32_bf16 a[140:143], v[212:215], v[76:79], a[140:143]\n"
"v_exp_f32 v43, v43\n"
"v_mfma_f32_16x16x32_bf16 a[124:127], v[124:127], v[76:79], a[124:127]\n"
"v_cvt_pk_bf16_f32 v85, v42, v43\n"
"v_mfma_f32_16x16x32_bf16 a[108:111], v[120:123], v[76:79], a[108:111]\n"
"v_exp_f32 v44, v44\n"
"v_mfma_f32_16x16x32_bf16 a[92:95], v[116:119], v[76:79], a[92:95]\n"
"buffer_load_dwordx4 v244, s[40:43], s90 offen offset:128 lds\n"
"v_mfma_f32_16x16x32_bf16 a[76:79], v[112:115], v[76:79], a[76:79]\n"
"s_addk_i32 m0, 4032\n"
"v_exp_f32 v45, v45\n"
"v_mfma_f32_16x16x32_bf16 a[60:63], v[108:111], v[76:79], a[60:63]\n"
"v_cvt_pk_bf16_f32 v86, v44, v45\n"
"v_mfma_f32_16x16x32_bf16 a[44:47], v[104:107], v[76:79], a[44:47]\n"
"v_exp_f32 v46, v46\n"
"v_mfma_f32_16x16x32_bf16 a[28:31], v[100:103], v[76:79], a[28:31]\n"
"v_exp_f32 v47, v47\n"
"v_mfma_f32_16x16x32_bf16 a[12:15], v[96:99], v[76:79], a[12:15]\n"
"s_waitcnt lgkmcnt(6)\n"
"v_cvt_pk_bf16_f32 v87, v46, v47\n"
"v_mfma_f32_16x16x32_bf16 v[0:3], a[144:147], v[128:131], v[192:195]\n"
"buffer_load_dwordx4 v244, s[40:43], s90 offen offset:192 lds\n"
"v_mfma_f32_16x16x32_bf16 v[0:3], a[148:151], v[132:135], v[0:3]\n"
"s_waitcnt lgkmcnt(4)\n"
"ds_read_b64_tr_b16 v[96:97], v247\n"
"v_mfma_f32_16x16x32_bf16 v[0:3], a[152:155], v[136:139], v[0:3]\n"
"s_addk_i32 m0, 4288\n"
"ds_read_b64_tr_b16 v[98:99], v247 offset:1024\n"
"v_mfma_f32_16x16x32_bf16 v[0:3], a[156:159], v[140:143], v[0:3]\n"
"s_waitcnt lgkmcnt(4)\n"
"v_exp_f32 v48, v48\n"
"v_mfma_f32_16x16x32_bf16 v[4:7], a[160:163], v[128:131], v[192:195]\n"
"ds_read_b64_tr_b16 v[100:101], v248\n"
"v_mfma_f32_16x16x32_bf16 v[4:7], a[164:167], v[132:135], v[4:7]\n"
"s_waitcnt lgkmcnt(3)\n"
"v_exp_f32 v49, v49\n"
"v_mfma_f32_16x16x32_bf16 v[4:7], a[168:171], v[136:139], v[4:7]\n"
"buffer_load_dwordx4 v245, s[44:47], s90 offen lds\n"
"v_mfma_f32_16x16x32_bf16 v[4:7], a[172:175], v[140:143], v[4:7]\n"
"s_addk_i32 m0, 4032\n"
"v_exp_f32 v50, v50\n"
"v_mfma_f32_16x16x32_bf16 v[8:11], a[144:147], v[144:147], v[196:199]\n"
"ds_read_b64_tr_b16 v[102:103], v248 offset:1024\n"
"v_cvt_pk_bf16_f32 v88, v48, v49\n"
"v_mfma_f32_16x16x32_bf16 v[8:11], a[148:151], v[148:151], v[8:11]\n"
"v_exp_f32 v51, v51\n"
"v_mfma_f32_16x16x32_bf16 v[8:11], a[152:155], v[152:155], v[8:11]\n"
"buffer_load_dwordx4 v245, s[44:47], s90 offen offset:64 lds\n"
"v_mfma_f32_16x16x32_bf16 v[8:11], a[156:159], v[156:159], v[8:11]\n"
"s_addk_i32 m0, 4032\n"
"v_exp_f32 v52, v52\n"
"v_mfma_f32_16x16x32_bf16 v[12:15], a[160:163], v[144:147], v[196:199]\n"
"ds_read_b64_tr_b16 v[104:105], v247 offset:4096\n"
"v_cvt_pk_bf16_f32 v89, v50, v51\n"
"v_mfma_f32_16x16x32_bf16 v[12:15], a[164:167], v[148:151], v[12:15]\n"
"v_exp_f32 v53, v53\n"
"v_mfma_f32_16x16x32_bf16 v[12:15], a[168:171], v[152:155], v[12:15]\n"
"ds_read_b64_tr_b16 v[106:107], v247 offset:5120\n"
"v_cvt_pk_bf16_f32 v90, v52, v53\n"
"v_mfma_f32_16x16x32_bf16 v[12:15], a[172:175], v[156:159], v[12:15]\n"
"v_exp_f32 v54, v54\n"
"v_mfma_f32_16x16x32_bf16 v[16:19], a[144:147], v[160:163], v[200:203]\n"
"buffer_load_dwordx4 v245, s[44:47], s90 offen offset:128 lds\n"
"v_mfma_f32_16x16x32_bf16 v[16:19], a[148:151], v[164:167], v[16:19]\n"
"s_addk_i32 m0, 4032\n"
"v_exp_f32 v55, v55\n"
"v_mfma_f32_16x16x32_bf16 v[16:19], a[152:155], v[168:171], v[16:19]\n"
"ds_read_b64_tr_b16 v[108:109], v248 offset:4096\n"
"v_cvt_pk_bf16_f32 v91, v54, v55\n"
"v_mfma_f32_16x16x32_bf16 v[16:19], a[156:159], v[172:175], v[16:19]\n"
"ds_read_b64_tr_b16 v[110:111], v248 offset:5120\n"
"ds_read_b64_tr_b16 v[112:113], v247 offset:8192\n"
"v_mfma_f32_16x16x32_bf16 v[20:23], a[160:163], v[160:163], v[200:203]\n"
"v_exp_f32 v56, v56\n"
"v_mfma_f32_16x16x32_bf16 v[20:23], a[164:167], v[164:167], v[20:23]\n"
"buffer_load_dwordx4 v245, s[44:47], s90 offen offset:192 lds\n"
"v_mfma_f32_16x16x32_bf16 v[20:23], a[168:171], v[168:171], v[20:23]\n"
"v_exp_f32 v57, v57\n"
"v_mfma_f32_16x16x32_bf16 v[20:23], a[172:175], v[172:175], v[20:23]\n"
"ds_read_b64_tr_b16 v[114:115], v247 offset:9216\n"
"v_cvt_pk_bf16_f32 v92, v56, v57\n"
"v_mfma_f32_16x16x32_bf16 v[24:27], a[144:147], v[176:179], v[204:207]\n"
"v_exp_f32 v58, v58\n"
"v_mfma_f32_16x16x32_bf16 v[24:27], a[148:151], v[180:183], v[24:27]\n"
"ds_read_b64_tr_b16 v[116:117], v248 offset:8192\n"
"ds_read_b64_tr_b16 v[118:119], v248 offset:9216\n"
"v_mfma_f32_16x16x32_bf16 v[24:27], a[152:155], v[184:187], v[24:27]\n"
"v_exp_f32 v59, v59\n"
"v_mfma_f32_16x16x32_bf16 v[24:27], a[156:159], v[188:191], v[24:27]\n"
"ds_read_b64_tr_b16 v[120:121], v247 offset:12288\n"
"v_cvt_pk_bf16_f32 v93, v58, v59\n"
"v_mfma_f32_16x16x32_bf16 v[28:31], a[160:163], v[176:179], v[204:207]\n"
"ds_read_b64_tr_b16 v[122:123], v247 offset:13312\n"
"ds_read_b64_tr_b16 v[124:125], v248 offset:12288\n"
"ds_read_b64_tr_b16 v[126:127], v248 offset:13312\n"
"v_exp_f32 v60, v60\n"
"v_mfma_f32_16x16x32_bf16 v[28:31], a[164:167], v[180:183], v[28:31]\n"
"v_exp_f32 v61, v61\n"
"v_mfma_f32_16x16x32_bf16 v[28:31], a[168:171], v[184:187], v[28:31]\n"
"v_cvt_pk_bf16_f32 v94, v60, v61\n"
"v_mfma_f32_16x16x32_bf16 v[28:31], a[172:175], v[188:191], v[28:31]\n"
"s_waitcnt lgkmcnt(12)\n"
"v_exp_f32 v62, v62\n"
"v_mfma_f32_16x16x32_bf16 a[0:3], v[96:99], v[80:83], a[0:3]\n"
"v_exp_f32 v63, v63\n"
"v_mfma_f32_16x16x32_bf16 a[16:19], v[100:103], v[80:83], a[16:19]\n"
"s_waitcnt lgkmcnt(8)\n"
"v_exp_f32 v0, v0\n"
"v_mfma_f32_16x16x32_bf16 a[32:35], v[104:107], v[80:83], a[32:35]\n"
"ds_read_b128 a[144:147], v246 offset:2048\n"
"v_cvt_pk_bf16_f32 v95, v62, v63\n"
"v_mfma_f32_16x16x32_bf16 a[48:51], v[108:111], v[80:83], a[48:51]\n"
"s_waitcnt vmcnt(8)\n"
"v_cmp_ne_u32 vcc, s95, v249\n"
"s_cbranch_vccnz L_slow1\n"
"L_back1:\n"
"s_waitcnt lgkmcnt(5)\n"
"v_mfma_f32_16x16x32_bf16 a[64:67], v[112:115], v[80:83], a[64:67]\n"
"s_xor_b32 s84, s8, 0x1000\n"
"v_exp_f32 v1, v1\n"
"v_mfma_f32_16x16x32_bf16 a[80:83], v[116:119], v[80:83], a[80:83]\n"
"s_waitcnt lgkmcnt(1)\n"
"ds_read_b128 a[148:151], v246 offset:6144\n"
"v_cvt_pk_bf16_f32 v64, v0, v1\n"
"v_mfma_f32_16x16x32_bf16 a[96:99], v[120:123], v[80:83], a[96:99]\n"
"s_mul_i32 s84, s84, 0xc0\n"
"v_exp_f32 v2, v2\n"
"v_mfma_f32_16x16x32_bf16 a[112:115], v[124:127], v[80:83], a[112:115]\n"
"s_add_u32 s84, s84, s87\n"
"v_exp_f32 v3, v3\n"
"v_mfma_f32_16x16x32_bf16 a[128:131], v[212:215], v[80:83], a[128:131]\n"
"s_add_u32 s84, s84, 0x2e2c000\n"
"ds_read_b128 a[152:155], v246 offset:10240\n"
"v_cvt_pk_bf16_f32 v65, v2, v3\n"
"v_mfma_f32_16x16x32_bf16 a[132:135], v[212:215], v[84:87], a[132:135]\n"
"v_exp_f32 v4, v4\n"
"v_mfma_f32_16x16x32_bf16 a[116:119], v[124:127], v[84:87], a[116:119]\n"
"buffer_load_dwordx4 a[176:179], v235, s[56:59], s84 offen sc1\n"
"v_mfma_f32_16x16x32_bf16 a[100:103], v[120:123], v[84:87], a[100:103]\n"
"ds_read_b128 a[156:159], v246 offset:14336\n"
"v_mfma_f32_16x16x32_bf16 a[84:87], v[116:119], v[84:87], a[84:87]\n"
"v_exp_f32 v5, v5\n"
"v_mfma_f32_16x16x32_bf16 a[68:71], v[112:115], v[84:87], a[68:71]\n"
"buffer_load_dwordx4 a[180:183], v235, s[56:59], s84 offen offset:1024 sc1\n"
"v_mfma_f32_16x16x32_bf16 a[52:55], v[108:111], v[84:87], a[52:55]\n"
"v_exp_f32 v6, v6\n"
"v_mfma_f32_16x16x32_bf16 a[36:39], v[104:107], v[84:87], a[36:39]\n"
"buffer_load_dwordx4 a[184:187], v235, s[56:59], s84 offen offset:2048 sc1\n"
"v_mfma_f32_16x16x32_bf16 a[20:23], v[100:103], v[84:87], a[20:23]\n"
"v_exp_f32 v7, v7\n"
"v_mfma_f32_16x16x32_bf16 a[4:7], v[96:99], v[84:87], a[4:7]\n"
"buffer_load_dwordx4 a[188:191], v235, s[56:59], s84 offen offset:3072 sc1\n"
"v_mfma_f32_16x16x32_bf16 a[8:11], v[96:99], v[88:91], a[8:11]\n"
"s_add_u32 s84, s84, 0x1000\n"
"buffer_load_dwordx4 a[192:195], v235, s[56:59], s84 offen sc1\n"
"v_mfma_f32_16x16x32_bf16 a[24:27], v[100:103], v[88:91], a[24:27]\n"
"v_exp_f32 v8, v8\n"
"v_mfma_f32_16x16x32_bf16 a[40:43], v[104:107], v[88:91], a[40:43]\n"
"buffer_load_dwordx4 a[196:199], v235, s[56:59], s84 offen offset:1024 sc1\n"
"v_mfma_f32_16x16x32_bf16 a[56:59], v[108:111], v[88:91], a[56:59]\n"
"v_exp_f32 v9, v9\n"
"v_mfma_f32_16x16x32_bf16 a[72:75], v[112:115], v[88:91], a[72:75]\n"
"ds_read_b128 a[160:163], v246 offset:3072\n"
"v_cvt_pk_bf16_f32 v66, v4, v5\n"
"v_mfma_f32_16x16x32_bf16 a[88:91], v[116:119], v[88:91], a[88:91]\n"
"v_exp_f32 v10, v10\n"
"v_mfma_f32_16x16x32_bf16 a[104:107], v[120:123], v[88:91], a[104:107]\n"
"ds_read_b128 a[164:167], v246 offset:7168\n"
"v_cvt_pk_bf16_f32 v67, v6, v7\n"
"v_mfma_f32_16x16x32_bf16 a[120:123], v[124:127], v[88:91], a[120:123]\n"
"v_exp_f32 v11, v11\n"
"v_mfma_f32_16x16x32_bf16 a[136:139], v[212:215], v[88:91], a[136:139]\n"
"ds_read_b128 a[168:171], v246 offset:11264\n"
"v_cvt_pk_bf16_f32 v68, v8, v9\n"
"v_mfma_f32_16x16x32_bf16 a[140:143], v[212:215], v[92:95], a[140:143]\n"
"v_exp_f32 v12, v12\n"
"v_mfma_f32_16x16x32_bf16 a[124:127], v[124:127], v[92:95], a[124:127]\n"
"ds_read_b128 a[172:175], v246 offset:15360\n"
"v_cvt_pk_bf16_f32 v69, v10, v11\n"
"v_mfma_f32_16x16x32_bf16 a[108:111], v[120:123], v[92:95], a[108:111]\n"
"v_exp_f32 v13, v13\n"
"v_mfma_f32_16x16x32_bf16 a[92:95], v[116:119], v[92:95], a[92:95]\n"
"v_exp_f32 v14, v14\n"
"v_mfma_f32_16x16x32_bf16 a[76:79], v[112:115], v[92:95], a[76:79]\n"
"v_cvt_pk_bf16_f32 v70, v12, v13\n"
"v_mfma_f32_16x16x32_bf16 a[60:63], v[108:111], v[92:95], a[60:63]\n"
"s_mov_b32 s84, 0\n"
"v_exp_f32 v15, v15\n"
"v_mfma_f32_16x16x32_bf16 a[44:47], v[104:107], v[92:95], a[44:47]\n"
"v_cvt_pk_bf16_f32 v71, v14, v15\n"
"v_mfma_f32_16x16x32_bf16 a[28:31], v[100:103], v[92:95], a[28:31]\n"
"v_exp_f32 v16, v16\n"
"v_mfma_f32_16x16x32_bf16 a[12:15], v[96:99], v[92:95], a[12:15]\n"
"s_waitcnt lgkmcnt(6)\n"
"buffer_load_dword v250, v219, s[28:31], s84 offen\n"
"v_mfma_f32_16x16x32_bf16 v[32:35], a[144:147], v[128:131], v[192:195]\n"
"ds_read_b64_tr_b16 v[96:97], v247 offset:2048\n"
"v_mfma_f32_16x16x32_bf16 v[32:35], a[148:151], v[132:135], v[32:35]\n"
"s_waitcnt lgkmcnt(5)\n"
"v_exp_f32 v17, v17\n"
"v_mfma_f32_16x16x32_bf16 v[32:35], a[152:155], v[136:139], v[32:35]\n"
"ds_read_b64_tr_b16 v[98:99], v247 offset:3072\n"
"v_cvt_pk_bf16_f32 v72, v16, v17\n"
"v_mfma_f32_16x16x32_bf16 v[32:35], a[156:159], v[140:143], v[32:35]\n"
"s_waitcnt lgkmcnt(4)\n"
"ds_read_b64_tr_b16 v[100:101], v248 offset:2048\n"
"v_mfma_f32_16x16x32_bf16 v[36:39], a[160:163], v[128:131], v[192:195]\n"
"v_exp_f32 v18, v18\n"
"v_mfma_f32_16x16x32_bf16 v[36:39], a[164:167], v[132:135], v[36:39]\n"
"s_waitcnt lgkmcnt(3)\n"
"ds_read_b64_tr_b16 v[102:103], v248 offset:3072\n"
"v_mfma_f32_16x16x32_bf16 v[36:39], a[168:171], v[136:139], v[36:39]\n"
"v_exp_f32 v19, v19\n"
"v_mfma_f32_16x16x32_bf16 v[36:39], a[172:175], v[140:143], v[36:39]\n"
"ds_read_b64_tr_b16 v[104:105], v247 offset:6144\n"
"v_cvt_pk_bf16_f32 v73, v18, v19\n"
"v_mfma_f32_16x16x32_bf16 v[40:43], a[144:147], v[144:147], v[196:199]\n"
"v_exp_f32 v20, v20\n"
"v_mfma_f32_16x16x32_bf16 v[40:43], a[148:151], v[148:151], v[40:43]\n"
"ds_read_b64_tr_b16 v[106:107], v247 offset:7168\n"
"v_mfma_f32_16x16x32_bf16 v[40:43], a[152:155], v[152:155], v[40:43]\n"
"v_exp_f32 v21, v21\n"
"v_mfma_f32_16x16x32_bf16 v[40:43], a[156:159], v[156:159], v[40:43]\n"
"ds_read_b64_tr_b16 v[108:109], v248 offset:6144\n"
"v_cvt_pk_bf16_f32 v74, v20, v21\n"
"v_mfma_f32_16x16x32_bf16 v[44:47], a[160:163], v[144:147], v[196:199]\n"
"v_exp_f32 v22, v22\n"
"v_mfma_f32_16x16x32_bf16 v[44:47], a[164:167], v[148:151], v[44:47]\n"
"ds_read_b64_tr_b16 v[110:111], v248 offset:7168\n"
"v_mfma_f32_16x16x32_bf16 v[44:47], a[168:171], v[152:155], v[44:47]\n"
"v_exp_f32 v23, v23\n"
"v_mfma_f32_16x16x32_bf16 v[44:47], a[172:175], v[156:159], v[44:47]\n"
"ds_read_b64_tr_b16 v[112:113], v247 offset:10240\n"
"v_cvt_pk_bf16_f32 v75, v22, v23\n"
"v_mfma_f32_16x16x32_bf16 v[48:51], a[144:147], v[160:163], v[200:203]\n"
"s_mov_b32 s84, 0x60000\n"
"ds_read_b64_tr_b16 v[114:115], v247 offset:11264\n"
"v_mfma_f32_16x16x32_bf16 v[48:51], a[148:151], v[164:167], v[48:51]\n"
"v_exp_f32 v24, v24\n"
"v_mfma_f32_16x16x32_bf16 v[48:51], a[152:155], v[168:171], v[48:51]\n"
"ds_read_b64_tr_b16 v[116:117], v248 offset:10240\n"
"ds_read_b64_tr_b16 v[118:119], v248 offset:11264\n"
"v_mfma_f32_16x16x32_bf16 v[48:51], a[156:159], v[172:175], v[48:51]\n"
"v_exp_f32 v25, v25\n"
"v_mfma_f32_16x16x32_bf16 v[52:55], a[160:163], v[160:163], v[200:203]\n"
"buffer_load_dword v250, v219, s[28:31], s84 offen\n"
"v_mfma_f32_16x16x32_bf16 v[52:55], a[164:167], v[164:167], v[52:55]\n"
"v_exp_f32 v26, v26\n"
"v_mfma_f32_16x16x32_bf16 v[52:55], a[168:171], v[168:171], v[52:55]\n"
"ds_read_b64_tr_b16 v[120:121], v247 offset:14336\n"
"v_cvt_pk_bf16_f32 v76, v24, v25\n"
"v_mfma_f32_16x16x32_bf16 v[52:55], a[172:175], v[172:175], v[52:55]\n"
"v_exp_f32 v27, v27\n"
"v_mfma_f32_16x16x32_bf16 v[56:59], a[144:147], v[176:179], v[204:207]\n"
"ds_read_b64_tr_b16 v[122:123], v247 offset:15360\n"
"ds_read_b64_tr_b16 v[124:125], v248 offset:14336\n"
"ds_read_b64_tr_b16 v[126:127], v248 offset:15360\n"
"v_mfma_f32_16x16x32_bf16 v[56:59], a[148:151], v[180:183], v[56:59]\n"
"v_exp_f32 v28, v28\n"
"v_mfma_f32_16x16x32_bf16 v[56:59], a[152:155], v[184:187], v[56:59]\n"
"v_cvt_pk_bf16_f32 v77, v26, v27\n"
"v_mfma_f32_16x16x32_bf16 v[56:59], a[156:159], v[188:191], v[56:59]\n"
"v_exp_f32 v29, v29\n"
"v_mfma_f32_16x16x32_bf16 v[60:63], a[160:163], v[176:179], v[204:207]\n"
"v_exp_f32 v30, v30\n"
"v_mfma_f32_16x16x32_bf16 v[60:63], a[164:167], v[180:183], v[60:63]\n"
"v_cvt_pk_bf16_f32 v78, v28, v29\n"
"v_mfma_f32_16x16x32_bf16 v[60:63], a[168:171], v[184:187], v[60:63]\n"
"v_exp_f32 v31, v31\n"
"v_mfma_f32_16x16x32_bf16 v[60:63], a[172:175], v[188:191], v[60:63]\n"
"s_waitcnt vmcnt(23) lgkmcnt(0)\n"
"s_barrier\n"
"v_cvt_pk_bf16_f32 v79, v30, v31\n"
"v_mfma_f32_16x16x32_bf16 a[0:3], v[96:99], v[64:67], a[0:3]\n"
"v_exp_f32 v32, v32\n"
"v_mfma_f32_16x16x32_bf16 a[16:19], v[100:103], v[64:67], a[16:19]\n"
"s_mov_b32 s90, 0x14000\n"
"ds_read_b128 a[144:147], v246 offset:32768\n"
"v_mfma_f32_16x16x32_bf16 a[32:35], v[104:107], v[64:67], a[32:35]\n"
"v_exp_f32 v33, v33\n"
"v_mfma_f32_16x16x32_bf16 a[48:51], v[108:111], v[64:67], a[48:51]\n"
"v_cvt_pk_bf16_f32 v80, v32, v33\n"
"v_mfma_f32_16x16x32_bf16 a[64:67], v[112:115], v[64:67], a[64:67]\n"
"s_add_u32 m0, s86, 0x18000\n"
"ds_read_b128 a[148:151], v246 offset:36864\n"
"v_mfma_f32_16x16x32_bf16 a[80:83], v[116:119], v[64:67], a[80:83]\n"
"v_exp_f32 v34, v34\n"
"v_mfma_f32_16x16x32_bf16 a[96:99], v[120:123], v[64:67], a[96:99]\n"
"v_exp_f32 v35, v35\n"
"v_mfma_f32_16x16x32_bf16 a[112:115], v[124:127], v[64:67], a[112:115]\n"
"ds_read_b128 a[152:155], v246 offset:40960\n"
"v_cvt_pk_bf16_f32 v81, v34, v35\n"
"v_mfma_f32_16x16x32_bf16 a[128:131], v[212:215], v[64:67], a[128:131]\n"
"v_exp_f32 v36, v36\n"
"v_mfma_f32_16x16x32_bf16 a[132:135], v[212:215], v[68:71], a[132:135]\n"
"buffer_load_dwordx4 v244, s[40:43], s90 offen lds\n"
"v_mfma_f32_16x16x32_bf16 a[116:119], v[124:127], v[68:71], a[116:119]\n"
"s_addk_i32 m0, 4032\n"
"ds_read_b128 a[156:159], v246 offset:45056\n"
"v_mfma_f32_16x16x32_bf16 a[100:103], v[120:123], v[68:71], a[100:103]\n"
"v_exp_f32 v37, v37\n"
"v_mfma_f32_16x16x32_bf16 a[84:87], v[116:119], v[68:71], a[84:87]\n"
"v_cvt_pk_bf16_f32 v82, v36, v37\n"
"v_mfma_f32_16x16x32_bf16 a[68:71], v[112:115], v[68:71], a[68:71]\n"
"v_exp_f32 v38, v38\n"
"v_mfma_f32_16x16x32_bf16 a[52:55], v[108:111], v[68:71], a[52:55]\n"
"ds_read_b128 a[160:163], v246 offset:33792\n"
"v_mfma_f32_16x16x32_bf16 a[36:39], v[104:107], v[68:71], a[36:39]\n"
"v_exp_f32 v39, v39\n"
"v_mfma_f32_16x16x32_bf16 a[20:23], v[100:103], v[68:71], a[20:23]\n"
"v_cvt_pk_bf16_f32 v83, v38, v39\n"
"v_mfma_f32_16x16x32_bf16 a[4:7], v[96:99], v[68:71], a[4:7]\n"
"buffer_load_dwordx4 v244, s[40:43], s90 offen offset:64 lds\n"
"v_mfma_f32_16x16x32_bf16 a[8:11], v[96:99], v[72:75], a[8:11]\n"
"s_addk_i32 m0, 4032\n"
"ds_read_b128 a[164:167], v246 offset:37888\n"
"v_mfma_f32_16x16x32_bf16 a[24:27], v[100:103], v[72:75], a[24:27]\n"
"v_exp_f32 v40, v40\n"
"v_mfma_f32_16x16x32_bf16 a[40:43], v[104:107], v[72:75], a[40:43]\n"
"v_exp_f32 v41, v41\n"
"v_mfma_f32_16x16x32_bf16 a[56:59], v[108:111], v[72:75], a[56:59]\n"
"ds_read_b128 a[168:171], v246 offset:41984\n"
"v_cvt_pk_bf16_f32 v84, v40, v41\n"
"v_mfma_f32_16x16x32_bf16 a[72:75], v[112:115], v[72:75], a[72:75]\n"
"v_exp_f32 v42, v42\n"
"v_mfma_f32_16x16x32_bf16 a[88:91], v[116:119], v[72:75], a[88:91]\n"
"buffer_load_dwordx4 v244, s[40:43], s90 offen offset:128 lds\n"
"v_mfma_f32_16x16x32_bf16 a[104:107], v[120:123], v[72:75], a[104:107]\n"
"s_addk_i32 m0, 4032\n"
"ds_read_b128 a[172:175], v246 offset:46080\n"
"v_mfma_f32_16x16x32_bf16 a[120:123], v[124:127], v[72:75], a[120:123]\n"
"v_exp_f32 v43, v43\n"
"v_mfma_f32_16x16x32_bf16 a[136:139], v[212:215], v[72:75], a[136:139]\n"
"v_exp_f32 v44, v44\n"
"v_mfma_f32_16x16x32_bf16 a[140:143], v[212:215], v[76:79], a[140:143]\n"
"v_cvt_pk_bf16_f32 v85, v42, v43\n"
"v_mfma_f32_16x16x32_bf16 a[124:127], v[124:127], v[76:79], a[124:127]\n"
"v_exp_f32 v45, v45\n"
"v_mfma_f32_16x16x32_bf16 a[108:111], v[120:123], v[76:79], a[108:111]\n"
"v_cvt_pk_bf16_f32 v86, v44, v45\n"
"v_mfma_f32_16x16x32_bf16 a[92:95], v[116:119], v[76:79], a[92:95]\n"
"buffer_load_dwordx4 v244, s[40:43], s90 offen offset:192 lds\n"
"v_mfma_f32_16x16x32_bf16 a[76:79], v[112:115], v[76:79], a[76:79]\n"
"s_addk_i32 m0, 4288\n"
"v_exp_f32 v46, v46\n"
"v_mfma_f32_16x16x32_bf16 a[60:63], v[108:111], v[76:79], a[60:63]\n"
"v_exp_f32 v47, v47\n"
"v_mfma_f32_16x16x32_bf16 a[44:47], v[104:107], v[76:79], a[44:47]\n"
"v_cvt_pk_bf16_f32 v87, v46, v47\n"
"v_mfma_f32_16x16x32_bf16 a[28:31], v[100:103], v[76:79], a[28:31]\n"
"v_exp_f32 v48, v48\n"
"v_mfma_f32_16x16x32_bf16 a[12:15], v[96:99], v[76:79], a[12:15]\n"
"s_nop 1\n"
"v_mov_b32 v124, 0xbdd2d3e8\n"
"v_mov_b32 v125, 0xc0135761\n"
"v_mov_b32 v126, 1.0\n"
"s_mov_b32 s83, s0\n"
"s_waitcnt vmcnt(6)\n"
"v_accvgpr_read_b32 v105, a176\n"
"v_accvgpr_read_b32 v107, a177\n"
"v_accvgpr_read_b32 v109, a178\n"
"v_accvgpr_read_b32 v111, a179\n"
"v_accvgpr_read_b32 v96, a208\n"
"v_accvgpr_read_b32 v97, a209\n"
"v_accvgpr_read_b32 v98, a210\n"
"v_accvgpr_read_b32 v99, a211\n"
"v_accvgpr_read_b32 v100, a212\n"
"v_accvgpr_read_b32 v101, a213\n"
"v_accvgpr_read_b32 v102, a214\n"
"v_accvgpr_read_b32 v103, a215\n"
"v_lshlrev_b32 v104, 16, v105\n"
"v_and_b32 v105, s14, v105\n"
"v_lshlrev_b32 v106, 16, v107\n"
"v_and_b32 v107, s14, v107\n"
"v_lshlrev_b32 v108, 16, v109\n"
"v_and_b32 v109, s14, v109\n"
"v_lshlrev_b32 v110, 16, v111\n"
"v_and_b32 v111, s14, v111\n"
"v_pk_add_f32 v[96:97], v[96:97], v[104:105]\n"
"v_pk_add_f32 v[98:99], v[98:99], v[106:107]\n"
"v_pk_add_f32 v[100:101], v[100:101], v[108:109]\n"
"v_pk_add_f32 v[102:103], v[102:103], v[110:111]\n"
"v_pk_mul_f32 v[104:105], v[96:97], v[96:97]\n"
"v_pk_mul_f32 v[106:107], v[98:99], v[98:99]\n"
"v_pk_mul_f32 v[108:109], v[100:101], v[100:101]\n"
"v_pk_mul_f32 v[110:111], v[102:103], v[102:103]\n"
"v_pk_fma_f32 v[104:105], v[104:105], v[124:125], v[124:125] op_sel:[0,0,1] op_sel_hi:[1,0,1]\n"
"v_pk_fma_f32 v[106:107], v[106:107], v[124:125], v[124:125] op_sel:[0,0,1] op_sel_hi:[1,0,1]\n"
"v_pk_fma_f32 v[108:109], v[108:109], v[124:125], v[124:125] op_sel:[0,0,1] op_sel_hi:[1,0,1]\n"
"v_pk_fma_f32 v[110:111], v[110:111], v[124:125], v[124:125] op_sel:[0,0,1] op_sel_hi:[1,0,1]\n"
"v_pk_mul_f32 v[104:105], v[104:105], v[96:97]\n"
"v_pk_mul_f32 v[106:107], v[106:107], v[98:99]\n"
"v_pk_mul_f32 v[108:109], v[108:109], v[100:101]\n"
"v_pk_mul_f32 v[110:111], v[110:111], v[102:103]\n"
"v_exp_f32 v104, v104\n"
"v_exp_f32 v105, v105\n"
"v_exp_f32 v106, v106\n"
"v_exp_f32 v107, v107\n"
"v_exp_f32 v108, v108\n"
"v_exp_f32 v109, v109\n"
"v_exp_f32 v110, v110\n"
"v_exp_f32 v111, v111\n"
"v_pk_add_f32 v[104:105], v[104:105], v[126:127] op_sel_hi:[1,0]\n"
"v_pk_add_f32 v[106:107], v[106:107], v[126:127] op_sel_hi:[1,0]\n"
"v_pk_add_f32 v[108:109], v[108:109], v[126:127] op_sel_hi:[1,0]\n"
"v_pk_add_f32 v[110:111], v[110:111], v[126:127] op_sel_hi:[1,0]\n"
"v_rcp_f32 v104, v104\n"
"v_rcp_f32 v105, v105\n"
"v_rcp_f32 v106, v106\n"
"v_rcp_f32 v107, v107\n"
"v_rcp_f32 v108, v108\n"
"v_rcp_f32 v109, v109\n"
"v_rcp_f32 v110, v110\n"
"v_rcp_f32 v111, v111\n"
"v_pk_mul_f32 v[104:105], v[104:105], v[96:97]\n"
"v_pk_mul_f32 v[106:107], v[106:107], v[98:99]\n"
"v_pk_mul_f32 v[108:109], v[108:109], v[100:101]\n"
"v_pk_mul_f32 v[110:111], v[110:111], v[102:103]\n"
"v_cvt_pk_bf16_f32 v112, v104, v105\n"
"v_cvt_pk_bf16_f32 v113, v106, v107\n"
"v_cvt_pk_bf16_f32 v114, v108, v109\n"
"v_cvt_pk_bf16_f32 v115, v110, v111\n"
"buffer_store_dwordx4 v[112:115], v220, s[56:59], s83 offen sc1\n"
"v_accvgpr_read_b32 v105, a180\n"
"v_accvgpr_read_b32 v107, a181\n"
"v_accvgpr_read_b32 v109, a182\n"
"v_accvgpr_read_b32 v111, a183\n"
"v_accvgpr_read_b32 v96, a216\n"
"v_accvgpr_read_b32 v97, a217\n"
"v_accvgpr_read_b32 v98, a218\n"
"v_accvgpr_read_b32 v99, a219\n"
"v_accvgpr_read_b32 v100, a220\n"
"v_accvgpr_read_b32 v101, a221\n"
"v_accvgpr_read_b32 v102, a222\n"
"v_accvgpr_read_b32 v103, a223\n"
"v_lshlrev_b32 v104, 16, v105\n"
"v_and_b32 v105, s14, v105\n"
"v_lshlrev_b32 v106, 16, v107\n"
"v_and_b32 v107, s14, v107\n"
"v_lshlrev_b32 v108, 16, v109\n"
"v_and_b32 v109, s14, v109\n"
"v_lshlrev_b32 v110, 16, v111\n"
"v_and_b32 v111, s14, v111\n"
"v_pk_add_f32 v[96:97], v[96:97], v[104:105]\n"
"v_pk_add_f32 v[98:99], v[98:99], v[106:107]\n"
"v_pk_add_f32 v[100:101], v[100:101], v[108:109]\n"
"v_pk_add_f32 v[102:103], v[102:103], v[110:111]\n"
"v_pk_mul_f32 v[104:105], v[96:97], v[96:97]\n"
"v_pk_mul_f32 v[106:107], v[98:99], v[98:99]\n"
"v_pk_mul_f32 v[108:109], v[100:101], v[100:101]\n"
"v_pk_mul_f32 v[110:111], v[102:103], v[102:103]\n"
"v_pk_fma_f32 v[104:105], v[104:105], v[124:125], v[124:125] op_sel:[0,0,1] op_sel_hi:[1,0,1]\n"
"v_pk_fma_f32 v[106:107], v[106:107], v[124:125], v[124:125] op_sel:[0,0,1] op_sel_hi:[1,0,1]\n"
"v_pk_fma_f32 v[108:109], v[108:109], v[124:125], v[124:125] op_sel:[0,0,1] op_sel_hi:[1,0,1]\n"
"v_pk_fma_f32 v[110:111], v[110:111], v[124:125], v[124:125] op_sel:[0,0,1] op_sel_hi:[1,0,1]\n"
"v_pk_mul_f32 v[104:105], v[104:105], v[96:97]\n"
"v_pk_mul_f32 v[106:107], v[106:107], v[98:99]\n"
"v_pk_mul_f32 v[108:109], v[108:109], v[100:101]\n"
"v_pk_mul_f32 v[110:111], v[110:111], v[102:103]\n"
"v_exp_f32 v104, v104\n"
"v_exp_f32 v105, v105\n"
"v_exp_f32 v106, v106\n"
"v_exp_f32 v107, v107\n"
"v_exp_f32 v108, v108\n"
"v_exp_f32 v109, v109\n"
"v_exp_f32 v110, v110\n"
"v_exp_f32 v111, v111\n"
"v_pk_add_f32 v[104:105], v[104:105], v[126:127] op_sel_hi:[1,0]\n"
"v_pk_add_f32 v[106:107], v[106:107], v[126:127] op_sel_hi:[1,0]\n"
"v_pk_add_f32 v[108:109], v[108:109], v[126:127] op_sel_hi:[1,0]\n"
"v_pk_add_f32 v[110:111], v[110:111], v[126:127] op_sel_hi:[1,0]\n"
"v_rcp_f32 v104, v104\n"
"v_rcp_f32 v105, v105\n"
"v_rcp_f32 v106, v106\n"
"v_rcp_f32 v107, v107\n"
"v_rcp_f32 v108, v108\n"
"v_rcp_f32 v109, v109\n"
"v_rcp_f32 v110, v110\n"
"v_rcp_f32 v111, v111\n"
"v_pk_mul_f32 v[104:105], v[104:105], v[96:97]\n"
"v_pk_mul_f32 v[106:107], v[106:107], v[98:99]\n"
"v_pk_mul_f32 v[108:109], v[108:109], v[100:101]\n"
"v_pk_mul_f32 v[110:111], v[110:111], v[102:103]\n"
"v_cvt_pk_bf16_f32 v116, v104, v105\n"
"v_cvt_pk_bf16_f32 v117, v106, v107\n"
"v_cvt_pk_bf16_f32 v118, v108, v109\n"
"v_cvt_pk_bf16_f32 v119, v110, v111\n"
"buffer_store_dwordx4 v[116:119], v220, s[56:59], s83 offen offset:2048 sc1\n"
"v_accvgpr_read_b32 v105, a184\n"
"v_accvgpr_read_b32 v107, a185\n"
"v_accvgpr_read_b32 v109, a186\n"
"v_accvgpr_read_b32 v111, a187\n"
"v_accvgpr_read_b32 v96, a224\n"
"v_accvgpr_read_b32 v97, a225\n"
"v_accvgpr_read_b32 v98, a226\n"
"v_accvgpr_read_b32 v99, a227\n"
"v_accvgpr_read_b32 v100, a228\n"
"v_accvgpr_read_b32 v101, a229\n"
"v_accvgpr_read_b32 v102, a230\n"
"v_accvgpr_read_b32 v103, a231\n"
"v_lshlrev_b32 v104, 16, v105\n"
"v_and_b32 v105, s14, v105\n"
"v_lshlrev_b32 v106, 16, v107\n"
"v_and_b32 v107, s14, v107\n"
"v_lshlrev_b32 v108, 16, v109\n"
"v_and_b32 v109, s14, v109\n"
"v_lshlrev_b32 v110, 16, v111\n"
"v_and_b32 v111, s14, v111\n"
"v_pk_add_f32 v[96:97], v[96:97], v[104:105]\n"
"v_pk_add_f32 v[98:99], v[98:99], v[106:107]\n"
"v_pk_add_f32 v[100:101], v[100:101], v[108:109]\n"
"v_pk_add_f32 v[102:103], v[102:103], v[110:111]\n"
"v_pk_mul_f32 v[104:105], v[96:97], v[96:97]\n"
"v_pk_mul_f32 v[106:107], v[98:99], v[98:99]\n"
"v_pk_mul_f32 v[108:109], v[100:101], v[100:101]\n"
"v_pk_mul_f32 v[110:111], v[102:103], v[102:103]\n"
"v_pk_fma_f32 v[104:105], v[104:105], v[124:125], v[124:125] op_sel:[0,0,1] op_sel_hi:[1,0,1]\n"
"v_pk_fma_f32 v[106:107], v[106:107], v[124:125], v[124:125] op_sel:[0,0,1] op_sel_hi:[1,0,1]\n"
"v_pk_fma_f32 v[108:109], v[108:109], v[124:125], v[124:125] op_sel:[0,0,1] op_sel_hi:[1,0,1]\n"
"v_pk_fma_f32 v[110:111], v[110:111], v[124:125], v[124:125] op_sel:[0,0,1] op_sel_hi:[1,0,1]\n"
"v_pk_mul_f32 v[104:105], v[104:105], v[96:97]\n"
"v_pk_mul_f32 v[106:107], v[106:107], v[98:99]\n"
"v_pk_mul_f32 v[108:109], v[108:109], v[100:101]\n"
"v_pk_mul_f32 v[110:111], v[110:111], v[102:103]\n"
"v_exp_f32 v104, v104\n"
"v_exp_f32 v105, v105\n"
"v_exp_f32 v106, v106\n"
"v_exp_f32 v107, v107\n"
"v_exp_f32 v108, v108\n"
"v_exp_f32 v109, v109\n"
"v_exp_f32 v110, v110\n"
"v_exp_f32 v111, v111\n"
"v_pk_add_f32 v[104:105], v[104:105], v[126:127] op_sel_hi:[1,0]\n"
"v_pk_add_f32 v[106:107], v[106:107], v[126:127] op_sel_hi:[1,0]\n"
"v_pk_add_f32 v[108:109], v[108:109], v[126:127] op_sel_hi:[1,0]\n"
"v_pk_add_f32 v[110:111], v[110:111], v[126:127] op_sel_hi:[1,0]\n"
"v_rcp_f32 v104, v104\n"
"v_rcp_f32 v105, v105\n"
"v_rcp_f32 v106, v106\n"
"v_rcp_f32 v107, v107\n"
"v_rcp_f32 v108, v108\n"
"v_rcp_f32 v109, v109\n"
"v_rcp_f32 v110, v110\n"
"v_rcp_f32 v111, v111\n"
"v_pk_mul_f32 v[104:105], v[104:105], v[96:97]\n"
"v_pk_mul_f32 v[106:107], v[106:107], v[98:99]\n"
"v_pk_mul_f32 v[108:109], v[108:109], v[100:101]\n"
"v_pk_mul_f32 v[110:111], v[110:111], v[102:103]\n"
"v_cvt_pk_bf16_f32 v120, v104, v105\n"
"v_cvt_pk_bf16_f32 v121, v106, v107\n"
"v_cvt_pk_bf16_f32 v122, v108, v109\n"
"s_add_u32 s83, s83, 0x1000\n"
"v_cvt_pk_bf16_f32 v123, v110, v111\n"
"buffer_store_dwordx4 v[120:123], v220, s[56:59], s83 offen sc1\n"
"v_accvgpr_read_b32 v105, a188\n"
"v_accvgpr_read_b32 v107, a189\n"
"v_accvgpr_read_b32 v109, a190\n"
"v_accvgpr_read_b32 v111, a191\n"
"v_accvgpr_read_b32 v96, a232\n"
"v_accvgpr_read_b32 v97, a233\n"
"v_accvgpr_read_b32 v98, a234\n"
"v_accvgpr_read_b32 v99, a235\n"
"v_accvgpr_read_b32 v100, a236\n"
"v_accvgpr_read_b32 v101, a237\n"
"v_accvgpr_read_b32 v102, a238\n"
"v_accvgpr_read_b32 v103, a239\n"
"v_lshlrev_b32 v104, 16, v105\n"
"v_and_b32 v105, s14, v105\n"
"v_lshlrev_b32 v106, 16, v107\n"
"v_and_b32 v107, s14, v107\n"
"v_lshlrev_b32 v108, 16, v109\n"
"v_and_b32 v109, s14, v109\n"
"v_lshlrev_b32 v110, 16, v111\n"
"v_and_b32 v111, s14, v111\n"
"v_pk_add_f32 v[96:97], v[96:97], v[104:105]\n"
"v_pk_add_f32 v[98:99], v[98:99], v[106:107]\n"
"v_pk_add_f32 v[100:101], v[100:101], v[108:109]\n"
"v_pk_add_f32 v[102:103], v[102:103], v[110:111]\n"
"v_pk_mul_f32 v[104:105], v[96:97], v[96:97]\n"
"v_pk_mul_f32 v[106:107], v[98:99], v[98:99]\n"
"v_pk_mul_f32 v[108:109], v[100:101], v[100:101]\n"
"v_pk_mul_f32 v[110:111], v[102:103], v[102:103]\n"
"v_pk_fma_f32 v[104:105], v[104:105], v[124:125], v[124:125] op_sel:[0,0,1] op_sel_hi:[1,0,1]\n"
"v_pk_fma_f32 v[106:107], v[106:107], v[124:125], v[124:125] op_sel:[0,0,1] op_sel_hi:[1,0,1]\n"
"v_pk_fma_f32 v[108:109], v[108:109], v[124:125], v[124:125] op_sel:[0,0,1] op_sel_hi:[1,0,1]\n"
"v_pk_fma_f32 v[110:111], v[110:111], v[124:125], v[124:125] op_sel:[0,0,1] op_sel_hi:[1,0,1]\n"
"v_pk_mul_f32 v[104:105], v[104:105], v[96:97]\n"
"v_pk_mul_f32 v[106:107], v[106:107], v[98:99]\n"
"v_pk_mul_f32 v[108:109], v[108:109], v[100:101]\n"
"v_pk_mul_f32 v[110:111], v[110:111], v[102:103]\n"
"v_exp_f32 v104, v104\n"
"v_exp_f32 v105, v105\n"
"v_exp_f32 v106, v106\n"
"v_exp_f32 v107, v107\n"
"v_exp_f32 v108, v108\n"
"v_exp_f32 v109, v109\n"
"v_exp_f32 v110, v110\n"
"v_exp_f32 v111, v111\n"
"v_pk_add_f32 v[104:105], v[104:105], v[126:127] op_sel_hi:[1,0]\n"
"v_pk_add_f32 v[106:107], v[106:107], v[126:127] op_sel_hi:[1,0]\n"
"v_pk_add_f32 v[108:109], v[108:109], v[126:127] op_sel_hi:[1,0]\n"
"v_pk_add_f32 v[110:111], v[110:111], v[126:127] op_sel_hi:[1,0]\n"
"v_rcp_f32 v104, v104\n"
"v_rcp_f32 v105, v105\n"
"v_rcp_f32 v106, v106\n"
"v_rcp_f32 v107, v107\n"
"v_rcp_f32 v108, v108\n"
"v_rcp_f32 v109, v109\n"
"v_rcp_f32 v110, v110\n"
"v_rcp_f32 v111, v111\n"
"v_pk_mul_f32 v[104:105], v[104:105], v[96:97]\n"
"v_pk_mul_f32 v[106:107], v[106:107], v[98:99]\n"
"v_pk_mul_f32 v[108:109], v[108:109], v[100:101]\n"
"v_pk_mul_f32 v[110:111], v[110:111], v[102:103]\n"
"v_cvt_pk_bf16_f32 v112, v104, v105\n"
"v_cvt_pk_bf16_f32 v113, v106, v107\n"
"v_cvt_pk_bf16_f32 v114, v108, v109\n"
"v_cvt_pk_bf16_f32 v115, v110, v111\n"
"buffer_store_dwordx4 v[112:115], v220, s[56:59], s83 offen offset:2048 sc1\n"
"v_accvgpr_read_b32 v105, a192\n"
"v_accvgpr_read_b32 v107, a193\n"
"v_accvgpr_read_b32 v109, a194\n"
"v_accvgpr_read_b32 v111, a195\n"
"v_accvgpr_read_b32 v96, a240\n"
"v_accvgpr_read_b32 v97, a241\n"
"v_accvgpr_read_b32 v98, a242\n"
"v_accvgpr_read_b32 v99, a243\n"
"v_accvgpr_read_b32 v100, a244\n"
"v_accvgpr_read_b32 v101, a245\n"
"v_accvgpr_read_b32 v102, a246\n"
"v_accvgpr_read_b32 v103, a247\n"
"v_lshlrev_b32 v104, 16, v105\n"
"v_and_b32 v105, s14, v105\n"
"v_lshlrev_b32 v106, 16, v107\n"
"v_and_b32 v107, s14, v107\n"
"v_lshlrev_b32 v108, 16, v109\n"
"v_and_b32 v109, s14, v109\n"
"v_lshlrev_b32 v110, 16, v111\n"
"v_and_b32 v111, s14, v111\n"
"v_pk_add_f32 v[96:97], v[96:97], v[104:105]\n"
"v_pk_add_f32 v[98:99], v[98:99], v[106:107]\n"
"v_pk_add_f32 v[100:101], v[100:101], v[108:109]\n"
"v_pk_add_f32 v[102:103], v[102:103], v[110:111]\n"
"v_pk_mul_f32 v[104:105], v[96:97], v[96:97]\n"
"v_pk_mul_f32 v[106:107], v[98:99], v[98:99]\n"
"v_pk_mul_f32 v[108:109], v[100:101], v[100:101]\n"
"v_pk_mul_f32 v[110:111], v[102:103], v[102:103]\n"
"v_pk_fma_f32 v[104:105], v[104:105], v[124:125], v[124:125] op_sel:[0,0,1] op_sel_hi:[1,0,1]\n"
"v_pk_fma_f32 v[106:107], v[106:107], v[124:125], v[124:125] op_sel:[0,0,1] op_sel_hi:[1,0,1]\n"
"v_pk_fma_f32 v[108:109], v[108:109], v[124:125], v[124:125] op_sel:[0,0,1] op_sel_hi:[1,0,1]\n"
"v_pk_fma_f32 v[110:111], v[110:111], v[124:125], v[124:125] op_sel:[0,0,1] op_sel_hi:[1,0,1]\n"
"v_pk_mul_f32 v[104:105], v[104:105], v[96:97]\n"
"v_pk_mul_f32 v[106:107], v[106:107], v[98:99]\n"
"v_pk_mul_f32 v[108:109], v[108:109], v[100:101]\n"
"v_pk_mul_f32 v[110:111], v[110:111], v[102:103]\n"
"v_exp_f32 v104, v104\n"
"v_exp_f32 v105, v105\n"
"v_exp_f32 v106, v106\n"
"v_exp_f32 v107, v107\n"
"v_exp_f32 v108, v108\n"
"v_exp_f32 v109, v109\n"
"v_exp_f32 v110, v110\n"
"v_exp_f32 v111, v111\n"
"v_pk_add_f32 v[104:105], v[104:105], v[126:127] op_sel_hi:[1,0]\n"
"v_pk_add_f32 v[106:107], v[106:107], v[126:127] op_sel_hi:[1,0]\n"
"v_pk_add_f32 v[108:109], v[108:109], v[126:127] op_sel_hi:[1,0]\n"
"v_pk_add_f32 v[110:111], v[110:111], v[126:127] op_sel_hi:[1,0]\n"
"v_rcp_f32 v104, v104\n"
"v_rcp_f32 v105, v105\n"
"v_rcp_f32 v106, v106\n"
"v_rcp_f32 v107, v107\n"
"v_rcp_f32 v108, v108\n"
"v_rcp_f32 v109, v109\n"
"v_rcp_f32 v110, v110\n"
"v_rcp_f32 v111, v111\n"
"v_pk_mul_f32 v[104:105], v[104:105], v[96:97]\n"
"v_pk_mul_f32 v[106:107], v[106:107], v[98:99]\n"
"v_pk_mul_f32 v[108:109], v[108:109], v[100:101]\n"
"v_pk_mul_f32 v[110:111], v[110:111], v[102:103]\n"
"v_cvt_pk_bf16_f32 v116, v104, v105\n"
"v_cvt_pk_bf16_f32 v117, v106, v107\n"
"v_cvt_pk_bf16_f32 v118, v108, v109\n"
"s_add_u32 s83, s83, 0x1000\n"
"v_cvt_pk_bf16_f32 v119, v110, v111\n"
"buffer_store_dwordx4 v[116:119], v220, s[56:59], s83 offen sc1\n"
"v_accvgpr_read_b32 v105, a196\n"
"v_accvgpr_read_b32 v107, a197\n"
"v_accvgpr_read_b32 v109, a198\n"
"v_accvgpr_read_b32 v111, a199\n"
"v_accvgpr_read_b32 v96, a248\n"
"v_accvgpr_read_b32 v97, a249\n"
"v_accvgpr_read_b32 v98, a250\n"
"v_accvgpr_read_b32 v99, a251\n"
"v_accvgpr_read_b32 v100, a252\n"
"v_accvgpr_read_b32 v101, a253\n"
"v_accvgpr_read_b32 v102, a254\n"
"v_accvgpr_read_b32 v103, a255\n"
"v_lshlrev_b32 v104, 16, v105\n"
"v_and_b32 v105, s14, v105\n"
"v_lshlrev_b32 v106, 16, v107\n"
"v_and_b32 v107, s14, v107\n"
"v_lshlrev_b32 v108, 16, v109\n"
"v_and_b32 v109, s14, v109\n"
"v_lshlrev_b32 v110, 16, v111\n"
"v_and_b32 v111, s14, v111\n"
"v_pk_add_f32 v[96:97], v[96:97], v[104:105]\n"
"v_pk_add_f32 v[98:99], v[98:99], v[106:107]\n"
"v_pk_add_f32 v[100:101], v[100:101], v[108:109]\n"
"v_pk_add_f32 v[102:103], v[102:103], v[110:111]\n"
"v_pk_mul_f32 v[104:105], v[96:97], v[96:97]\n"
"v_pk_mul_f32 v[106:107], v[98:99], v[98:99]\n"
"v_pk_mul_f32 v[108:109], v[100:101], v[100:101]\n"
"v_pk_mul_f32 v[110:111], v[102:103], v[102:103]\n"
"v_pk_fma_f32 v[104:105], v[104:105], v[124:125], v[124:125] op_sel:[0,0,1] op_sel_hi:[1,0,1]\n"
"v_pk_fma_f32 v[106:107], v[106:107], v[124:125], v[124:125] op_sel:[0,0,1] op_sel_hi:[1,0,1]\n"
"v_pk_fma_f32 v[108:109], v[108:109], v[124:125], v[124:125] op_sel:[0,0,1] op_sel_hi:[1,0,1]\n"
"v_pk_fma_f32 v[110:111], v[110:111], v[124:125], v[124:125] op_sel:[0,0,1] op_sel_hi:[1,0,1]\n"
"v_pk_mul_f32 v[104:105], v[104:105], v[96:97]\n"
"v_pk_mul_f32 v[106:107], v[106:107], v[98:99]\n"
"v_pk_mul_f32 v[108:109], v[108:109], v[100:101]\n"
"v_pk_mul_f32 v[110:111], v[110:111], v[102:103]\n"
"v_exp_f32 v104, v104\n"
"v_exp_f32 v105, v105\n"
"v_exp_f32 v106, v106\n"
"v_exp_f32 v107, v107\n"
"v_exp_f32 v108, v108\n"
"v_exp_f32 v109, v109\n"
"v_exp_f32 v110, v110\n"
"v_exp_f32 v111, v111\n"
"v_pk_add_f32 v[104:105], v[104:105], v[126:127] op_sel_hi:[1,0]\n"
"v_pk_add_f32 v[106:107], v[106:107], v[126:127] op_sel_hi:[1,0]\n"
"v_pk_add_f32 v[108:109], v[108:109], v[126:127] op_sel_hi:[1,0]\n"
"v_pk_add_f32 v[110:111], v[110:111], v[126:127] op_sel_hi:[1,0]\n"
"v_rcp_f32 v104, v104\n"
"v_rcp_f32 v105, v105\n"
"v_rcp_f32 v106, v106\n"
"v_rcp_f32 v107, v107\n"
"v_rcp_f32 v108, v108\n"
"v_rcp_f32 v109, v109\n"
"v_rcp_f32 v110, v110\n"
"v_rcp_f32 v111, v111\n"
"v_pk_mul_f32 v[104:105], v[104:105], v[96:97]\n"
"v_pk_mul_f32 v[106:107], v[106:107], v[98:99]\n"
"v_pk_mul_f32 v[108:109], v[108:109], v[100:101]\n"
"v_pk_mul_f32 v[110:111], v[110:111], v[102:103]\n"
"v_cvt_pk_bf16_f32 v120, v104, v105\n"
"v_cvt_pk_bf16_f32 v121, v106, v107\n"
"v_cvt_pk_bf16_f32 v122, v108, v109\n"
"v_cvt_pk_bf16_f32 v123, v110, v111\n"
"buffer_store_dwordx4 v[120:123], v220, s[56:59], s83 offen offset:2048 sc1\n"
"s_nop 1\n"
"s_waitcnt lgkmcnt(6)\n"
"v_mfma_f32_16x16x32_bf16 v[0:3], a[144:147], v[128:131], v[192:195]\n"
"buffer_load_dwordx4 v245, s[44:47], s90 offen lds\n"
"v_mfma_f32_16x16x32_bf16 v[0:3], a[148:151], v[132:135], v[0:3]\n"
"s_waitcnt lgkmcnt(4)\n"
"ds_read_b64_tr_b16 v[96:97], v247 offset:32768\n"
"v_mfma_f32_16x16x32_bf16 v[0:3], a[152:155], v[136:139], v[0:3]\n"
"s_addk_i32 m0, 4032\n"
"v_exp_f32 v49, v49\n"
"v_mfma_f32_16x16x32_bf16 v[0:3], a[156:159], v[140:143], v[0:3]\n"
"s_waitcnt lgkmcnt(3)\n"
"ds_read_b64_tr_b16 v[98:99], v247 offset:33792\n"
"v_cvt_pk_bf16_f32 v88, v48, v49\n"
"v_mfma_f32_16x16x32_bf16 v[4:7], a[160:163], v[128:131], v[192:195]\n"
"v_exp_f32 v50, v50\n"
"v_mfma_f32_16x16x32_bf16 v[4:7], a[164:167], v[132:135], v[4:7]\n"
"s_waitcnt lgkmcnt(2)\n"
"ds_read_b64_tr_b16 v[100:101], v248 offset:32768\n"
"v_mfma_f32_16x16x32_bf16 v[4:7], a[168:171], v[136:139], v[4:7]\n"
"v_exp_f32 v51, v51\n"
"v_mfma_f32_16x16x32_bf16 v[4:7], a[172:175], v[140:143], v[4:7]\n"
"buffer_load_dwordx4 v245, s[44:47], s90 offen offset:64 lds\n"
"v_mfma_f32_16x16x32_bf16 v[8:11], a[144:147], v[144:147], v[196:199]\n"
"s_addk_i32 m0, 4032\n"
"v_exp_f32 v52, v52\n"
"v_mfma_f32_16x16x32_bf16 v[8:11], a[148:151], v[148:151], v[8:11]\n"
"ds_read_b64_tr_b16 v[102:103], v248 offset:33792\n"
"v_cvt_pk_bf16_f32 v89, v50, v51\n"
"v_mfma_f32_16x16x32_bf16 v[8:11], a[152:155], v[152:155], v[8:11]\n"
"v_exp_f32 v53, v53\n"
"v_mfma_f32_16x16x32_bf16 v[8:11], a[156:159], v[156:159], v[8:11]\n"
"ds_read_b64_tr_b16 v[104:105], v247 offset:36864\n"
"v_cvt_pk_bf16_f32 v90, v52, v53\n"
"v_mfma_f32_16x16x32_bf16 v[12:15], a[160:163], v[144:147], v[196:199]\n"
"v_exp_f32 v54, v54\n"
"v_mfma_f32_16x16x32_bf16 v[12:15], a[164:167], v[148:151], v[12:15]\n"
"ds_read_b64_tr_b16 v[106:107], v247 offset:37888\n"
"v_mfma_f32_16x16x32_bf16 v[12:15], a[168:171], v[152:155], v[12:15]\n"
"v_exp_f32 v55, v55\n"
"v_mfma_f32_16x16x32_bf16 v[12:15], a[172:175], v[156:159], v[12:15]\n"
"buffer_load_dwordx4 v245, s[44:47], s90 offen offset:128 lds\n"
"v_mfma_f32_16x16x32_bf16 v[16:19], a[144:147], v[160:163], v[200:203]\n"
"s_addk_i32 m0, 4032\n"
"ds_read_b64_tr_b16 v[108:109], v248 offset:36864\n"
"v_cvt_pk_bf16_f32 v91, v54, v55\n"
"v_mfma_f32_16x16x32_bf16 v[16:19], a[148:151], v[164:167], v[16:19]\n"
"v_exp_f32 v56, v56\n"
"v_mfma_f32_16x16x32_bf16 v[16:19], a[152:155], v[168:171], v[16:19]\n"
"ds_read_b64_tr_b16 v[110:111], v248 offset:37888\n"
"ds_read_b64_tr_b16 v[112:113], v247 offset:40960\n"
"v_mfma_f32_16x16x32_bf16 v[16:19], a[156:159], v[172:175], v[16:19]\n"
"v_exp_f32 v57, v57\n"
"v_mfma_f32_16x16x32_bf16 v[20:23], a[160:163], v[160:163], v[200:203]\n"
"buffer_load_dwordx4 v245, s[44:47], s90 offen offset:192 lds\n"
"v_mfma_f32_16x16x32_bf16 v[20:23], a[164:167], v[164:167], v[20:23]\n"
"v_exp_f32 v58, v58\n"
"v_mfma_f32_16x16x32_bf16 v[20:23], a[168:171], v[168:171], v[20:23]\n"
"ds_read_b64_tr_b16 v[114:115], v247 offset:41984\n"
"v_cvt_pk_bf16_f32 v92, v56, v57\n"
"v_mfma_f32_16x16x32_bf16 v[20:23], a[172:175], v[172:175], v[20:23]\n"
"v_exp_f32 v59, v59\n"
"v_mfma_f32_16x16x32_bf16 v[24:27], a[144:147], v[176:179], v[204:207]\n"
"ds_read_b64_tr_b16 v[116:117], v248 offset:40960\n"
"v_cvt_pk_bf16_f32 v93, v58, v59\n"
"v_mfma_f32_16x16x32_bf16 v[24:27], a[148:151], v[180:183], v[24:27]\n"
"v_exp_f32 v60, v60\n"
"v_mfma_f32_16x16x32_bf16 v[24:27], a[152:155], v[184:187], v[24:27]\n"
"ds_read_b64_tr_b16 v[118:119], v248 offset:41984\n"
"ds_read_b64_tr_b16 v[120:121], v247 offset:45056\n"
"v_mfma_f32_16x16x32_bf16 v[24:27], a[156:159], v[188:191], v[24:27]\n"
"v_exp_f32 v61, v61\n"
"v_mfma_f32_16x16x32_bf16 v[28:31], a[160:163], v[176:179], v[204:207]\n"
"ds_read_b64_tr_b16 v[122:123], v247 offset:46080\n"
"ds_read_b64_tr_b16 v[124:125], v248 offset:45056\n"
"ds_read_b64_tr_b16 v[126:127], v248 offset:46080\n"
"v_mfma_f32_16x16x32_bf16 v[28:31], a[164:167], v[180:183], v[28:31]\n"
"v_exp_f32 v62, v62\n"
"v_mfma_f32_16x16x32_bf16 v[28:31], a[168:171], v[184:187], v[28:31]\n"
"v_cvt_pk_bf16_f32 v94, v60, v61\n"
"v_mfma_f32_16x16x32_bf16 v[28:31], a[172:175], v[188:191], v[28:31]\n"
"s_waitcnt lgkmcnt(12)\n"
"v_exp_f32 v63, v63\n"
"v_mfma_f32_16x16x32_bf16 a[0:3], v[96:99], v[80:83], a[0:3]\n"
"v_cvt_pk_bf16_f32 v95, v62, v63\n"
"v_mfma_f32_16x16x32_bf16 a[16:19], v[100:103], v[80:83], a[16:19]\n"
"s_waitcnt lgkmcnt(8)\n"
"v_exp_f32 v0, v0\n"
"v_mfma_f32_16x16x32_bf16 a[32:35], v[104:107], v[80:83], a[32:35]\n"
"ds_read_b128 a[144:147], v246 offset:34816\n"
"v_mfma_f32_16x16x32_bf16 a[48:51], v[108:111], v[80:83], a[48:51]\n"
"s_waitcnt lgkmcnt(5)\n"
"v_exp_f32 v1, v1\n"
"v_mfma_f32_16x16x32_bf16 a[64:67], v[112:115], v[80:83], a[64:67]\n"
"s_mov_b32 s84, 0xc0000\n"
"ds_read_b128 a[148:151], v246 offset:38912\n"
"v_cvt_pk_bf16_f32 v64, v0, v1\n"
"v_mfma_f32_16x16x32_bf16 a[80:83], v[116:119], v[80:83], a[80:83]\n"
"s_waitcnt lgkmcnt(2)\n"
"v_exp_f32 v2, v2\n"
"v_mfma_f32_16x16x32_bf16 a[96:99], v[120:123], v[80:83], a[96:99]\n"
"v_exp_f32 v3, v3\n"
"v_mfma_f32_16x16x32_bf16 a[112:115], v[124:127], v[80:83], a[112:115]\n"
"ds_read_b128 a[152:155], v246 offset:43008\n"
"v_cvt_pk_bf16_f32 v65, v2, v3\n"
"v_mfma_f32_16x16x32_bf16 a[128:131], v[212:215], v[80:83], a[128:131]\n"
"buffer_load_dword v250, v219, s[28:31], s84 offen\n"
"v_mfma_f32_16x16x32_bf16 a[132:135], v[212:215], v[84:87], a[132:135]\n"
"v_exp_f32 v4, v4\n"
"v_mfma_f32_16x16x32_bf16 a[116:119], v[124:127], v[84:87], a[116:119]\n"
"ds_read_b128 a[156:159], v246 offset:47104\n"
"v_mfma_f32_16x16x32_bf16 a[100:103], v[120:123], v[84:87], a[100:103]\n"
"v_exp_f32 v5, v5\n"
"v_mfma_f32_16x16x32_bf16 a[84:87], v[116:119], v[84:87], a[84:87]\n"
"ds_read_b64_tr_b16 v[208:209], v216\n"
"v_cvt_pk_bf16_f32 v66, v4, v5\n"
"v_mfma_f32_16x16x32_bf16 a[68:71], v[112:115], v[84:87], a[68:71]\n"
"v_exp_f32 v6, v6\n"
"v_mfma_f32_16x16x32_bf16 a[52:55], v[108:111], v[84:87], a[52:55]\n"
"ds_read_b64_tr_b16 v[210:211], v216 offset:1024\n"
"v_mfma_f32_16x16x32_bf16 a[36:39], v[104:107], v[84:87], a[36:39]\n"
"s_add_u32 s99, s16, 0\n"
"ds_read_b128 a[160:163], v246 offset:35840\n"
"v_mfma_f32_16x16x32_bf16 a[20:23], v[100:103], v[84:87], a[20:23]\n"
"v_exp_f32 v7, v7\n"
"v_mfma_f32_16x16x32_bf16 a[4:7], v[96:99], v[84:87], a[4:7]\n"
"ds_read_b128 a[164:167], v246 offset:39936\n"
"v_cvt_pk_bf16_f32 v67, v6, v7\n"
"v_mfma_f32_16x16x32_bf16 a[8:11], v[96:99], v[88:91], a[8:11]\n"
"v_exp_f32 v8, v8\n"
"v_mfma_f32_16x16x32_bf16 a[24:27], v[100:103], v[88:91], a[24:27]\n"
"v_exp_f32 v9, v9\n"
"v_mfma_f32_16x16x32_bf16 a[40:43], v[104:107], v[88:91], a[40:43]\n"
"s_waitcnt lgkmcnt(2)\n"
"buffer_store_dwordx4 v[208:211], v235, s[56:59], s99 offen sc1\n"
"v_mfma_f32_16x16x32_bf16 a[56:59], v[108:111], v[88:91], a[56:59]\n"
"ds_read_b128 a[168:171], v246 offset:44032\n"
"v_cvt_pk_bf16_f32 v68, v8, v9\n"
"v_mfma_f32_16x16x32_bf16 a[72:75], v[112:115], v[88:91], a[72:75]\n"
"v_exp_f32 v10, v10\n"
"v_mfma_f32_16x16x32_bf16 a[88:91], v[116:119], v[88:91], a[88:91]\n"
"ds_read_b128 a[172:175], v246 offset:48128\n"
"v_mfma_f32_16x16x32_bf16 a[104:107], v[120:123], v[88:91], a[104:107]\n"
"s_mov_b32 s84, 0x120000\n"
"v_exp_f32 v11, v11\n"
"v_mfma_f32_16x16x32_bf16 a[120:123], v[124:127], v[88:91], a[120:123]\n"
"v_cvt_pk_bf16_f32 v69, v10, v11\n"
"v_mfma_f32_16x16x32_bf16 a[136:139], v[212:215], v[88:91], a[136:139]\n"
"v_exp_f32 v12, v12\n"
"v_mfma_f32_16x16x32_bf16 a[140:143], v[212:215], v[92:95], a[140:143]\n"
"buffer_load_dword v250, v219, s[28:31], s84 offen\n"
"v_mfma_f32_16x16x32_bf16 a[124:127], v[124:127], v[92:95], a[124:127]\n"
"v_exp_f32 v13, v13\n"
"v_mfma_f32_16x16x32_bf16 a[108:111], v[120:123], v[92:95], a[108:111]\n"
"v_cvt_pk_bf16_f32 v70, v12, v13\n"
"v_mfma_f32_16x16x32_bf16 a[92:95], v[116:119], v[92:95], a[92:95]\n"
"v_exp_f32 v14, v14\n"
"v_mfma_f32_16x16x32_bf16 a[76:79], v[112:115], v[92:95], a[76:79]\n"
"v_exp_f32 v15, v15\n"
"v_mfma_f32_16x16x32_bf16 a[60:63], v[108:111], v[92:95], a[60:63]\n"
"v_cvt_pk_bf16_f32 v71, v14, v15\n"
"v_mfma_f32_16x16x32_bf16 a[44:47], v[104:107], v[92:95], a[44:47]\n"
"v_exp_f32 v16, v16\n"
"v_mfma_f32_16x16x32_bf16 a[28:31], v[100:103], v[92:95], a[28:31]\n"
"v_exp_f32 v17, v17\n"
"v_mfma_f32_16x16x32_bf16 a[12:15], v[96:99], v[92:95], a[12:15]\n"
"s_waitcnt lgkmcnt(8)\n"
"v_cvt_pk_bf16_f32 v72, v16, v17\n"
"v_mfma_f32_16x16x32_bf16 v[32:35], a[144:147], v[128:131], v[192:195]\n"
"ds_read_b64_tr_b16 v[96:97], v247 offset:34816\n"
"v_mfma_f32_16x16x32_bf16 v[32:35], a[148:151], v[132:135], v[32:35]\n"
"s_waitcnt lgkmcnt(7)\n"
"ds_read_b64_tr_b16 v[98:99], v247 offset:35840\n"
"v_mfma_f32_16x16x32_bf16 v[32:35], a[152:155], v[136:139], v[32:35]\n"
"ds_read_b64_tr_b16 v[100:101], v248 offset:34816\n"
"v_mfma_f32_16x16x32_bf16 v[32:35], a[156:159], v[140:143], v[32:35]\n"
"s_waitcnt lgkmcnt(5)\n"
"v_exp_f32 v18, v18\n"
"v_mfma_f32_16x16x32_bf16 v[36:39], a[160:163], v[128:131], v[192:195]\n"
"ds_read_b64_tr_b16 v[102:103], v248 offset:35840\n"
"v_mfma_f32_16x16x32_bf16 v[36:39], a[164:167], v[132:135], v[36:39]\n"
"s_waitcnt lgkmcnt(4)\n"
"v_exp_f32 v19, v19\n"
"v_mfma_f32_16x16x32_bf16 v[36:39], a[168:171], v[136:139], v[36:39]\n"
"ds_read_b64_tr_b16 v[104:105], v247 offset:38912\n"
"v_cvt_pk_bf16_f32 v73, v18, v19\n"
"v_mfma_f32_16x16x32_bf16 v[36:39], a[172:175], v[140:143], v[36:39]\n"
"ds_read_b64_tr_b16 v[106:107], v247 offset:39936\n"
"v_mfma_f32_16x16x32_bf16 v[40:43], a[144:147], v[144:147], v[196:199]\n"
"s_mov_b32 s84, 0x180000\n"
"v_exp_f32 v20, v20\n"
"v_mfma_f32_16x16x32_bf16 v[40:43], a[148:151], v[148:151], v[40:43]\n"
"ds_read_b64_tr_b16 v[108:109], v248 offset:38912\n"
"v_mfma_f32_16x16x32_bf16 v[40:43], a[152:155], v[152:155], v[40:43]\n"
"v_exp_f32 v21, v21\n"
"v_mfma_f32_16x16x32_bf16 v[40:43], a[156:159], v[156:159], v[40:43]\n"
"ds_read_b64_tr_b16 v[110:111], v248 offset:39936\n"
"v_cvt_pk_bf16_f32 v74, v20, v21\n"
"v_mfma_f32_16x16x32_bf16 v[44:47], a[160:163], v[144:147], v[196:199]\n"
"v_exp_f32 v22, v22\n"
"v_mfma_f32_16x16x32_bf16 v[44:47], a[164:167], v[148:151], v[44:47]\n"
"buffer_load_dword v250, v219, s[28:31], s84 offen\n"
"v_mfma_f32_16x16x32_bf16 v[44:47], a[168:171], v[152:155], v[44:47]\n"
"v_exp_f32 v23, v23\n"
"v_mfma_f32_16x16x32_bf16 v[44:47], a[172:175], v[156:159], v[44:47]\n"
"ds_read_b64_tr_b16 v[112:113], v247 offset:43008\n"
"v_cvt_pk_bf16_f32 v75, v22, v23\n"
"v_mfma_f32_16x16x32_bf16 v[48:51], a[144:147], v[160:163], v[200:203]\n"
"ds_read_b64_tr_b16 v[114:115], v247 offset:44032\n"
"v_mfma_f32_16x16x32_bf16 v[48:51], a[148:151], v[164:167], v[48:51]\n"
"v_exp_f32 v24, v24\n"
"v_mfma_f32_16x16x32_bf16 v[48:51], a[152:155], v[168:171], v[48:51]\n"
"ds_read_b64_tr_b16 v[116:117], v248 offset:43008\n"
"ds_read_b64_tr_b16 v[118:119], v248 offset:44032\n"
"v_mfma_f32_16x16x32_bf16 v[48:51], a[156:159], v[172:175], v[48:51]\n"
"v_exp_f32 v25, v25\n"
"v_mfma_f32_16x16x32_bf16 v[52:55], a[160:163], v[160:163], v[200:203]\n"
"ds_read_b64_tr_b16 v[120:121], v247 offset:47104\n"
"v_cvt_pk_bf16_f32 v76, v24, v25\n"
"v_mfma_f32_16x16x32_bf16 v[52:55], a[164:167], v[164:167], v[52:55]\n"
"v_exp_f32 v26, v26\n"
"v_mfma_f32_16x16x32_bf16 v[52:55], a[168:171], v[168:171], v[52:55]\n"
"ds_read_b64_tr_b16 v[122:123], v247 offset:48128\n"
"ds_read_b64_tr_b16 v[124:125], v248 offset:47104\n"
"v_mfma_f32_16x16x32_bf16 v[52:55], a[172:175], v[172:175], v[52:55]\n"
"v_exp_f32 v27, v27\n"
"v_mfma_f32_16x16x32_bf16 v[56:59], a[144:147], v[176:179], v[204:207]\n"
"ds_read_b64_tr_b16 v[126:127], v248 offset:48128\n"
"v_cvt_pk_bf16_f32 v77, v26, v27\n"
"v_mfma_f32_16x16x32_bf16 v[56:59], a[148:151], v[180:183], v[56:59]\n"
"v_exp_f32 v28, v28\n"
"v_mfma_f32_16x16x32_bf16 v[56:59], a[152:155], v[184:187], v[56:59]\n"
"v_exp_f32 v29, v29\n"
"v_mfma_f32_16x16x32_bf16 v[56:59], a[156:159], v[188:191], v[56:59]\n"
"v_cvt_pk_bf16_f32 v78, v28, v29\n"
"v_mfma_f32_16x16x32_bf16 v[60:63], a[160:163], v[176:179], v[204:207]\n"
"v_exp_f32 v30, v30\n"
"v_mfma_f32_16x16x32_bf16 v[60:63], a[164:167], v[180:183], v[60:63]\n"
"v_exp_f32 v31, v31\n"
"v_mfma_f32_16x16x32_bf16 v[60:63], a[168:171], v[184:187], v[60:63]\n"
"v_cvt_pk_bf16_f32 v79, v30, v31\n"
"v_mfma_f32_16x16x32_bf16 v[60:63], a[172:175], v[188:191], v[60:63]\n"
"s_waitcnt vmcnt(26) lgkmcnt(0)\n"
"s_barrier\n"
"v_mfma_f32_16x16x32_bf16 a[0:3], v[96:99], v[64:67], a[0:3]\n"
"v_exp_f32 v32, v32\n"
"v_mfma_f32_16x16x32_bf16 a[16:19], v[100:103], v[64:67], a[16:19]\n"
"s_mov_b32 s90, 0x18000\n"
"ds_read_b128 a[144:147], v231\n"
"v_mfma_f32_16x16x32_bf16 a[32:35], v[104:107], v[64:67], a[32:35]\n"
"v_exp_f32 v33, v33\n"
"v_mfma_f32_16x16x32_bf16 a[48:51], v[108:111], v[64:67], a[48:51]\n"
"v_cvt_pk_bf16_f32 v80, v32, v33\n"
"v_mfma_f32_16x16x32_bf16 a[64:67], v[112:115], v[64:67], a[64:67]\n"
"s_addk_i32 m0, 4288\n"
"ds_read_b128 a[148:151], v231 offset:4096\n"
"v_mfma_f32_16x16x32_bf16 a[80:83], v[116:119], v[64:67], a[80:83]\n"
"v_exp_f32 v34, v34\n"
"v_mfma_f32_16x16x32_bf16 a[96:99], v[120:123], v[64:67], a[96:99]\n"
"v_exp_f32 v35, v35\n"
"v_mfma_f32_16x16x32_bf16 a[112:115], v[124:127], v[64:67], a[112:115]\n"
"ds_read_b128 a[152:155], v231 offset:8192\n"
"v_cvt_pk_bf16_f32 v81, v34, v35\n"
"v_mfma_f32_16x16x32_bf16 a[128:131], v[212:215], v[64:67], a[128:131]\n"
"v_exp_f32 v36, v36\n"
"v_mfma_f32_16x16x32_bf16 a[132:135], v[212:215], v[68:71], a[132:135]\n"
"buffer_load_dwordx4 v244, s[40:43], s90 offen lds\n"
"v_mfma_f32_16x16x32_bf16 a[116:119], v[124:127], v[68:71], a[116:119]\n"
"s_addk_i32 m0, 4032\n"
"ds_read_b128 a[156:159], v231 offset:12288\n"
"v_mfma_f32_16x16x32_bf16 a[100:103], v[120:123], v[68:71], a[100:103]\n"
"v_exp_f32 v37, v37\n"
"v_mfma_f32_16x16x32_bf16 a[84:87], v[116:119], v[68:71], a[84:87]\n"
"v_cvt_pk_bf16_f32 v82, v36, v37\n"
"v_mfma_f32_16x16x32_bf16 a[68:71], v[112:115], v[68:71], a[68:71]\n"
"v_exp_f32 v38, v38\n"
"v_mfma_f32_16x16x32_bf16 a[52:55], v[108:111], v[68:71], a[52:55]\n"
"ds_read_b128 a[160:163], v231 offset:1024\n"
"v_mfma_f32_16x16x32_bf16 a[36:39], v[104:107], v[68:71], a[36:39]\n"
"v_exp_f32 v39, v39\n"
"v_mfma_f32_16x16x32_bf16 a[20:23], v[100:103], v[68:71], a[20:23]\n"
"v_cvt_pk_bf16_f32 v83, v38, v39\n"
"v_mfma_f32_16x16x32_bf16 a[4:7], v[96:99], v[68:71], a[4:7]\n"
"buffer_load_dwordx4 v244, s[40:43], s90 offen offset:64 lds\n"
"v_mfma_f32_16x16x32_bf16 a[8:11], v[96:99], v[72:75], a[8:11]\n"
"s_addk_i32 m0, 4032\n"
"ds_read_b128 a[164:167], v231 offset:5120\n"
"v_mfma_f32_16x16x32_bf16 a[24:27], v[100:103], v[72:75], a[24:27]\n"
"v_exp_f32 v40, v40\n"
"v_mfma_f32_16x16x32_bf16 a[40:43], v[104:107], v[72:75], a[40:43]\n"
"v_exp_f32 v41, v41\n"
"v_mfma_f32_16x16x32_bf16 a[56:59], v[108:111], v[72:75], a[56:59]\n"
"ds_read_b128 a[168:171], v231 offset:9216\n"
"v_cvt_pk_bf16_f32 v84, v40, v41\n"
"v_mfma_f32_16x16x32_bf16 a[72:75], v[112:115], v[72:75], a[72:75]\n"
"v_exp_f32 v42, v42\n"
"v_mfma_f32_16x16x32_bf16 a[88:91], v[116:119], v[72:75], a[88:91]\n"
"buffer_load_dwordx4 v244, s[40:43], s90 offen offset:128 lds\n"
"v_mfma_f32_16x16x32_bf16 a[104:107], v[120:123], v[72:75], a[104:107]\n"
"s_addk_i32 m0, 4032\n"
"ds_read_b128 a[172:175], v231 offset:13312\n"
"v_mfma_f32_16x16x32_bf16 a[120:123], v[124:127], v[72:75], a[120:123]\n"
"v_exp_f32 v43, v43\n"
"v_mfma_f32_16x16x32_bf16 a[136:139], v[212:215], v[72:75], a[136:139]\n"
"v_exp_f32 v44, v44\n"
"v_mfma_f32_16x16x32_bf16 a[140:143], v[212:215], v[76:79], a[140:143]\n"
"v_cvt_pk_bf16_f32 v85, v42, v43\n"
"v_mfma_f32_16x16x32_bf16 a[124:127], v[124:127], v[76:79], a[124:127]\n"
"v_exp_f32 v45, v45\n"
"v_mfma_f32_16x16x32_bf16 a[108:111], v[120:123], v[76:79], a[108:111]\n"
"v_cvt_pk_bf16_f32 v86, v44, v45\n"
"v_mfma_f32_16x16x32_bf16 a[92:95], v[116:119], v[76:79], a[92:95]\n"
"buffer_load_dwordx4 v244, s[40:43], s90 offen offset:192 lds\n"
"v_mfma_f32_16x16x32_bf16 a[76:79], v[112:115], v[76:79], a[76:79]\n"
"s_addk_i32 m0, 4288\n"
"v_exp_f32 v46, v46\n"
"v_mfma_f32_16x16x32_bf16 a[60:63], v[108:111], v[76:79], a[60:63]\n"
"v_exp_f32 v47, v47\n"
"v_mfma_f32_16x16x32_bf16 a[44:47], v[104:107], v[76:79], a[44:47]\n"
"v_cvt_pk_bf16_f32 v87, v46, v47\n"
"v_mfma_f32_16x16x32_bf16 a[28:31], v[100:103], v[76:79], a[28:31]\n"
"v_exp_f32 v48, v48\n"
"v_mfma_f32_16x16x32_bf16 a[12:15], v[96:99], v[76:79], a[12:15]\n"
"s_waitcnt lgkmcnt(6)\n"
"buffer_load_dwordx4 v245, s[44:47], s90 offen lds\n"
"v_mfma_f32_16x16x32_bf16 v[0:3], a[144:147], v[128:131], v[192:195]\n"
"ds_read_b64_tr_b16 v[96:97], v232\n"
"v_mfma_f32_16x16x32_bf16 v[0:3], a[148:151], v[132:135], v[0:3]\n"
"s_waitcnt lgkmcnt(5)\n"
"v_exp_f32 v49, v49\n"
"v_mfma_f32_16x16x32_bf16 v[0:3], a[152:155], v[136:139], v[0:3]\n"
"s_addk_i32 m0, 4032\n"
"ds_read_b64_tr_b16 v[98:99], v232 offset:1024\n"
"v_cvt_pk_bf16_f32 v88, v48, v49\n"
"v_mfma_f32_16x16x32_bf16 v[0:3], a[156:159], v[140:143], v[0:3]\n"
"s_waitcnt lgkmcnt(4)\n"
"v_exp_f32 v50, v50\n"
"v_mfma_f32_16x16x32_bf16 v[4:7], a[160:163], v[128:131], v[192:195]\n"
"ds_read_b64_tr_b16 v[100:101], v252\n"
"v_mfma_f32_16x16x32_bf16 v[4:7], a[164:167], v[132:135], v[4:7]\n"
"s_waitcnt lgkmcnt(3)\n"
"ds_read_b64_tr_b16 v[102:103], v252 offset:1024\n"
"v_mfma_f32_16x16x32_bf16 v[4:7], a[168:171], v[136:139], v[4:7]\n"
"v_exp_f32 v51, v51\n"
"v_mfma_f32_16x16x32_bf16 v[4:7], a[172:175], v[140:143], v[4:7]\n"
"buffer_load_dwordx4 v245, s[44:47], s90 offen offset:64 lds\n"
"v_mfma_f32_16x16x32_bf16 v[8:11], a[144:147], v[144:147], v[196:199]\n"
"s_addk_i32 m0, 4032\n"
"v_exp_f32 v52, v52\n"
"v_mfma_f32_16x16x32_bf16 v[8:11], a[148:151], v[148:151], v[8:11]\n"
"ds_read_b64_tr_b16 v[104:105], v232 offset:4096\n"
"v_cvt_pk_bf16_f32 v89, v50, v51\n"
"v_mfma_f32_16x16x32_bf16 v[8:11], a[152:155], v[152:155], v[8:11]\n"
"v_exp_f32 v53, v53\n"
"v_mfma_f32_16x16x32_bf16 v[8:11], a[156:159], v[156:159], v[8:11]\n"
"ds_read_b64_tr_b16 v[106:107], v232 offset:5120\n"
"v_cvt_pk_bf16_f32 v90, v52, v53\n"
"v_mfma_f32_16x16x32_bf16 v[12:15], a[160:163], v[144:147], v[196:199]\n"
"v_exp_f32 v54, v54\n"
"v_mfma_f32_16x16x32_bf16 v[12:15], a[164:167], v[148:151], v[12:15]\n"
"ds_read_b64_tr_b16 v[108:109], v252 offset:4096\n"
"v_mfma_f32_16x16x32_bf16 v[12:15], a[168:171], v[152:155], v[12:15]\n"
"v_exp_f32 v55, v55\n"
"v_mfma_f32_16x16x32_bf16 v[12:15], a[172:175], v[156:159], v[12:15]\n"
"buffer_load_dwordx4 v245, s[44:47], s90 offen offset:128 lds\n"
"v_mfma_f32_16x16x32_bf16 v[16:19], a[144:147], v[160:163], v[200:203]\n"
"s_addk_i32 m0, 4032\n"
"ds_read_b64_tr_b16 v[110:111], v252 offset:5120\n"
"v_cvt_pk_bf16_f32 v91, v54, v55\n"
"v_mfma_f32_16x16x32_bf16 v[16:19], a[148:151], v[164:167], v[16:19]\n"
"v_exp_f32 v56, v56\n"
"v_mfma_f32_16x16x32_bf16 v[16:19], a[152:155], v[168:171], v[16:19]\n"
"ds_read_b64_tr_b16 v[112:113], v232 offset:8192\n"
"ds_read_b64_tr_b16 v[114:115], v232 offset:9216\n"
"v_mfma_f32_16x16x32_bf16 v[16:19], a[156:159], v[172:175], v[16:19]\n"
"v_exp_f32 v57, v57\n"
"v_mfma_f32_16x16x32_bf16 v[20:23], a[160:163], v[160:163], v[200:203]\n"
"buffer_load_dwordx4 v245, s[44:47], s90 offen offset:192 lds\n"
"v_mfma_f32_16x16x32_bf16 v[20:23], a[164:167], v[164:167], v[20:23]\n"
"v_exp_f32 v58, v58\n"
"v_mfma_f32_16x16x32_bf16 v[20:23], a[168:171], v[168:171], v[20:23]\n"
"ds_read_b64_tr_b16 v[116:117], v252 offset:8192\n"
"v_cvt_pk_bf16_f32 v92, v56, v57\n"
"v_mfma_f32_16x16x32_bf16 v[20:23], a[172:175], v[172:175], v[20:23]\n"
"v_exp_f32 v59, v59\n"
"v_mfma_f32_16x16x32_bf16 v[24:27], a[144:147], v[176:179], v[204:207]\n"
"ds_read_b64_tr_b16 v[118:119], v252 offset:9216\n"
"v_cvt_pk_bf16_f32 v93, v58, v59\n"
"v_mfma_f32_16x16x32_bf16 v[24:27], a[148:151], v[180:183], v[24:27]\n"
"v_exp_f32 v60, v60\n"
"v_mfma_f32_16x16x32_bf16 v[24:27], a[152:155], v[184:187], v[24:27]\n"
"ds_read_b64_tr_b16 v[120:121], v232 offset:12288\n"
"ds_read_b64_tr_b16 v[122:123], v232 offset:13312\n"
"v_mfma_f32_16x16x32_bf16 v[24:27], a[156:159], v[188:191], v[24:27]\n"
"v_exp_f32 v61, v61\n"
"v_mfma_f32_16x16x32_bf16 v[28:31], a[160:163], v[176:179], v[204:207]\n"
"ds_read_b64_tr_b16 v[124:125], v252 offset:12288\n"
"ds_read_b64_tr_b16 v[126:127], v252 offset:13312\n"
"v_mfma_f32_16x16x32_bf16 v[28:31], a[164:167], v[180:183], v[28:31]\n"
"v_exp_f32 v62, v62\n"
"v_mfma_f32_16x16x32_bf16 v[28:31], a[168:171], v[184:187], v[28:31]\n"
"v_cvt_pk_bf16_f32 v94, v60, v61\n"
"v_mfma_f32_16x16x32_bf16 v[28:31], a[172:175], v[188:191], v[28:31]\n"
"s_waitcnt lgkmcnt(12)\n"
"v_exp_f32 v63, v63\n"
"v_mfma_f32_16x16x32_bf16 a[0:3], v[96:99], v[80:83], a[0:3]\n"
"v_cvt_pk_bf16_f32 v95, v62, v63\n"
"v_mfma_f32_16x16x32_bf16 a[16:19], v[100:103], v[80:83], a[16:19]\n"
"s_waitcnt lgkmcnt(8)\n"
"v_exp_f32 v0, v0\n"
"v_mfma_f32_16x16x32_bf16 a[32:35], v[104:107], v[80:83], a[32:35]\n"
"ds_read_b128 a[144:147], v231 offset:2048\n"
"v_mfma_f32_16x16x32_bf16 a[48:51], v[108:111], v[80:83], a[48:51]\n"
"s_waitcnt vmcnt(16)\n"
"s_barrier\n"
"s_waitcnt lgkmcnt(5)\n"
"v_mfma_f32_16x16x32_bf16 a[64:67], v[112:115], v[80:83], a[64:67]\n"
"s_add_u32 s84, s8, 0x10000\n"
"v_exp_f32 v1, v1\n"
"v_mfma_f32_16x16x32_bf16 a[80:83], v[116:119], v[80:83], a[80:83]\n"
"s_waitcnt lgkmcnt(1)\n"
"ds_read_b128 a[148:151], v231 offset:6144\n"
"v_cvt_pk_bf16_f32 v64, v0, v1\n"
"v_mfma_f32_16x16x32_bf16 a[96:99], v[120:123], v[80:83], a[96:99]\n"
"v_exp_f32 v2, v2\n"
"v_mfma_f32_16x16x32_bf16 a[112:115], v[124:127], v[80:83], a[112:115]\n"
"v_exp_f32 v3, v3\n"
"v_mfma_f32_16x16x32_bf16 a[128:131], v[212:215], v[80:83], a[128:131]\n"
"ds_read_b128 a[152:155], v231 offset:10240\n"
"v_mov_b32 v254, s95\n"
"v_mfma_f32_16x16x32_bf16 a[132:135], v[212:215], v[84:87], a[132:135]\n"
"buffer_store_dword v254, v234, s[56:59], s84 offen sc1\n"
"v_mfma_f32_16x16x32_bf16 a[116:119], v[124:127], v[84:87], a[116:119]\n"
"v_exp_f32 v4, v4\n"
"v_mfma_f32_16x16x32_bf16 a[100:103], v[120:123], v[84:87], a[100:103]\n"
"ds_read_b128 a[156:159], v231 offset:14336\n"
"v_cvt_pk_bf16_f32 v65, v2, v3\n"
"v_mfma_f32_16x16x32_bf16 a[84:87], v[116:119], v[84:87], a[84:87]\n"
"v_exp_f32 v5, v5\n"
"v_mfma_f32_16x16x32_bf16 a[68:71], v[112:115], v[84:87], a[68:71]\n"
"v_exp_f32 v6, v6\n"
"v_mfma_f32_16x16x32_bf16 a[52:55], v[108:111], v[84:87], a[52:55]\n"
"ds_read_b64_tr_b16 v[208:209], v216 offset:4096\n"
"v_cvt_pk_bf16_f32 v66, v4, v5\n"
"v_mfma_f32_16x16x32_bf16 a[36:39], v[104:107], v[84:87], a[36:39]\n"
"ds_read_b64_tr_b16 v[210:211], v216 offset:5120\n"
"v_mfma_f32_16x16x32_bf16 a[20:23], v[100:103], v[84:87], a[20:23]\n"
"s_add_u32 s99, s16, 0x2000\n"
"v_exp_f32 v7, v7\n"
"v_mfma_f32_16x16x32_bf16 a[4:7], v[96:99], v[84:87], a[4:7]\n"
"ds_read_b128 a[160:163], v231 offset:3072\n"
"v_cvt_pk_bf16_f32 v67, v6, v7\n"
"v_mfma_f32_16x16x32_bf16 a[8:11], v[96:99], v[88:91], a[8:11]\n"
"ds_read_b128 a[164:167], v231 offset:7168\n"
"v_mfma_f32_16x16x32_bf16 a[24:27], v[100:103], v[88:91], a[24:27]\n"
"v_exp_f32 v8, v8\n"
"v_mfma_f32_16x16x32_bf16 a[40:43], v[104:107], v[88:91], a[40:43]\n"
"v_exp_f32 v9, v9\n"
"v_mfma_f32_16x16x32_bf16 a[56:59], v[108:111], v[88:91], a[56:59]\n"
"s_waitcnt lgkmcnt(2)\n"
"buffer_store_dwordx4 v[208:211], v235, s[56:59], s99 offen sc1\n"
"v_mfma_f32_16x16x32_bf16 a[72:75], v[112:115], v[88:91], a[72:75]\n"
"ds_read_b128 a[168:171], v231 offset:11264\n"
"v_cvt_pk_bf16_f32 v68, v8, v9\n"
"v_mfma_f32_16x16x32_bf16 a[88:91], v[116:119], v[88:91], a[88:91]\n"
"v_exp_f32 v10, v10\n"
"v_mfma_f32_16x16x32_bf16 a[104:107], v[120:123], v[88:91], a[104:107]\n"
"ds_read_b128 a[172:175], v231 offset:15360\n"
"v_mfma_f32_16x16x32_bf16 a[120:123], v[124:127], v[88:91], a[120:123]\n"
"v_exp_f32 v11, v11\n"
"v_mfma_f32_16x16x32_bf16 a[136:139], v[212:215], v[88:91], a[136:139]\n"
"s_mov_b32 s84, 0x1e0000\n"
"v_exp_f32 v12, v12\n"
"v_mfma_f32_16x16x32_bf16 a[140:143], v[212:215], v[92:95], a[140:143]\n"
"v_cvt_pk_bf16_f32 v69, v10, v11\n"
"v_mfma_f32_16x16x32_bf16 a[124:127], v[124:127], v[92:95], a[124:127]\n"
"v_exp_f32 v13, v13\n"
"v_mfma_f32_16x16x32_bf16 a[108:111], v[120:123], v[92:95], a[108:111]\n"
"v_cvt_pk_bf16_f32 v70, v12, v13\n"
"v_mfma_f32_16x16x32_bf16 a[92:95], v[116:119], v[92:95], a[92:95]\n"
"buffer_load_dword v250, v219, s[28:31], s84 offen\n"
"v_mfma_f32_16x16x32_bf16 a[76:79], v[112:115], v[92:95], a[76:79]\n"
"v_exp_f32 v14, v14\n"
"v_mfma_f32_16x16x32_bf16 a[60:63], v[108:111], v[92:95], a[60:63]\n"
"v_exp_f32 v15, v15\n"
"v_mfma_f32_16x16x32_bf16 a[44:47], v[104:107], v[92:95], a[44:47]\n"
"v_cvt_pk_bf16_f32 v71, v14, v15\n"
"v_mfma_f32_16x16x32_bf16 a[28:31], v[100:103], v[92:95], a[28:31]\n"
"v_exp_f32 v16, v16\n"
"v_mfma_f32_16x16x32_bf16 a[12:15], v[96:99], v[92:95], a[12:15]\n"
"s_waitcnt lgkmcnt(8)\n"
"v_exp_f32 v17, v17\n"
"v_mfma_f32_16x16x32_bf16 v[32:35], a[144:147], v[128:131], v[192:195]\n"
"ds_read_b64_tr_b16 v[96:97], v232 offset:2048\n"
"v_cvt_pk_bf16_f32 v72, v16, v17\n"
"v_mfma_f32_16x16x32_bf16 v[32:35], a[148:151], v[132:135], v[32:35]\n"
"s_waitcnt lgkmcnt(7)\n"
"ds_read_b64_tr_b16 v[98:99], v232 offset:3072\n"
"v_mfma_f32_16x16x32_bf16 v[32:35], a[152:155], v[136:139], v[32:35]\n"
"ds_read_b64_tr_b16 v[100:101], v252 offset:2048\n"
"v_mfma_f32_16x16x32_bf16 v[32:35], a[156:159], v[140:143], v[32:35]\n"
"s_waitcnt lgkmcnt(5)\n"
"v_exp_f32 v18, v18\n"
"v_mfma_f32_16x16x32_bf16 v[36:39], a[160:163], v[128:131], v[192:195]\n"
"ds_read_b64_tr_b16 v[208:209], v216 offset:8192\n"
"v_mfma_f32_16x16x32_bf16 v[36:39], a[164:167], v[132:135], v[36:39]\n"
"s_waitcnt lgkmcnt(4)\n"
"ds_read_b64_tr_b16 v[210:211], v216 offset:9216\n"
"v_mfma_f32_16x16x32_bf16 v[36:39], a[168:171], v[136:139], v[36:39]\n"
"s_add_u32 s99, s16, 0x4000\n"
"v_exp_f32 v19, v19\n"
"v_mfma_f32_16x16x32_bf16 v[36:39], a[172:175], v[140:143], v[36:39]\n"
"ds_read_b64_tr_b16 v[102:103], v252 offset:3072\n"
"v_cvt_pk_bf16_f32 v73, v18, v19\n"
"v_mfma_f32_16x16x32_bf16 v[40:43], a[144:147], v[144:147], v[196:199]\n"
"v_exp_f32 v20, v20\n"
"v_mfma_f32_16x16x32_bf16 v[40:43], a[148:151], v[148:151], v[40:43]\n"
"ds_read_b64_tr_b16 v[104:105], v232 offset:6144\n"
"v_mfma_f32_16x16x32_bf16 v[40:43], a[152:155], v[152:155], v[40:43]\n"
"s_mov_b32 s84, 0x240000\n"
"v_exp_f32 v21, v21\n"
"v_mfma_f32_16x16x32_bf16 v[40:43], a[156:159], v[156:159], v[40:43]\n"
"s_waitcnt lgkmcnt(2)\n"
"buffer_store_dwordx4 v[208:211], v235, s[56:59], s99 offen sc1\n"
"v_mfma_f32_16x16x32_bf16 v[44:47], a[160:163], v[144:147], v[196:199]\n"
"v_exp_f32 v22, v22\n"
"v_mfma_f32_16x16x32_bf16 v[44:47], a[164:167], v[148:151], v[44:47]\n"
"ds_read_b64_tr_b16 v[106:107], v232 offset:7168\n"
"v_cvt_pk_bf16_f32 v74, v20, v21\n"
"v_mfma_f32_16x16x32_bf16 v[44:47], a[168:171], v[152:155], v[44:47]\n"
"v_exp_f32 v23, v23\n"
"v_mfma_f32_16x16x32_bf16 v[44:47], a[172:175], v[156:159], v[44:47]\n"
"buffer_load_dword v250, v219, s[28:31], s84 offen\n"
"v_mfma_f32_16x16x32_bf16 v[48:51], a[144:147], v[160:163], v[200:203]\n"
"ds_read_b64_tr_b16 v[108:109], v252 offset:6144\n"
"v_cvt_pk_bf16_f32 v75, v22, v23\n"
"v_mfma_f32_16x16x32_bf16 v[48:51], a[148:151], v[164:167], v[48:51]\n"
"v_exp_f32 v24, v24\n"
"v_mfma_f32_16x16x32_bf16 v[48:51], a[152:155], v[168:171], v[48:51]\n"
"ds_read_b64_tr_b16 v[110:111], v252 offset:7168\n"
"ds_read_b64_tr_b16 v[112:113], v232 offset:10240\n"
"v_mfma_f32_16x16x32_bf16 v[48:51], a[156:159], v[172:175], v[48:51]\n"
"v_exp_f32 v25, v25\n"
"v_mfma_f32_16x16x32_bf16 v[52:55], a[160:163], v[160:163], v[200:203]\n"
"ds_read_b64_tr_b16 v[114:115], v232 offset:11264\n"
"v_cvt_pk_bf16_f32 v76, v24, v25\n"
"v_mfma_f32_16x16x32_bf16 v[52:55], a[164:167], v[164:167], v[52:55]\n"
"v_exp_f32 v26, v26\n"
"v_mfma_f32_16x16x32_bf16 v[52:55], a[168:171], v[168:171], v[52:55]\n"
"ds_read_b64_tr_b16 v[116:117], v252 offset:10240\n"
"ds_read_b64_tr_b16 v[118:119], v252 offset:11264\n"
"v_mfma_f32_16x16x32_bf16 v[52:55], a[172:175], v[172:175], v[52:55]\n"
"v_exp_f32 v27, v27\n"
"v_mfma_f32_16x16x32_bf16 v[56:59], a[144:147], v[176:179], v[204:207]\n"
"ds_read_b64_tr_b16 v[120:121], v232 offset:14336\n"
"ds_read_b64_tr_b16 v[122:123], v232 offset:15360\n"
"ds_read_b64_tr_b16 v[124:125], v252 offset:14336\n"
"ds_read_b64_tr_b16 v[126:127], v252 offset:15360\n"
"v_mfma_f32_16x16x32_bf16 v[56:59], a[148:151], v[180:183], v[56:59]\n"
"v_exp_f32 v28, v28\n"
"v_mfma_f32_16x16x32_bf16 v[56:59], a[152:155], v[184:187], v[56:59]\n"
"v_cvt_pk_bf16_f32 v77, v26, v27\n"
"v_mfma_f32_16x16x32_bf16 v[56:59], a[156:159], v[188:191], v[56:59]\n"
"v_exp_f32 v29, v29\n"
"v_mfma_f32_16x16x32_bf16 v[60:63], a[160:163], v[176:179], v[204:207]\n"
"v_exp_f32 v30, v30\n"
"v_mfma_f32_16x16x32_bf16 v[60:63], a[164:167], v[180:183], v[60:63]\n"
"v_cvt_pk_bf16_f32 v78, v28, v29\n"
"v_mfma_f32_16x16x32_bf16 v[60:63], a[168:171], v[184:187], v[60:63]\n"
"v_exp_f32 v31, v31\n"
"v_mfma_f32_16x16x32_bf16 v[60:63], a[172:175], v[188:191], v[60:63]\n"
"s_waitcnt vmcnt(17) lgkmcnt(0)\n"
"s_barrier\n"
"v_cvt_pk_bf16_f32 v79, v30, v31\n"
"v_mfma_f32_16x16x32_bf16 a[0:3], v[96:99], v[64:67], a[0:3]\n"
"v_exp_f32 v32, v32\n"
"v_mfma_f32_16x16x32_bf16 a[16:19], v[100:103], v[64:67], a[16:19]\n"
"s_mov_b32 s90, 0x1c000\n"
"ds_read_b128 a[144:147], v246\n"
"v_mfma_f32_16x16x32_bf16 a[32:35], v[104:107], v[64:67], a[32:35]\n"
"v_exp_f32 v33, v33\n"
"v_mfma_f32_16x16x32_bf16 a[48:51], v[108:111], v[64:67], a[48:51]\n"
"v_cvt_pk_bf16_f32 v80, v32, v33\n"
"v_mfma_f32_16x16x32_bf16 a[64:67], v[112:115], v[64:67], a[64:67]\n"
"s_add_u32 m0, s86, 0x9000\n"
"ds_read_b128 a[148:151], v246 offset:4096\n"
"v_mfma_f32_16x16x32_bf16 a[80:83], v[116:119], v[64:67], a[80:83]\n"
"v_exp_f32 v34, v34\n"
"v_mfma_f32_16x16x32_bf16 a[96:99], v[120:123], v[64:67], a[96:99]\n"
"v_exp_f32 v35, v35\n"
"v_mfma_f32_16x16x32_bf16 a[112:115], v[124:127], v[64:67], a[112:115]\n"
"ds_read_b128 a[152:155], v246 offset:8192\n"
"v_cvt_pk_bf16_f32 v81, v34, v35\n"
"v_mfma_f32_16x16x32_bf16 a[128:131], v[212:215], v[64:67], a[128:131]\n"
"v_exp_f32 v36, v36\n"
"v_mfma_f32_16x16x32_bf16 a[132:135], v[212:215], v[68:71], a[132:135]\n"
"buffer_load_dwordx4 v244, s[40:43], s90 offen lds\n"
"v_mfma_f32_16x16x32_bf16 a[116:119], v[124:127], v[68:71], a[116:119]\n"
"s_addk_i32 m0, 4032\n"
"ds_read_b128 a[156:159], v246 offset:12288\n"
"v_mfma_f32_16x16x32_bf16 a[100:103], v[120:123], v[68:71], a[100:103]\n"
"v_exp_f32 v37, v37\n"
"v_mfma_f32_16x16x32_bf16 a[84:87], v[116:119], v[68:71], a[84:87]\n"
"v_cvt_pk_bf16_f32 v82, v36, v37\n"
"v_mfma_f32_16x16x32_bf16 a[68:71], v[112:115], v[68:71], a[68:71]\n"
"v_exp_f32 v38, v38\n"
"v_mfma_f32_16x16x32_bf16 a[52:55], v[108:111], v[68:71], a[52:55]\n"
"ds_read_b128 a[160:163], v246 offset:1024\n"
"v_mfma_f32_16x16x32_bf16 a[36:39], v[104:107], v[68:71], a[36:39]\n"
"v_exp_f32 v39, v39\n"
"v_mfma_f32_16x16x32_bf16 a[20:23], v[100:103], v[68:71], a[20:23]\n"
"v_cvt_pk_bf16_f32 v83, v38, v39\n"
"v_mfma_f32_16x16x32_bf16 a[4:7], v[96:99], v[68:71], a[4:7]\n"
"buffer_load_dwordx4 v244, s[40:43], s90 offen offset:64 lds\n"
"v_mfma_f32_16x16x32_bf16 a[8:11], v[96:99], v[72:75], a[8:11]\n"
"s_addk_i32 m0, 4032\n"
"ds_read_b128 a[164:167], v246 offset:5120\n"
"v_mfma_f32_16x16x32_bf16 a[24:27], v[100:103], v[72:75], a[24:27]\n"
"v_exp_f32 v40, v40\n"
"v_mfma_f32_16x16x32_bf16 a[40:43], v[104:107], v[72:75], a[40:43]\n"
"v_exp_f32 v41, v41\n"
"v_mfma_f32_16x16x32_bf16 a[56:59], v[108:111], v[72:75], a[56:59]\n"
"ds_read_b128 a[168:171], v246 offset:9216\n"
"v_cvt_pk_bf16_f32 v84, v40, v41\n"
"v_mfma_f32_16x16x32_bf16 a[72:75], v[112:115], v[72:75], a[72:75]\n"
"v_exp_f32 v42, v42\n"
"v_mfma_f32_16x16x32_bf16 a[88:91], v[116:119], v[72:75], a[88:91]\n"
"buffer_load_dwordx4 v244, s[40:43], s90 offen offset:128 lds\n"
"v_mfma_f32_16x16x32_bf16 a[104:107], v[120:123], v[72:75], a[104:107]\n"
"s_addk_i32 m0, 4032\n"
"ds_read_b128 a[172:175], v246 offset:13312\n"
"v_mfma_f32_16x16x32_bf16 a[120:123], v[124:127], v[72:75], a[120:123]\n"
"v_exp_f32 v43, v43\n"
"v_mfma_f32_16x16x32_bf16 a[136:139], v[212:215], v[72:75], a[136:139]\n"
"v_exp_f32 v44, v44\n"
"v_mfma_f32_16x16x32_bf16 a[140:143], v[212:215], v[76:79], a[140:143]\n"
"v_cvt_pk_bf16_f32 v85, v42, v43\n"
"v_mfma_f32_16x16x32_bf16 a[124:127], v[124:127], v[76:79], a[124:127]\n"
"v_exp_f32 v45, v45\n"
"v_mfma_f32_16x16x32_bf16 a[108:111], v[120:123], v[76:79], a[108:111]\n"
"v_cvt_pk_bf16_f32 v86, v44, v45\n"
"v_mfma_f32_16x16x32_bf16 a[92:95], v[116:119], v[76:79], a[92:95]\n"
"buffer_load_dwordx4 v244, s[40:43], s90 offen offset:192 lds\n"
"v_mfma_f32_16x16x32_bf16 a[76:79], v[112:115], v[76:79], a[76:79]\n"
"s_addk_i32 m0, 4288\n"
"v_exp_f32 v46, v46\n"
"v_mfma_f32_16x16x32_bf16 a[60:63], v[108:111], v[76:79], a[60:63]\n"
"v_exp_f32 v47, v47\n"
"v_mfma_f32_16x16x32_bf16 a[44:47], v[104:107], v[76:79], a[44:47]\n"
"v_cvt_pk_bf16_f32 v87, v46, v47\n"
"v_mfma_f32_16x16x32_bf16 a[28:31], v[100:103], v[76:79], a[28:31]\n"
"v_exp_f32 v48, v48\n"
"v_mfma_f32_16x16x32_bf16 a[12:15], v[96:99], v[76:79], a[12:15]\n"
"s_waitcnt lgkmcnt(6)\n"
"buffer_load_dwordx4 v245, s[44:47], s90 offen lds\n"
"v_mfma_f32_16x16x32_bf16 v[0:3], a[144:147], v[128:131], v[192:195]\n"
"ds_read_b64_tr_b16 v[96:97], v247\n"
"v_mfma_f32_16x16x32_bf16 v[0:3], a[148:151], v[132:135], v[0:3]\n"
"s_waitcnt lgkmcnt(5)\n"
"v_exp_f32 v49, v49\n"
"v_mfma_f32_16x16x32_bf16 v[0:3], a[152:155], v[136:139], v[0:3]\n"
"s_addk_i32 m0, 4032\n"
"ds_read_b64_tr_b16 v[98:99], v247 offset:1024\n"
"v_cvt_pk_bf16_f32 v88, v48, v49\n"
"v_mfma_f32_16x16x32_bf16 v[0:3], a[156:159], v[140:143], v[0:3]\n"
"s_waitcnt lgkmcnt(4)\n"
"v_exp_f32 v50, v50\n"
"v_mfma_f32_16x16x32_bf16 v[4:7], a[160:163], v[128:131], v[192:195]\n"
"ds_read_b64_tr_b16 v[100:101], v248\n"
"v_mfma_f32_16x16x32_bf16 v[4:7], a[164:167], v[132:135], v[4:7]\n"
"s_waitcnt lgkmcnt(3)\n"
"ds_read_b64_tr_b16 v[102:103], v248 offset:1024\n"
"v_mfma_f32_16x16x32_bf16 v[4:7], a[168:171], v[136:139], v[4:7]\n"
"v_exp_f32 v51, v51\n"
"v_mfma_f32_16x16x32_bf16 v[4:7], a[172:175], v[140:143], v[4:7]\n"
"buffer_load_dwordx4 v245, s[44:47], s90 offen offset:64 lds\n"
"v_mfma_f32_16x16x32_bf16 v[8:11], a[144:147], v[144:147], v[196:199]\n"
"s_addk_i32 m0, 4032\n"
"v_exp_f32 v52, v52\n"
"v_mfma_f32_16x16x32_bf16 v[8:11], a[148:151], v[148:151], v[8:11]\n"
"ds_read_b64_tr_b16 v[104:105], v247 offset:4096\n"
"v_cvt_pk_bf16_f32 v89, v50, v51\n"
"v_mfma_f32_16x16x32_bf16 v[8:11], a[152:155], v[152:155], v[8:11]\n"
"v_exp_f32 v53, v53\n"
"v_mfma_f32_16x16x32_bf16 v[8:11], a[156:159], v[156:159], v[8:11]\n"
"ds_read_b64_tr_b16 v[106:107], v247 offset:5120\n"
"v_cvt_pk_bf16_f32 v90, v52, v53\n"
"v_mfma_f32_16x16x32_bf16 v[12:15], a[160:163], v[144:147], v[196:199]\n"
"v_exp_f32 v54, v54\n"
"v_mfma_f32_16x16x32_bf16 v[12:15], a[164:167], v[148:151], v[12:15]\n"
"ds_read_b64_tr_b16 v[108:109], v248 offset:4096\n"
"v_mfma_f32_16x16x32_bf16 v[12:15], a[168:171], v[152:155], v[12:15]\n"
"v_exp_f32 v55, v55\n"
"v_mfma_f32_16x16x32_bf16 v[12:15], a[172:175], v[156:159], v[12:15]\n"
"buffer_load_dwordx4 v245, s[44:47], s90 offen offset:128 lds\n"
"v_mfma_f32_16x16x32_bf16 v[16:19], a[144:147], v[160:163], v[200:203]\n"
"s_addk_i32 m0, 4032\n"
"ds_read_b64_tr_b16 v[110:111], v248 offset:5120\n"
"v_cvt_pk_bf16_f32 v91, v54, v55\n"
"v_mfma_f32_16x16x32_bf16 v[16:19], a[148:151], v[164:167], v[16:19]\n"
"v_exp_f32 v56, v56\n"
"v_mfma_f32_16x16x32_bf16 v[16:19], a[152:155], v[168:171], v[16:19]\n"
"ds_read_b64_tr_b16 v[112:113], v247 offset:8192\n"
"ds_read_b64_tr_b16 v[114:115], v247 offset:9216\n"
"v_mfma_f32_16x16x32_bf16 v[16:19], a[156:159], v[172:175], v[16:19]\n"
"v_exp_f32 v57, v57\n"
"v_mfma_f32_16x16x32_bf16 v[20:23], a[160:163], v[160:163], v[200:203]\n"
"buffer_load_dwordx4 v245, s[44:47], s90 offen offset:192 lds\n"
"v_mfma_f32_16x16x32_bf16 v[20:23], a[164:167], v[164:167], v[20:23]\n"
"v_exp_f32 v58, v58\n"
"v_mfma_f32_16x16x32_bf16 v[20:23], a[168:171], v[168:171], v[20:23]\n"
"ds_read_b64_tr_b16 v[116:117], v248 offset:8192\n"
"v_cvt_pk_bf16_f32 v92, v56, v57\n"
"v_mfma_f32_16x16x32_bf16 v[20:23], a[172:175], v[172:175], v[20:23]\n"
"v_exp_f32 v59, v59\n"
"v_mfma_f32_16x16x32_bf16 v[24:27], a[144:147], v[176:179], v[204:207]\n"
"ds_read_b64_tr_b16 v[118:119], v248 offset:9216\n"
"v_cvt_pk_bf16_f32 v93, v58, v59\n"
"v_mfma_f32_16x16x32_bf16 v[24:27], a[148:151], v[180:183], v[24:27]\n"
"v_exp_f32 v60, v60\n"
"v_mfma_f32_16x16x32_bf16 v[24:27], a[152:155], v[184:187], v[24:27]\n"
"ds_read_b64_tr_b16 v[120:121], v247 offset:12288\n"
"ds_read_b64_tr_b16 v[122:123], v247 offset:13312\n"
"v_mfma_f32_16x16x32_bf16 v[24:27], a[156:159], v[188:191], v[24:27]\n"
"v_exp_f32 v61, v61\n"
"v_mfma_f32_16x16x32_bf16 v[28:31], a[160:163], v[176:179], v[204:207]\n"
"ds_read_b64_tr_b16 v[124:125], v248 offset:12288\n"
"ds_read_b64_tr_b16 v[126:127], v248 offset:13312\n"
"v_mfma_f32_16x16x32_bf16 v[28:31], a[164:167], v[180:183], v[28:31]\n"
"v_exp_f32 v62, v62\n"
"v_mfma_f32_16x16x32_bf16 v[28:31], a[168:171], v[184:187], v[28:31]\n"
"v_cvt_pk_bf16_f32 v94, v60, v61\n"
"v_mfma_f32_16x16x32_bf16 v[28:31], a[172:175], v[188:191], v[28:31]\n"
"s_waitcnt lgkmcnt(12)\n"
"v_exp_f32 v63, v63\n"
"v_mfma_f32_16x16x32_bf16 a[0:3], v[96:99], v[80:83], a[0:3]\n"
"v_cvt_pk_bf16_f32 v95, v62, v63\n"
"v_mfma_f32_16x16x32_bf16 a[16:19], v[100:103], v[80:83], a[16:19]\n"
"s_waitcnt lgkmcnt(8)\n"
"v_exp_f32 v0, v0\n"
"v_mfma_f32_16x16x32_bf16 a[32:35], v[104:107], v[80:83], a[32:35]\n"
"ds_read_b128 a[144:147], v246 offset:2048\n"
"v_mfma_f32_16x16x32_bf16 a[48:51], v[108:111], v[80:83], a[48:51]\n"
"s_waitcnt lgkmcnt(5)\n"
"v_exp_f32 v1, v1\n"
"v_mfma_f32_16x16x32_bf16 a[64:67], v[112:115], v[80:83], a[64:67]\n"
"s_lshl_b32 s84, s78, 2\n"
"ds_read_b128 a[148:151], v246 offset:6144\n"
"v_cvt_pk_bf16_f32 v64, v0, v1\n"
"v_mfma_f32_16x16x32_bf16 a[80:83], v[116:119], v[80:83], a[80:83]\n"
"s_waitcnt lgkmcnt(2)\n"
"v_exp_f32 v2, v2\n"
"v_mfma_f32_16x16x32_bf16 a[96:99], v[120:123], v[80:83], a[96:99]\n"
"s_add_u32 s84, s84, s76\n"
"v_exp_f32 v3, v3\n"
"v_mfma_f32_16x16x32_bf16 a[112:115], v[124:127], v[80:83], a[112:115]\n"
"s_lshl_b32 s84, s84, 11\n"
"ds_read_b128 a[152:155], v246 offset:10240\n"
"v_cvt_pk_bf16_f32 v65, v2, v3\n"
"v_mfma_f32_16x16x32_bf16 a[128:131], v[212:215], v[80:83], a[128:131]\n"
"ds_read_b64_tr_b16 v[208:209], v216 offset:12288\n"
"v_and_b32 v254, 15, v224\n"
"v_mfma_f32_16x16x32_bf16 a[132:135], v[212:215], v[84:87], a[132:135]\n"
"v_lshlrev_b32 v254, 7, v254\n"
"s_add_u32 s84, s84, 0x10000\n"
"v_mfma_f32_16x16x32_bf16 a[116:119], v[124:127], v[84:87], a[116:119]\n"
"v_exp_f32 v4, v4\n"
"v_mfma_f32_16x16x32_bf16 a[100:103], v[120:123], v[84:87], a[100:103]\n"
"buffer_load_dword v249, v254, s[56:59], s84 offen sc1\n"
"s_mov_b32 s84, 0x2a0000\n"
"buffer_load_dword v250, v219, s[28:31], s84 offen\n"
"v_mfma_f32_16x16x32_bf16 a[84:87], v[116:119], v[84:87], a[84:87]\n"
"v_exp_f32 v5, v5\n"
"v_mfma_f32_16x16x32_bf16 a[68:71], v[112:115], v[84:87], a[68:71]\n"
"ds_read_b128 a[156:159], v246 offset:14336\n"
"v_cvt_pk_bf16_f32 v66, v4, v5\n"
"v_mfma_f32_16x16x32_bf16 a[52:55], v[108:111], v[84:87], a[52:55]\n"
"v_exp_f32 v6, v6\n"
"v_mfma_f32_16x16x32_bf16 a[36:39], v[104:107], v[84:87], a[36:39]\n"
"ds_read_b64_tr_b16 v[210:211], v216 offset:13312\n"
"v_mfma_f32_16x16x32_bf16 a[20:23], v[100:103], v[84:87], a[20:23]\n"
"s_add_u32 s99, s16, 0x6000\n"
"v_exp_f32 v7, v7\n"
"v_mfma_f32_16x16x32_bf16 a[4:7], v[96:99], v[84:87], a[4:7]\n"
"ds_read_b128 a[160:163], v246 offset:3072\n"
"v_cvt_pk_bf16_f32 v67, v6, v7\n"
"v_mfma_f32_16x16x32_bf16 a[8:11], v[96:99], v[88:91], a[8:11]\n"
"ds_read_b128 a[164:167], v246 offset:7168\n"
"v_mfma_f32_16x16x32_bf16 a[24:27], v[100:103], v[88:91], a[24:27]\n"
"v_exp_f32 v8, v8\n"
"v_mfma_f32_16x16x32_bf16 a[40:43], v[104:107], v[88:91], a[40:43]\n"
"v_exp_f32 v9, v9\n"
"v_mfma_f32_16x16x32_bf16 a[56:59], v[108:111], v[88:91], a[56:59]\n"
"s_waitcnt lgkmcnt(2)\n"
"buffer_store_dwordx4 v[208:211], v235, s[56:59], s99 offen sc1\n"
"v_mfma_f32_16x16x32_bf16 a[72:75], v[112:115], v[88:91], a[72:75]\n"
"ds_read_b128 a[168:171], v246 offset:11264\n"
"v_cvt_pk_bf16_f32 v68, v8, v9\n"
"v_mfma_f32_16x16x32_bf16 a[88:91], v[116:119], v[88:91], a[88:91]\n"
"v_exp_f32 v10, v10\n"
"v_mfma_f32_16x16x32_bf16 a[104:107], v[120:123], v[88:91], a[104:107]\n"
"ds_read_b128 a[172:175], v246 offset:15360\n"
"v_mfma_f32_16x16x32_bf16 a[120:123], v[124:127], v[88:91], a[120:123]\n"
"v_exp_f32 v11, v11\n"
"v_mfma_f32_16x16x32_bf16 a[136:139], v[212:215], v[88:91], a[136:139]\n"
"v_exp_f32 v12, v12\n"
"v_mfma_f32_16x16x32_bf16 a[140:143], v[212:215], v[92:95], a[140:143]\n"
"v_cvt_pk_bf16_f32 v69, v10, v11\n"
"v_mfma_f32_16x16x32_bf16 a[124:127], v[124:127], v[92:95], a[124:127]\n"
"v_exp_f32 v13, v13\n"
"v_mfma_f32_16x16x32_bf16 a[108:111], v[120:123], v[92:95], a[108:111]\n"
"v_cvt_pk_bf16_f32 v70, v12, v13\n"
"v_mfma_f32_16x16x32_bf16 a[92:95], v[116:119], v[92:95], a[92:95]\n"
"v_exp_f32 v14, v14\n"
"v_mfma_f32_16x16x32_bf16 a[76:79], v[112:115], v[92:95], a[76:79]\n"
"v_exp_f32 v15, v15\n"
"v_mfma_f32_16x16x32_bf16 a[60:63], v[108:111], v[92:95], a[60:63]\n"
"v_cvt_pk_bf16_f32 v71, v14, v15\n"
"v_mfma_f32_16x16x32_bf16 a[44:47], v[104:107], v[92:95], a[44:47]\n"
"v_exp_f32 v16, v16\n"
"v_mfma_f32_16x16x32_bf16 a[28:31], v[100:103], v[92:95], a[28:31]\n"
"s_mov_b32 s84, 0x300000\n"
"v_exp_f32 v17, v17\n"
"v_mfma_f32_16x16x32_bf16 a[12:15], v[96:99], v[92:95], a[12:15]\n"
"s_waitcnt lgkmcnt(8)\n"
"ds_read_b64_tr_b16 v[208:209], v216 offset:16384\n"
"v_cvt_pk_bf16_f32 v72, v16, v17\n"
"v_mfma_f32_16x16x32_bf16 v[32:35], a[144:147], v[128:131], v[192:195]\n"
"ds_read_b64_tr_b16 v[96:97], v247 offset:2048\n"
"v_mfma_f32_16x16x32_bf16 v[32:35], a[148:151], v[132:135], v[32:35]\n"
"s_waitcnt lgkmcnt(7)\n"
"buffer_load_dword v250, v219, s[28:31], s84 offen\n"
"v_mfma_f32_16x16x32_bf16 v[32:35], a[152:155], v[136:139], v[32:35]\n"
"ds_read_b64_tr_b16 v[98:99], v247 offset:3072\n"
"v_mfma_f32_16x16x32_bf16 v[32:35], a[156:159], v[140:143], v[32:35]\n"
"s_waitcnt lgkmcnt(5)\n"
"ds_read_b64_tr_b16 v[100:101], v248 offset:2048\n"
"v_mfma_f32_16x16x32_bf16 v[36:39], a[160:163], v[128:131], v[192:195]\n"
"v_exp_f32 v18, v18\n"
"v_mfma_f32_16x16x32_bf16 v[36:39], a[164:167], v[132:135], v[36:39]\n"
"s_waitcnt lgkmcnt(4)\n"
"ds_read_b64_tr_b16 v[210:211], v216 offset:17408\n"
"v_mfma_f32_16x16x32_bf16 v[36:39], a[168:171], v[136:139], v[36:39]\n"
"s_add_u32 s99, s16, 0x8000\n"
"v_exp_f32 v19, v19\n"
"v_mfma_f32_16x16x32_bf16 v[36:39], a[172:175], v[140:143], v[36:39]\n"
"ds_read_b64_tr_b16 v[102:103], v248 offset:3072\n"
"v_cvt_pk_bf16_f32 v73, v18, v19\n"
"v_mfma_f32_16x16x32_bf16 v[40:43], a[144:147], v[144:147], v[196:199]\n"
"v_exp_f32 v20, v20\n"
"v_mfma_f32_16x16x32_bf16 v[40:43], a[148:151], v[148:151], v[40:43]\n"
"ds_read_b64_tr_b16 v[104:105], v247 offset:6144\n"
"v_mfma_f32_16x16x32_bf16 v[40:43], a[152:155], v[152:155], v[40:43]\n"
"v_exp_f32 v21, v21\n"
"v_mfma_f32_16x16x32_bf16 v[40:43], a[156:159], v[156:159], v[40:43]\n"
"s_waitcnt lgkmcnt(2)\n"
"buffer_store_dwordx4 v[208:211], v217, s[56:59], s99 offen sc1\n"
"v_mfma_f32_16x16x32_bf16 v[44:47], a[160:163], v[144:147], v[196:199]\n"
"v_exp_f32 v22, v22\n"
"v_mfma_f32_16x16x32_bf16 v[44:47], a[164:167], v[148:151], v[44:47]\n"
"ds_read_b64_tr_b16 v[106:107], v247 offset:7168\n"
"v_cvt_pk_bf16_f32 v74, v20, v21\n"
"v_mfma_f32_16x16x32_bf16 v[44:47], a[168:171], v[152:155], v[44:47]\n"
"v_exp_f32 v23, v23\n"
"v_mfma_f32_16x16x32_bf16 v[44:47], a[172:175], v[156:159], v[44:47]\n"
"ds_read_b64_tr_b16 v[108:109], v248 offset:6144\n"
"v_cvt_pk_bf16_f32 v75, v22, v23\n"
"v_mfma_f32_16x16x32_bf16 v[48:51], a[144:147], v[160:163], v[200:203]\n"
"ds_read_b64_tr_b16 v[110:111], v248 offset:7168\n"
"ds_read_b64_tr_b16 v[112:113], v247 offset:10240\n"
"v_mfma_f32_16x16x32_bf16 v[48:51], a[148:151], v[164:167], v[48:51]\n"
"v_exp_f32 v24, v24\n"
"v_mfma_f32_16x16x32_bf16 v[48:51], a[152:155], v[168:171], v[48:51]\n"
"s_mov_b32 s84, 0x360000\n"
"ds_read_b64_tr_b16 v[114:115], v247 offset:11264\n"
"ds_read_b64_tr_b16 v[116:117], v248 offset:10240\n"
"v_mfma_f32_16x16x32_bf16 v[48:51], a[156:159], v[172:175], v[48:51]\n"
"v_exp_f32 v25, v25\n"
"v_mfma_f32_16x16x32_bf16 v[52:55], a[160:163], v[160:163], v[200:203]\n"
"ds_read_b64_tr_b16 v[118:119], v248 offset:11264\n"
"v_cvt_pk_bf16_f32 v76, v24, v25\n"
"v_mfma_f32_16x16x32_bf16 v[52:55], a[164:167], v[164:167], v[52:55]\n"
"v_exp_f32 v26, v26\n"
"v_mfma_f32_16x16x32_bf16 v[52:55], a[168:171], v[168:171], v[52:55]\n"
"buffer_load_dword v250, v219, s[28:31], s84 offen\n"
"v_mfma_f32_16x16x32_bf16 v[52:55], a[172:175], v[172:175], v[52:55]\n"
"v_exp_f32 v27, v27\n"
"v_mfma_f32_16x16x32_bf16 v[56:59], a[144:147], v[176:179], v[204:207]\n"
"ds_read_b64_tr_b16 v[120:121], v247 offset:14336\n"
"ds_read_b64_tr_b16 v[122:123], v247 offset:15360\n"
"ds_read_b64_tr_b16 v[124:125], v248 offset:14336\n"
"ds_read_b64_tr_b16 v[126:127], v248 offset:15360\n"
"v_mfma_f32_16x16x32_bf16 v[56:59], a[148:151], v[180:183], v[56:59]\n"
"v_exp_f32 v28, v28\n"
"v_mfma_f32_16x16x32_bf16 v[56:59], a[152:155], v[184:187], v[56:59]\n"
"v_cvt_pk_bf16_f32 v77, v26, v27\n"
"v_mfma_f32_16x16x32_bf16 v[56:59], a[156:159], v[188:191], v[56:59]\n"
"v_exp_f32 v29, v29\n"
"v_mfma_f32_16x16x32_bf16 v[60:63], a[160:163], v[176:179], v[204:207]\n"
"v_exp_f32 v30, v30\n"
"v_mfma_f32_16x16x32_bf16 v[60:63], a[164:167], v[180:183], v[60:63]\n"
"v_cvt_pk_bf16_f32 v78, v28, v29\n"
"v_mfma_f32_16x16x32_bf16 v[60:63], a[168:171], v[184:187], v[60:63]\n"
"v_exp_f32 v31, v31\n"
"v_mfma_f32_16x16x32_bf16 v[60:63], a[172:175], v[188:191], v[60:63]\n"
"s_waitcnt vmcnt(19) lgkmcnt(0)\n"
"s_waitcnt vmcnt(5)\n"
"v_cmp_ne_u32 vcc, s95, v249\n"
"s_cbranch_vccnz L_slow2\n"
"L_back2:\n"
"s_barrier\n"
"v_mfma_f32_16x16x32_bf16 a[0:3], v[96:99], v[64:67], a[0:3]\n"
"v_cvt_pk_bf16_f32 v79, v30, v31\n"
"v_mfma_f32_16x16x32_bf16 a[16:19], v[100:103], v[64:67], a[16:19]\n"
"s_mov_b32 s90, 0x20000\n"
"v_exp_f32 v32, v32\n"
"v_mfma_f32_16x16x32_bf16 a[32:35], v[104:107], v[64:67], a[32:35]\n"
"ds_read_b128 a[144:147], v246 offset:32768\n"
"v_mfma_f32_16x16x32_bf16 a[48:51], v[108:111], v[64:67], a[48:51]\n"
"s_add_u32 m0, s86, 0x1c000\n"
"v_exp_f32 v33, v33\n"
"v_mfma_f32_16x16x32_bf16 a[64:67], v[112:115], v[64:67], a[64:67]\n"
"ds_read_b128 a[148:151], v246 offset:36864\n"
"v_cvt_pk_bf16_f32 v80, v32, v33\n"
"v_mfma_f32_16x16x32_bf16 a[80:83], v[116:119], v[64:67], a[80:83]\n"
"v_exp_f32 v34, v34\n"
"v_mfma_f32_16x16x32_bf16 a[96:99], v[120:123], v[64:67], a[96:99]\n"
"v_exp_f32 v35, v35\n"
"v_mfma_f32_16x16x32_bf16 a[112:115], v[124:127], v[64:67], a[112:115]\n"
"ds_read_b128 a[152:155], v246 offset:40960\n"
"v_cvt_pk_bf16_f32 v81, v34, v35\n"
"v_mfma_f32_16x16x32_bf16 a[128:131], v[212:215], v[64:67], a[128:131]\n"
"buffer_load_dwordx4 v245, s[44:47], s90 offen lds\n"
"v_mfma_f32_16x16x32_bf16 a[132:135], v[212:215], v[68:71], a[132:135]\n"
"s_addk_i32 m0, 4032\n"
"v_exp_f32 v36, v36\n"
"v_mfma_f32_16x16x32_bf16 a[116:119], v[124:127], v[68:71], a[116:119]\n"
"ds_read_b128 a[156:159], v246 offset:45056\n"
"v_mfma_f32_16x16x32_bf16 a[100:103], v[120:123], v[68:71], a[100:103]\n"
"v_exp_f32 v37, v37\n"
"v_mfma_f32_16x16x32_bf16 a[84:87], v[116:119], v[68:71], a[84:87]\n"
"v_cvt_pk_bf16_f32 v82, v36, v37\n"
"v_mfma_f32_16x16x32_bf16 a[68:71], v[112:115], v[68:71], a[68:71]\n"
"buffer_load_dwordx4 v245, s[44:47], s90 offen offset:64 lds\n"
"v_mfma_f32_16x16x32_bf16 a[52:55], v[108:111], v[68:71], a[52:55]\n"
"s_addk_i32 m0, 4032\n"
"v_exp_f32 v38, v38\n"
"v_mfma_f32_16x16x32_bf16 a[36:39], v[104:107], v[68:71], a[36:39]\n"
"ds_read_b128 a[160:163], v246 offset:33792\n"
"v_mfma_f32_16x16x32_bf16 a[20:23], v[100:103], v[68:71], a[20:23]\n"
"v_exp_f32 v39, v39\n"
"v_mfma_f32_16x16x32_bf16 a[4:7], v[96:99], v[68:71], a[4:7]\n"
"ds_read_b128 a[164:167], v246 offset:37888\n"
"v_cvt_pk_bf16_f32 v83, v38, v39\n"
"v_mfma_f32_16x16x32_bf16 a[8:11], v[96:99], v[72:75], a[8:11]\n"
"buffer_load_dwordx4 v245, s[44:47], s90 offen offset:128 lds\n"
"v_mfma_f32_16x16x32_bf16 a[24:27], v[100:103], v[72:75], a[24:27]\n"
"s_addk_i32 m0, 4032\n"
"v_exp_f32 v40, v40\n"
"v_mfma_f32_16x16x32_bf16 a[40:43], v[104:107], v[72:75], a[40:43]\n"
"v_exp_f32 v41, v41\n"
"v_mfma_f32_16x16x32_bf16 a[56:59], v[108:111], v[72:75], a[56:59]\n"
"ds_read_b128 a[168:171], v246 offset:41984\n"
"v_cvt_pk_bf16_f32 v84, v40, v41\n"
"v_mfma_f32_16x16x32_bf16 a[72:75], v[112:115], v[72:75], a[72:75]\n"
"buffer_load_dwordx4 v245, s[44:47], s90 offen offset:192 lds\n"
"v_mfma_f32_16x16x32_bf16 a[88:91], v[116:119], v[72:75], a[88:91]\n"
"s_mul_i32 s84, s78, 0x144000\n"
"v_exp_f32 v42, v42\n"
"v_mfma_f32_16x16x32_bf16 a[104:107], v[120:123], v[72:75], a[104:107]\n"
"s_add_u32 s84, s84, 0x718000\n"
"ds_read_b128 a[172:175], v246 offset:46080\n"
"v_mfma_f32_16x16x32_bf16 a[120:123], v[124:127], v[72:75], a[120:123]\n"
"v_exp_f32 v43, v43\n"
"v_mfma_f32_16x16x32_bf16 a[136:139], v[212:215], v[72:75], a[136:139]\n"
"v_exp_f32 v44, v44\n"
"v_mfma_f32_16x16x32_bf16 a[140:143], v[212:215], v[76:79], a[140:143]\n"
"v_cvt_pk_bf16_f32 v85, v42, v43\n"
"v_mfma_f32_16x16x32_bf16 a[124:127], v[124:127], v[76:79], a[124:127]\n"
"v_exp_f32 v45, v45\n"
"v_mfma_f32_16x16x32_bf16 a[108:111], v[120:123], v[76:79], a[108:111]\n"
"buffer_load_dword v250, v222, s[56:59], s84 offen sc1\n"
"v_mfma_f32_16x16x32_bf16 a[92:95], v[116:119], v[76:79], a[92:95]\n"
"s_mul_i32 s84, s78, 0x144000\n"
"v_cvt_pk_bf16_f32 v86, v44, v45\n"
"v_mfma_f32_16x16x32_bf16 a[76:79], v[112:115], v[76:79], a[76:79]\n"
"s_add_u32 s84, s84, 0x719400\n"
"v_exp_f32 v46, v46\n"
"v_mfma_f32_16x16x32_bf16 a[60:63], v[108:111], v[76:79], a[60:63]\n"
"v_exp_f32 v47, v47\n"
"v_mfma_f32_16x16x32_bf16 a[44:47], v[104:107], v[76:79], a[44:47]\n"
"v_cvt_pk_bf16_f32 v87, v46, v47\n"
"v_mfma_f32_16x16x32_bf16 a[28:31], v[100:103], v[76:79], a[28:31]\n"
"v_exp_f32 v48, v48\n"
"v_mfma_f32_16x16x32_bf16 a[12:15], v[96:99], v[76:79], a[12:15]\n"
"s_waitcnt lgkmcnt(6)\n"
"buffer_load_dword v250, v222, s[56:59], s84 offen sc1\n"
"v_mfma_f32_16x16x32_bf16 v[0:3], a[144:147], v[128:131], v[192:195]\n"
"s_lshr_b32 s84, s77, 3\n"
"ds_read_b64_tr_b16 v[96:97], v247 offset:32768\n"
"v_mfma_f32_16x16x32_bf16 v[0:3], a[148:151], v[132:135], v[0:3]\n"
"s_waitcnt lgkmcnt(5)\n"
"v_exp_f32 v49, v49\n"
"v_mfma_f32_16x16x32_bf16 v[0:3], a[152:155], v[136:139], v[0:3]\n"
"s_lshl_b32 s84, s84, 2\n"
"ds_read_b64_tr_b16 v[98:99], v247 offset:33792\n"
"v_cvt_pk_bf16_f32 v88, v48, v49\n"
"v_mfma_f32_16x16x32_bf16 v[0:3], a[156:159], v[140:143], v[0:3]\n"
"s_waitcnt lgkmcnt(4)\n"
"v_exp_f32 v50, v50\n"
"v_mfma_f32_16x16x32_bf16 v[4:7], a[160:163], v[128:131], v[192:195]\n"
"s_add_u32 s84, s84, s76\n"
"ds_read_b64_tr_b16 v[100:101], v248 offset:32768\n"
"v_mfma_f32_16x16x32_bf16 v[4:7], a[164:167], v[132:135], v[4:7]\n"
"s_waitcnt lgkmcnt(3)\n"
"ds_read_b64_tr_b16 v[102:103], v248 offset:33792\n"
"v_mfma_f32_16x16x32_bf16 v[4:7], a[168:171], v[136:139], v[4:7]\n"
"v_exp_f32 v51, v51\n"
"v_mfma_f32_16x16x32_bf16 v[4:7], a[172:175], v[140:143], v[4:7]\n"
"s_lshl_b32 s84, s84, 7\n"
"ds_read_b64_tr_b16 v[104:105], v247 offset:36864\n"
"v_cvt_pk_bf16_f32 v89, v50, v51\n"
"v_mfma_f32_16x16x32_bf16 v[8:11], a[144:147], v[144:147], v[196:199]\n"
"v_exp_f32 v52, v52\n"
"v_mfma_f32_16x16x32_bf16 v[8:11], a[148:151], v[148:151], v[8:11]\n"
"s_add_u32 s84, s84, L_ct2-L_entry\n"
"ds_read_b64_tr_b16 v[106:107], v247 offset:37888\n"
"v_mfma_f32_16x16x32_bf16 v[8:11], a[152:155], v[152:155], v[8:11]\n"
"v_exp_f32 v53, v53\n"
"v_mfma_f32_16x16x32_bf16 v[8:11], a[156:159], v[156:159], v[8:11]\n"
"ds_read_b64_tr_b16 v[108:109], v248 offset:36864\n"
"v_cvt_pk_bf16_f32 v90, v52, v53\n"
"v_mfma_f32_16x16x32_bf16 v[12:15], a[160:163], v[144:147], v[196:199]\n"
"v_exp_f32 v54, v54\n"
"v_mfma_f32_16x16x32_bf16 v[12:15], a[164:167], v[148:151], v[12:15]\n"
"ds_read_b64_tr_b16 v[110:111], v248 offset:37888\n"
"v_and_b32 v254, 1, v224\n"
"v_mfma_f32_16x16x32_bf16 v[12:15], a[168:171], v[152:155], v[12:15]\n"
"v_exp_f32 v55, v55\n"
"v_mfma_f32_16x16x32_bf16 v[12:15], a[172:175], v[156:159], v[12:15]\n"
"ds_read_b64_tr_b16 v[112:113], v247 offset:40960\n"
"v_cvt_pk_bf16_f32 v91, v54, v55\n"
"v_mfma_f32_16x16x32_bf16 v[16:19], a[144:147], v[160:163], v[200:203]\n"
"ds_read_b64_tr_b16 v[114:115], v247 offset:41984\n"
"v_lshl_add_u32 v254, v254, 14, s84\n"
"v_mfma_f32_16x16x32_bf16 v[16:19], a[148:151], v[164:167], v[16:19]\n"
"v_exp_f32 v56, v56\n"
"v_mfma_f32_16x16x32_bf16 v[16:19], a[152:155], v[168:171], v[16:19]\n"
"ds_read_b64_tr_b16 v[116:117], v248 offset:40960\n"
"v_min_u32 v254, L_end-L_entry-4, v254\n"
"v_mfma_f32_16x16x32_bf16 v[16:19], a[156:159], v[172:175], v[16:19]\n"
"v_exp_f32 v57, v57\n"
"v_mfma_f32_16x16x32_bf16 v[20:23], a[160:163], v[160:163], v[200:203]\n"
"ds_read_b64_tr_b16 v[118:119], v248 offset:41984\n"
"v_cvt_pk_bf16_f32 v92, v56, v57\n"
"v_mfma_f32_16x16x32_bf16 v[20:23], a[164:167], v[164:167], v[20:23]\n"
"v_exp_f32 v58, v58\n"
"v_mfma_f32_16x16x32_bf16 v[20:23], a[168:171], v[168:171], v[20:23]\n"
"global_load_dword v250, v254, s[22:23]\n"
"v_mfma_f32_16x16x32_bf16 v[20:23], a[172:175], v[172:175], v[20:23]\n"
"v_exp_f32 v59, v59\n"
"v_mfma_f32_16x16x32_bf16 v[24:27], a[144:147], v[176:179], v[204:207]\n"
"ds_read_b64_tr_b16 v[120:121], v247 offset:45056\n"
"v_cvt_pk_bf16_f32 v93, v58, v59\n"
"v_mfma_f32_16x16x32_bf16 v[24:27], a[148:151], v[180:183], v[24:27]\n"
"v_exp_f32 v60, v60\n"
"v_mfma_f32_16x16x32_bf16 v[24:27], a[152:155], v[184:187], v[24:27]\n"
"ds_read_b64_tr_b16 v[122:123], v247 offset:46080\n"
"ds_read_b64_tr_b16 v[124:125], v248 offset:45056\n"
"v_mfma_f32_16x16x32_bf16 v[24:27], a[156:159], v[188:191], v[24:27]\n"
"v_exp_f32 v61, v61\n"
"v_mfma_f32_16x16x32_bf16 v[28:31], a[160:163], v[176:179], v[204:207]\n"
"ds_read_b64_tr_b16 v[126:127], v248 offset:46080\n"
"v_cvt_pk_bf16_f32 v94, v60, v61\n"
"v_mfma_f32_16x16x32_bf16 v[28:31], a[164:167], v[180:183], v[28:31]\n"
"v_exp_f32 v62, v62\n"
"v_mfma_f32_16x16x32_bf16 v[28:31], a[168:171], v[184:187], v[28:31]\n"
"v_exp_f32 v63, v63\n"
"v_mfma_f32_16x16x32_bf16 v[28:31], a[172:175], v[188:191], v[28:31]\n"
"s_waitcnt lgkmcnt(12)\n"
"v_cvt_pk_bf16_f32 v95, v62, v63\n"
"v_mfma_f32_16x16x32_bf16 a[0:3], v[96:99], v[80:83], a[0:3]\n"
"v_exp_f32 v0, v0\n"
"v_mfma_f32_16x16x32_bf16 a[16:19], v[100:103], v[80:83], a[16:19]\n"
"s_waitcnt lgkmcnt(8)\n"
"ds_read_b128 a[144:147], v246 offset:34816\n"
"v_mfma_f32_16x16x32_bf16 a[32:35], v[104:107], v[80:83], a[32:35]\n"
"s_mov_b32 s84, 0x3c0000\n"
"v_exp_f32 v1, v1\n"
"v_mfma_f32_16x16x32_bf16 a[48:51], v[108:111], v[80:83], a[48:51]\n"
"s_waitcnt lgkmcnt(5)\n"
"v_cvt_pk_bf16_f32 v64, v0, v1\n"
"v_mfma_f32_16x16x32_bf16 a[64:67], v[112:115], v[80:83], a[64:67]\n"
"ds_read_b128 a[148:151], v246 offset:38912\n"
"v_mfma_f32_16x16x32_bf16 a[80:83], v[116:119], v[80:83], a[80:83]\n"
"s_waitcnt lgkmcnt(2)\n"
"v_exp_f32 v2, v2\n"
"v_mfma_f32_16x16x32_bf16 a[96:99], v[120:123], v[80:83], a[96:99]\n"
"buffer_load_dword v250, v219, s[28:31], s84 offen\n"
"v_mfma_f32_16x16x32_bf16 a[112:115], v[124:127], v[80:83], a[112:115]\n"
"v_exp_f32 v3, v3\n"
"v_mfma_f32_16x16x32_bf16 a[128:131], v[212:215], v[80:83], a[128:131]\n"
"ds_read_b128 a[152:155], v246 offset:43008\n"
"v_cvt_pk_bf16_f32 v65, v2, v3\n"
"v_mfma_f32_16x16x32_bf16 a[132:135], v[212:215], v[84:87], a[132:135]\n"
"v_exp_f32 v4, v4\n"
"v_mfma_f32_16x16x32_bf16 a[116:119], v[124:127], v[84:87], a[116:119]\n"
"ds_read_b128 a[156:159], v246 offset:47104\n"
"v_mfma_f32_16x16x32_bf16 a[100:103], v[120:123], v[84:87], a[100:103]\n"
"v_exp_f32 v5, v5\n"
"v_mfma_f32_16x16x32_bf16 a[84:87], v[116:119], v[84:87], a[84:87]\n"
"v_cvt_pk_bf16_f32 v66, v4, v5\n"
"v_mfma_f32_16x16x32_bf16 a[68:71], v[112:115], v[84:87], a[68:71]\n"
"v_exp_f32 v6, v6\n"
"v_mfma_f32_16x16x32_bf16 a[52:55], v[108:111], v[84:87], a[52:55]\n"
"ds_read_b128 a[160:163], v246 offset:35840\n"
"v_mfma_f32_16x16x32_bf16 a[36:39], v[104:107], v[84:87], a[36:39]\n"
"v_exp_f32 v7, v7\n"
"v_mfma_f32_16x16x32_bf16 a[20:23], v[100:103], v[84:87], a[20:23]\n"
"v_cvt_pk_bf16_f32 v67, v6, v7\n"
"v_mfma_f32_16x16x32_bf16 a[4:7], v[96:99], v[84:87], a[4:7]\n"
"ds_read_b128 a[164:167], v246 offset:39936\n"
"v_mfma_f32_16x16x32_bf16 a[8:11], v[96:99], v[88:91], a[8:11]\n"
"v_exp_f32 v8, v8\n"
"v_mfma_f32_16x16x32_bf16 a[24:27], v[100:103], v[88:91], a[24:27]\n"
"v_exp_f32 v9, v9\n"
"v_mfma_f32_16x16x32_bf16 a[40:43], v[104:107], v[88:91], a[40:43]\n"
"ds_read_b128 a[168:171], v246 offset:44032\n"
"v_cvt_pk_bf16_f32 v68, v8, v9\n"
"v_mfma_f32_16x16x32_bf16 a[56:59], v[108:111], v[88:91], a[56:59]\n"
"v_exp_f32 v10, v10\n"
"v_mfma_f32_16x16x32_bf16 a[72:75], v[112:115], v[88:91], a[72:75]\n"
"s_mov_b32 s84, 0x420000\n"
"v_exp_f32 v11, v11\n"
"v_mfma_f32_16x16x32_bf16 a[88:91], v[116:119], v[88:91], a[88:91]\n"
"ds_read_b128 a[172:175], v246 offset:48128\n"
"v_cvt_pk_bf16_f32 v69, v10, v11\n"
"v_mfma_f32_16x16x32_bf16 a[104:107], v[120:123], v[88:91], a[104:107]\n"
"buffer_load_dword v250, v219, s[28:31], s84 offen\n"
"v_mfma_f32_16x16x32_bf16 a[120:123], v[124:127], v[88:91], a[120:123]\n"
"v_exp_f32 v12, v12\n"
"v_mfma_f32_16x16x32_bf16 a[136:139], v[212:215], v[88:91], a[136:139]\n"
"v_exp_f32 v13, v13\n"
"v_mfma_f32_16x16x32_bf16 a[140:143], v[212:215], v[92:95], a[140:143]\n"
"v_cvt_pk_bf16_f32 v70, v12, v13\n"
"v_mfma_f32_16x16x32_bf16 a[124:127], v[124:127], v[92:95], a[124:127]\n"
"v_exp_f32 v14, v14\n"
"v_mfma_f32_16x16x32_bf16 a[108:111], v[120:123], v[92:95], a[108:111]\n"
"v_exp_f32 v15, v15\n"
"v_mfma_f32_16x16x32_bf16 a[92:95], v[116:119], v[92:95], a[92:95]\n"
"v_cvt_pk_bf16_f32 v71, v14, v15\n"
"v_mfma_f32_16x16x32_bf16 a[76:79], v[112:115], v[92:95], a[76:79]\n"
"v_mfma_f32_16x16x32_bf16 a[60:63], v[108:111], v[92:95], a[60:63]\n"
"v_exp_f32 v16, v16\n"
"v_mfma_f32_16x16x32_bf16 a[44:47], v[104:107], v[92:95], a[44:47]\n"
"v_mfma_f32_16x16x32_bf16 a[28:31], v[100:103], v[92:95], a[28:31]\n"
"v_exp_f32 v17, v17\n"
"v_mfma_f32_16x16x32_bf16 a[12:15], v[96:99], v[92:95], a[12:15]\n"
"s_waitcnt lgkmcnt(6)\n"
"v_cvt_pk_bf16_f32 v72, v16, v17\n"
"v_mfma_f32_16x16x32_bf16 v[32:35], a[144:147], v[128:131], v[192:195]\n"
"ds_read_b64_tr_b16 v[96:97], v247 offset:34816\n"
"v_mfma_f32_16x16x32_bf16 v[32:35], a[148:151], v[132:135], v[32:35]\n"
"s_waitcnt lgkmcnt(5)\n"
"ds_read_b64_tr_b16 v[98:99], v247 offset:35840\n"
"v_mfma_f32_16x16x32_bf16 v[32:35], a[152:155], v[136:139], v[32:35]\n"
"ds_read_b64_tr_b16 v[100:101], v248 offset:34816\n"
"v_mfma_f32_16x16x32_bf16 v[32:35], a[156:159], v[140:143], v[32:35]\n"
"s_waitcnt lgkmcnt(5)\n"
"v_exp_f32 v18, v18\n"
"v_mfma_f32_16x16x32_bf16 v[36:39], a[160:163], v[128:131], v[192:195]\n"
"ds_read_b64_tr_b16 v[102:103], v248 offset:35840\n"
"v_mfma_f32_16x16x32_bf16 v[36:39], a[164:167], v[132:135], v[36:39]\n"
"s_waitcnt lgkmcnt(4)\n"
"v_exp_f32 v19, v19\n"
"v_mfma_f32_16x16x32_bf16 v[36:39], a[168:171], v[136:139], v[36:39]\n"
"ds_read_b64_tr_b16 v[104:105], v247 offset:38912\n"
"v_cvt_pk_bf16_f32 v73, v18, v19\n"
"v_mfma_f32_16x16x32_bf16 v[36:39], a[172:175], v[140:143], v[36:39]\n"
"ds_read_b64_tr_b16 v[106:107], v247 offset:39936\n"
"v_mfma_f32_16x16x32_bf16 v[40:43], a[144:147], v[144:147], v[196:199]\n"
"v_exp_f32 v20, v20\n"
"v_mfma_f32_16x16x32_bf16 v[40:43], a[148:151], v[148:151], v[40:43]\n"
"ds_read_b64_tr_b16 v[108:109], v248 offset:38912\n"
"v_mfma_f32_16x16x32_bf16 v[40:43], a[152:155], v[152:155], v[40:43]\n"
"v_exp_f32 v21, v21\n"
"v_mfma_f32_16x16x32_bf16 v[40:43], a[156:159], v[156:159], v[40:43]\n"
"ds_read_b64_tr_b16 v[110:111], v248 offset:39936\n"
"v_cvt_pk_bf16_f32 v74, v20, v21\n"
"v_mfma_f32_16x16x32_bf16 v[44:47], a[160:163], v[144:147], v[196:199]\n"
"v_exp_f32 v22, v22\n"
"v_mfma_f32_16x16x32_bf16 v[44:47], a[164:167], v[148:151], v[44:47]\n"
"ds_read_b64_tr_b16 v[112:113], v247 offset:43008\n"
"v_mfma_f32_16x16x32_bf16 v[44:47], a[168:171], v[152:155], v[44:47]\n"
"v_exp_f32 v23, v23\n"
"v_mfma_f32_16x16x32_bf16 v[44:47], a[172:175], v[156:159], v[44:47]\n"
"ds_read_b64_tr_b16 v[114:115], v247 offset:44032\n"
"v_cvt_pk_bf16_f32 v75, v22, v23\n"
"v_mfma_f32_16x16x32_bf16 v[48:51], a[144:147], v[160:163], v[200:203]\n"
"ds_read_b64_tr_b16 v[116:117], v248 offset:43008\n"
"v_mfma_f32_16x16x32_bf16 v[48:51], a[148:151], v[164:167], v[48:51]\n"
"v_exp_f32 v24, v24\n"
"v_mfma_f32_16x16x32_bf16 v[48:51], a[152:155], v[168:171], v[48:51]\n"
"ds_read_b64_tr_b16 v[118:119], v248 offset:44032\n"
"v_mfma_f32_16x16x32_bf16 v[48:51], a[156:159], v[172:175], v[48:51]\n"
"v_exp_f32 v25, v25\n"
"v_mfma_f32_16x16x32_bf16 v[52:55], a[160:163], v[160:163], v[200:203]\n"
"ds_read_b64_tr_b16 v[120:121], v247 offset:47104\n"
"v_cvt_pk_bf16_f32 v76, v24, v25\n"
"v_mfma_f32_16x16x32_bf16 v[52:55], a[164:167], v[164:167], v[52:55]\n"
"v_exp_f32 v26, v26\n"
"v_mfma_f32_16x16x32_bf16 v[52:55], a[168:171], v[168:171], v[52:55]\n"
"ds_read_b64_tr_b16 v[122:123], v247 offset:48128\n"
"ds_read_b64_tr_b16 v[124:125], v248 offset:47104\n"
"v_mfma_f32_16x16x32_bf16 v[52:55], a[172:175], v[172:175], v[52:55]\n"
"v_exp_f32 v27, v27\n"
"v_mfma_f32_16x16x32_bf16 v[56:59], a[144:147], v[176:179], v[204:207]\n"
"ds_read_b64_tr_b16 v[126:127], v248 offset:48128\n"
"v_cvt_pk_bf16_f32 v77, v26, v27\n"
"v_mfma_f32_16x16x32_bf16 v[56:59], a[148:151], v[180:183], v[56:59]\n"
"v_exp_f32 v28, v28\n"
"v_mfma_f32_16x16x32_bf16 v[56:59], a[152:155], v[184:187], v[56:59]\n"
"v_exp_f32 v29, v29\n"
"v_mfma_f32_16x16x32_bf16 v[56:59], a[156:159], v[188:191], v[56:59]\n"
"v_exp_f32 v30, v30\n"
"v_mfma_f32_16x16x32_bf16 v[60:63], a[160:163], v[176:179], v[204:207]\n"
"v_cvt_pk_bf16_f32 v78, v28, v29\n"
"v_mfma_f32_16x16x32_bf16 v[60:63], a[164:167], v[180:183], v[60:63]\n"
"v_exp_f32 v31, v31\n"
"v_mfma_f32_16x16x32_bf16 v[60:63], a[168:171], v[184:187], v[60:63]\n"
"v_exp_f32 v32, v32\n"
"v_mfma_f32_16x16x32_bf16 v[60:63], a[172:175], v[188:191], v[60:63]\n"
"s_waitcnt vmcnt(15) lgkmcnt(0)\n"
"s_barrier\n"
"v_cvt_pk_bf16_f32 v79, v30, v31\n"
"v_mfma_f32_16x16x32_bf16 a[0:3], v[96:99], v[64:67], a[0:3]\n"
"v_exp_f32 v33, v33\n"
"v_mfma_f32_16x16x32_bf16 a[16:19], v[100:103], v[64:67], a[16:19]\n"
"s_mov_b32 s92, s4\n"
"v_exp_f32 v34, v34\n"
"v_mfma_f32_16x16x32_bf16 a[32:35], v[104:107], v[64:67], a[32:35]\n"
"ds_read_b128 a[144:147], v231\n"
"v_cvt_pk_bf16_f32 v80, v32, v33\n"
"v_mfma_f32_16x16x32_bf16 a[48:51], v[108:111], v[64:67], a[48:51]\n"
"v_exp_f32 v35, v35\n"
"v_mfma_f32_16x16x32_bf16 a[64:67], v[112:115], v[64:67], a[64:67]\n"
"ds_read_b128 a[148:151], v231 offset:4096\n"
"v_cvt_pk_bf16_f32 v81, v34, v35\n"
"v_mfma_f32_16x16x32_bf16 a[80:83], v[116:119], v[64:67], a[80:83]\n"
"v_exp_f32 v36, v36\n"
"v_mfma_f32_16x16x32_bf16 a[96:99], v[120:123], v[64:67], a[96:99]\n"
"v_exp_f32 v37, v37\n"
"v_mfma_f32_16x16x32_bf16 a[112:115], v[124:127], v[64:67], a[112:115]\n"
"ds_read_b128 a[152:155], v231 offset:8192\n"
"v_cvt_pk_bf16_f32 v82, v36, v37\n"
"v_mfma_f32_16x16x32_bf16 a[128:131], v[212:215], v[64:67], a[128:131]\n"
"s_add_u32 m0, s88, 0\n"
"v_exp_f32 v38, v38\n"
"v_mfma_f32_16x16x32_bf16 a[132:135], v[212:215], v[68:71], a[132:135]\n"
"v_exp_f32 v39, v39\n"
"v_mfma_f32_16x16x32_bf16 a[116:119], v[124:127], v[68:71], a[116:119]\n"
"ds_read_b128 a[156:159], v231 offset:12288\n"
"v_cvt_pk_bf16_f32 v83, v38, v39\n"
"v_mfma_f32_16x16x32_bf16 a[100:103], v[120:123], v[68:71], a[100:103]\n"
"v_exp_f32 v40, v40\n"
"v_mfma_f32_16x16x32_bf16 a[84:87], v[116:119], v[68:71], a[84:87]\n"
"v_mfma_f32_16x16x32_bf16 a[68:71], v[112:115], v[68:71], a[68:71]\n"
"v_exp_f32 v41, v41\n"
"v_mfma_f32_16x16x32_bf16 a[52:55], v[108:111], v[68:71], a[52:55]\n"
"ds_read_b128 a[160:163], v231 offset:1024\n"
"v_cvt_pk_bf16_f32 v84, v40, v41\n"
"v_mfma_f32_16x16x32_bf16 a[36:39], v[104:107], v[68:71], a[36:39]\n"
"v_exp_f32 v42, v42\n"
"v_mfma_f32_16x16x32_bf16 a[20:23], v[100:103], v[68:71], a[20:23]\n"
"s_mov_b32 exec_hi, 0\n"
"buffer_load_dwordx4 v233, s[56:59], s92 offen sc1 lds\n"
"s_mov_b32 exec_hi, -1\n"
"v_mfma_f32_16x16x32_bf16 a[4:7], v[96:99], v[68:71], a[4:7]\n"
"ds_read_b128 a[164:167], v231 offset:5120\n"
"v_mfma_f32_16x16x32_bf16 a[8:11], v[96:99], v[72:75], a[8:11]\n"
"v_exp_f32 v43, v43\n"
"v_mfma_f32_16x16x32_bf16 a[24:27], v[100:103], v[72:75], a[24:27]\n"
"v_cvt_pk_bf16_f32 v85, v42, v43\n"
"v_mfma_f32_16x16x32_bf16 a[40:43], v[104:107], v[72:75], a[40:43]\n"
"ds_read_b128 a[168:171], v231 offset:9216\n"
"v_mfma_f32_16x16x32_bf16 a[56:59], v[108:111], v[72:75], a[56:59]\n"
"v_exp_f32 v44, v44\n"
"v_mfma_f32_16x16x32_bf16 a[72:75], v[112:115], v[72:75], a[72:75]\n"
"v_exp_f32 v45, v45\n"
"v_mfma_f32_16x16x32_bf16 a[88:91], v[116:119], v[72:75], a[88:91]\n"
"ds_read_b128 a[172:175], v231 offset:13312\n"
"v_cvt_pk_bf16_f32 v86, v44, v45\n"
"v_mfma_f32_16x16x32_bf16 a[104:107], v[120:123], v[72:75], a[104:107]\n"
"buffer_load_dwordx4 v233, s[56:59], s92 offen offset:512 sc1 lds\n"
"v_mfma_f32_16x16x32_bf16 a[120:123], v[124:127], v[72:75], a[120:123]\n"
"v_exp_f32 v46, v46\n"
"v_mfma_f32_16x16x32_bf16 a[136:139], v[212:215], v[72:75], a[136:139]\n"
"v_exp_f32 v47, v47\n"
"v_mfma_f32_16x16x32_bf16 a[140:143], v[212:215], v[76:79], a[140:143]\n"
"v_cvt_pk_bf16_f32 v87, v46, v47\n"
"v_mfma_f32_16x16x32_bf16 a[124:127], v[124:127], v[76:79], a[124:127]\n"
"v_exp_f32 v48, v48\n"
"v_mfma_f32_16x16x32_bf16 a[108:111], v[120:123], v[76:79], a[108:111]\n"
"v_mfma_f32_16x16x32_bf16 a[92:95], v[116:119], v[76:79], a[92:95]\n"
"v_exp_f32 v49, v49\n"
"v_mfma_f32_16x16x32_bf16 a[76:79], v[112:115], v[76:79], a[76:79]\n"
"v_cvt_pk_bf16_f32 v88, v48, v49\n"
"v_mfma_f32_16x16x32_bf16 a[60:63], v[108:111], v[76:79], a[60:63]\n"
"buffer_load_dwordx4 v233, s[56:59], s92 offen offset:1536 sc1 lds\n"
"v_mfma_f32_16x16x32_bf16 a[44:47], v[104:107], v[76:79], a[44:47]\n"
"v_exp_f32 v50, v50\n"
"v_mfma_f32_16x16x32_bf16 a[28:31], v[100:103], v[76:79], a[28:31]\n"
"v_exp_f32 v51, v51\n"
"v_mfma_f32_16x16x32_bf16 a[12:15], v[96:99], v[76:79], a[12:15]\n"
"s_waitcnt lgkmcnt(6)\n"
"v_cvt_pk_bf16_f32 v89, v50, v51\n"
"v_mfma_f32_16x16x32_bf16 v[0:3], a[144:147], v[128:131], v[192:195]\n"
"ds_read_b64_tr_b16 v[96:97], v232\n"
"v_mfma_f32_16x16x32_bf16 v[0:3], a[148:151], v[132:135], v[0:3]\n"
"s_waitcnt lgkmcnt(5)\n"
"ds_read_b64_tr_b16 v[98:99], v232 offset:1024\n"
"v_mfma_f32_16x16x32_bf16 v[0:3], a[152:155], v[136:139], v[0:3]\n"
"v_exp_f32 v52, v52\n"
"v_mfma_f32_16x16x32_bf16 v[0:3], a[156:159], v[140:143], v[0:3]\n"
"s_waitcnt lgkmcnt(4)\n"
"ds_read_b64_tr_b16 v[100:101], v252\n"
"v_mfma_f32_16x16x32_bf16 v[4:7], a[160:163], v[128:131], v[192:195]\n"
"buffer_load_dwordx4 v233, s[56:59], s92 offen offset:2560 sc1 lds\n"
"v_mfma_f32_16x16x32_bf16 v[4:7], a[164:167], v[132:135], v[4:7]\n"
"s_waitcnt lgkmcnt(3)\n"
"ds_read_b64_tr_b16 v[102:103], v252 offset:1024\n"
"v_mfma_f32_16x16x32_bf16 v[4:7], a[168:171], v[136:139], v[4:7]\n"
"v_exp_f32 v53, v53\n"
"v_mfma_f32_16x16x32_bf16 v[4:7], a[172:175], v[140:143], v[4:7]\n"
"ds_read_b64_tr_b16 v[104:105], v232 offset:4096\n"
"v_cvt_pk_bf16_f32 v90, v52, v53\n"
"v_mfma_f32_16x16x32_bf16 v[8:11], a[144:147], v[144:147], v[196:199]\n"
"v_exp_f32 v54, v54\n"
"v_mfma_f32_16x16x32_bf16 v[8:11], a[148:151], v[148:151], v[8:11]\n"
"ds_read_b64_tr_b16 v[106:107], v232 offset:5120\n"
"v_mfma_f32_16x16x32_bf16 v[8:11], a[152:155], v[152:155], v[8:11]\n"
"v_exp_f32 v55, v55\n"
"v_mfma_f32_16x16x32_bf16 v[8:11], a[156:159], v[156:159], v[8:11]\n"
"ds_read_b64_tr_b16 v[108:109], v252 offset:4096\n"
"v_cvt_pk_bf16_f32 v91, v54, v55\n"
"v_mfma_f32_16x16x32_bf16 v[12:15], a[160:163], v[144:147], v[196:199]\n"
"ds_read_b64_tr_b16 v[110:111], v252 offset:5120\n"
"v_mfma_f32_16x16x32_bf16 v[12:15], a[164:167], v[148:151], v[12:15]\n"
"v_exp_f32 v56, v56\n"
"v_mfma_f32_16x16x32_bf16 v[12:15], a[168:171], v[152:155], v[12:15]\n"
"buffer_load_dwordx4 v233, s[56:59], s92 offen offset:3584 sc1 lds\n"
"v_mfma_f32_16x16x32_bf16 v[12:15], a[172:175], v[156:159], v[12:15]\n"
"v_exp_f32 v57, v57\n"
"v_mfma_f32_16x16x32_bf16 v[16:19], a[144:147], v[160:163], v[200:203]\n"
"ds_read_b64_tr_b16 v[112:113], v232 offset:8192\n"
"v_cvt_pk_bf16_f32 v92, v56, v57\n"
"v_mfma_f32_16x16x32_bf16 v[16:19], a[148:151], v[164:167], v[16:19]\n"
"s_mov_b32 s93, 0\n"
"v_exp_f32 v58, v58\n"
"v_mfma_f32_16x16x32_bf16 v[16:19], a[152:155], v[168:171], v[16:19]\n"
"ds_read_b64_tr_b16 v[114:115], v232 offset:9216\n"
"v_mfma_f32_16x16x32_bf16 v[16:19], a[156:159], v[172:175], v[16:19]\n"
"v_exp_f32 v59, v59\n"
"v_mfma_f32_16x16x32_bf16 v[20:23], a[160:163], v[160:163], v[200:203]\n"
"ds_read_b64_tr_b16 v[116:117], v252 offset:8192\n"
"v_cvt_pk_bf16_f32 v93, v58, v59\n"
"v_mfma_f32_16x16x32_bf16 v[20:23], a[164:167], v[164:167], v[20:23]\n"
"v_exp_f32 v60, v60\n"
"v_mfma_f32_16x16x32_bf16 v[20:23], a[168:171], v[168:171], v[20:23]\n"
"ds_read_b64_tr_b16 v[118:119], v252 offset:9216\n"
"v_mfma_f32_16x16x32_bf16 v[20:23], a[172:175], v[172:175], v[20:23]\n"
"v_exp_f32 v61, v61\n"
"v_mfma_f32_16x16x32_bf16 v[24:27], a[144:147], v[176:179], v[204:207]\n"
"ds_read_b64_tr_b16 v[120:121], v232 offset:12288\n"
"v_cvt_pk_bf16_f32 v94, v60, v61\n"
"v_mfma_f32_16x16x32_bf16 v[24:27], a[148:151], v[180:183], v[24:27]\n"
"s_add_u32 m0, s85, 0x4800\n"
"v_exp_f32 v62, v62\n"
"v_mfma_f32_16x16x32_bf16 v[24:27], a[152:155], v[184:187], v[24:27]\n"
"ds_read_b64_tr_b16 v[122:123], v232 offset:13312\n"
"ds_read_b64_tr_b16 v[124:125], v252 offset:12288\n"
"v_mfma_f32_16x16x32_bf16 v[24:27], a[156:159], v[188:191], v[24:27]\n"
"v_exp_f32 v63, v63\n"
"v_exp_f32 v0, v0\n"
"v_exp_f32 v1, v1\n"
"v_mfma_f32_16x16x32_bf16 v[28:31], a[160:163], v[176:179], v[204:207]\n"
"ds_read_b64_tr_b16 v[126:127], v252 offset:13312\n"
"v_cvt_pk_bf16_f32 v95, v62, v63\n"
"v_mfma_f32_16x16x32_bf16 v[28:31], a[164:167], v[180:183], v[28:31]\n"
"v_exp_f32 v2, v2\n"
"v_mfma_f32_16x16x32_bf16 v[28:31], a[168:171], v[184:187], v[28:31]\n"
"v_cvt_pk_bf16_f32 v64, v0, v1\n"
"v_mfma_f32_16x16x32_bf16 v[28:31], a[172:175], v[188:191], v[28:31]\n"
"s_waitcnt lgkmcnt(12)\n"
"v_exp_f32 v3, v3\n"
"v_mfma_f32_16x16x32_bf16 a[0:3], v[96:99], v[80:83], a[0:3]\n"
"v_exp_f32 v4, v4\n"
"v_mfma_f32_16x16x32_bf16 a[16:19], v[100:103], v[80:83], a[16:19]\n"
"s_waitcnt lgkmcnt(8)\n"
"ds_read_b128 a[144:147], v231 offset:2048\n"
"v_cvt_pk_bf16_f32 v65, v2, v3\n"
"v_mfma_f32_16x16x32_bf16 a[32:35], v[104:107], v[80:83], a[32:35]\n"
"v_exp_f32 v5, v5\n"
"v_mfma_f32_16x16x32_bf16 a[48:51], v[108:111], v[80:83], a[48:51]\n"
"s_waitcnt lgkmcnt(5)\n"
"buffer_load_dwordx4 v225, s[52:55], s93 offen lds\n"
"v_mfma_f32_16x16x32_bf16 a[64:67], v[112:115], v[80:83], a[64:67]\n"
"ds_read_b128 a[148:151], v231 offset:6144\n"
"v_cvt_pk_bf16_f32 v66, v4, v5\n"
"v_mfma_f32_16x16x32_bf16 a[80:83], v[116:119], v[80:83], a[80:83]\n"
"s_waitcnt lgkmcnt(2)\n"
"v_exp_f32 v6, v6\n"
"v_mfma_f32_16x16x32_bf16 a[96:99], v[120:123], v[80:83], a[96:99]\n"
"v_exp_f32 v7, v7\n"
"v_mfma_f32_16x16x32_bf16 a[112:115], v[124:127], v[80:83], a[112:115]\n"
"ds_read_b128 a[152:155], v231 offset:10240\n"
"v_cvt_pk_bf16_f32 v67, v6, v7\n"
"v_mfma_f32_16x16x32_bf16 a[128:131], v[212:215], v[80:83], a[128:131]\n"
"s_addk_i32 m0, 1984\n"
"v_exp_f32 v8, v8\n"
"v_mfma_f32_16x16x32_bf16 a[132:135], v[212:215], v[84:87], a[132:135]\n"
"v_exp_f32 v9, v9\n"
"v_mfma_f32_16x16x32_bf16 a[116:119], v[124:127], v[84:87], a[116:119]\n"
"ds_read_b128 a[156:159], v231 offset:14336\n"
"v_cvt_pk_bf16_f32 v68, v8, v9\n"
"v_mfma_f32_16x16x32_bf16 a[100:103], v[120:123], v[84:87], a[100:103]\n"
"v_exp_f32 v10, v10\n"
"v_mfma_f32_16x16x32_bf16 a[84:87], v[116:119], v[84:87], a[84:87]\n"
"v_exp_f32 v11, v11\n"
"v_mfma_f32_16x16x32_bf16 a[68:71], v[112:115], v[84:87], a[68:71]\n"
"v_cvt_pk_bf16_f32 v69, v10, v11\n"
"v_mfma_f32_16x16x32_bf16 a[52:55], v[108:111], v[84:87], a[52:55]\n"
"ds_read_b128 a[160:163], v231 offset:3072\n"
"v_mfma_f32_16x16x32_bf16 a[36:39], v[104:107], v[84:87], a[36:39]\n"
"v_exp_f32 v12, v12\n"
"v_mfma_f32_16x16x32_bf16 a[20:23], v[100:103], v[84:87], a[20:23]\n"
"buffer_load_dwordx4 v225, s[52:55], s93 offen offset:64 lds\n"
"v_mfma_f32_16x16x32_bf16 a[4:7], v[96:99], v[84:87], a[4:7]\n"
"ds_read_b128 a[164:167], v231 offset:7168\n"
"v_mfma_f32_16x16x32_bf16 a[8:11], v[96:99], v[88:91], a[8:11]\n"
"v_exp_f32 v13, v13\n"
"v_mfma_f32_16x16x32_bf16 a[24:27], v[100:103], v[88:91], a[24:27]\n"
"v_cvt_pk_bf16_f32 v70, v12, v13\n"
"v_mfma_f32_16x16x32_bf16 a[40:43], v[104:107], v[88:91], a[40:43]\n"
"ds_read_b128 a[168:171], v231 offset:11264\n"
"v_mfma_f32_16x16x32_bf16 a[56:59], v[108:111], v[88:91], a[56:59]\n"
"v_exp_f32 v14, v14\n"
"v_mfma_f32_16x16x32_bf16 a[72:75], v[112:115], v[88:91], a[72:75]\n"
"v_exp_f32 v15, v15\n"
"v_mfma_f32_16x16x32_bf16 a[88:91], v[116:119], v[88:91], a[88:91]\n"
"s_add_u32 s93, s93, 0x20000\n"
"ds_read_b128 a[172:175], v231 offset:15360\n"
"v_cvt_pk_bf16_f32 v71, v14, v15\n"
"v_mfma_f32_16x16x32_bf16 a[104:107], v[120:123], v[88:91], a[104:107]\n"
"v_exp_f32 v16, v16\n"
"v_mfma_f32_16x16x32_bf16 a[120:123], v[124:127], v[88:91], a[120:123]\n"
"v_exp_f32 v17, v17\n"
"v_mfma_f32_16x16x32_bf16 a[136:139], v[212:215], v[88:91], a[136:139]\n"
"v_cvt_pk_bf16_f32 v72, v16, v17\n"
"v_mfma_f32_16x16x32_bf16 a[140:143], v[212:215], v[92:95], a[140:143]\n"
"v_exp_f32 v18, v18\n"
"v_mfma_f32_16x16x32_bf16 a[124:127], v[124:127], v[92:95], a[124:127]\n"
"v_mfma_f32_16x16x32_bf16 a[108:111], v[120:123], v[92:95], a[108:111]\n"
"v_exp_f32 v19, v19\n"
"v_mfma_f32_16x16x32_bf16 a[92:95], v[116:119], v[92:95], a[92:95]\n"
"s_addk_i32 m0, 6208\n"
"v_cvt_pk_bf16_f32 v73, v18, v19\n"
"v_mfma_f32_16x16x32_bf16 a[76:79], v[112:115], v[92:95], a[76:79]\n"
"v_exp_f32 v20, v20\n"
"v_mfma_f32_16x16x32_bf16 a[60:63], v[108:111], v[92:95], a[60:63]\n"
"v_mfma_f32_16x16x32_bf16 a[44:47], v[104:107], v[92:95], a[44:47]\n"
"v_exp_f32 v21, v21\n"
"v_mfma_f32_16x16x32_bf16 a[28:31], v[100:103], v[92:95], a[28:31]\n"
"v_cvt_pk_bf16_f32 v74, v20, v21\n"
"v_mfma_f32_16x16x32_bf16 a[12:15], v[96:99], v[92:95], a[12:15]\n"
"s_waitcnt lgkmcnt(6)\n"
"v_exp_f32 v22, v22\n"
"v_mfma_f32_16x16x32_bf16 v[32:35], a[144:147], v[128:131], v[192:195]\n"
"ds_read_b64_tr_b16 v[96:97], v232 offset:2048\n"
"v_mfma_f32_16x16x32_bf16 v[32:35], a[148:151], v[132:135], v[32:35]\n"
"s_waitcnt lgkmcnt(5)\n"
"ds_read_b64_tr_b16 v[98:99], v232 offset:3072\n"
"v_mfma_f32_16x16x32_bf16 v[32:35], a[152:155], v[136:139], v[32:35]\n"
"ds_read_b64_tr_b16 v[100:101], v252 offset:2048\n"
"v_mfma_f32_16x16x32_bf16 v[32:35], a[156:159], v[140:143], v[32:35]\n"
"s_waitcnt lgkmcnt(5)\n"
"ds_read_b64_tr_b16 v[102:103], v252 offset:3072\n"
"v_mfma_f32_16x16x32_bf16 v[36:39], a[160:163], v[128:131], v[192:195]\n"
"v_exp_f32 v23, v23\n"
"v_mfma_f32_16x16x32_bf16 v[36:39], a[164:167], v[132:135], v[36:39]\n"
"s_waitcnt lgkmcnt(4)\n"
"buffer_load_dwordx4 v225, s[52:55], s93 offen lds\n"
"v_mfma_f32_16x16x32_bf16 v[36:39], a[168:171], v[136:139], v[36:39]\n"
"ds_read_b64_tr_b16 v[104:105], v232 offset:6144\n"
"v_cvt_pk_bf16_f32 v75, v22, v23\n"
"v_mfma_f32_16x16x32_bf16 v[36:39], a[172:175], v[140:143], v[36:39]\n"
"ds_read_b64_tr_b16 v[106:107], v232 offset:7168\n"
"v_mfma_f32_16x16x32_bf16 v[40:43], a[144:147], v[144:147], v[196:199]\n"
"v_exp_f32 v24, v24\n"
"v_mfma_f32_16x16x32_bf16 v[40:43], a[148:151], v[148:151], v[40:43]\n"
"s_addk_i32 m0, 1984\n"
"ds_read_b64_tr_b16 v[108:109], v252 offset:6144\n"
"v_mfma_f32_16x16x32_bf16 v[40:43], a[152:155], v[152:155], v[40:43]\n"
"v_exp_f32 v25, v25\n"
"v_mfma_f32_16x16x32_bf16 v[40:43], a[156:159], v[156:159], v[40:43]\n"
"ds_read_b64_tr_b16 v[110:111], v252 offset:7168\n"
"v_cvt_pk_bf16_f32 v76, v24, v25\n"
"v_mfma_f32_16x16x32_bf16 v[44:47], a[160:163], v[144:147], v[196:199]\n"
"v_exp_f32 v26, v26\n"
"v_mfma_f32_16x16x32_bf16 v[44:47], a[164:167], v[148:151], v[44:47]\n"
"ds_read_b64_tr_b16 v[112:113], v232 offset:10240\n"
"v_mfma_f32_16x16x32_bf16 v[44:47], a[168:171], v[152:155], v[44:47]\n"
"v_exp_f32 v27, v27\n"
"v_mfma_f32_16x16x32_bf16 v[44:47], a[172:175], v[156:159], v[44:47]\n"
"ds_read_b64_tr_b16 v[114:115], v232 offset:11264\n"
"v_cvt_pk_bf16_f32 v77, v26, v27\n"
"v_mfma_f32_16x16x32_bf16 v[48:51], a[144:147], v[160:163], v[200:203]\n"
"v_exp_f32 v28, v28\n"
"v_mfma_f32_16x16x32_bf16 v[48:51], a[148:151], v[164:167], v[48:51]\n"
"ds_read_b64_tr_b16 v[116:117], v252 offset:10240\n"
"v_mfma_f32_16x16x32_bf16 v[48:51], a[152:155], v[168:171], v[48:51]\n"
"v_exp_f32 v29, v29\n"
"v_mfma_f32_16x16x32_bf16 v[48:51], a[156:159], v[172:175], v[48:51]\n"
"ds_read_b64_tr_b16 v[118:119], v252 offset:11264\n"
"v_cvt_pk_bf16_f32 v78, v28, v29\n"
"v_mfma_f32_16x16x32_bf16 v[52:55], a[160:163], v[160:163], v[200:203]\n"
"v_exp_f32 v30, v30\n"
"v_mfma_f32_16x16x32_bf16 v[52:55], a[164:167], v[164:167], v[52:55]\n"
"buffer_load_dwordx4 v225, s[52:55], s93 offen offset:64 lds\n"
"v_mfma_f32_16x16x32_bf16 v[52:55], a[168:171], v[168:171], v[52:55]\n"
"v_exp_f32 v31, v31\n"
"v_exp_f32 v32, v32\n"
"v_exp_f32 v33, v33\n"
"v_mfma_f32_16x16x32_bf16 v[52:55], a[172:175], v[172:175], v[52:55]\n"
"ds_read_b64_tr_b16 v[120:121], v232 offset:14336\n"
"v_cvt_pk_bf16_f32 v79, v30, v31\n"
"v_mfma_f32_16x16x32_bf16 v[56:59], a[144:147], v[176:179], v[204:207]\n"
"ds_read_b64_tr_b16 v[122:123], v232 offset:15360\n"
"ds_read_b64_tr_b16 v[124:125], v252 offset:14336\n"
"ds_read_b64_tr_b16 v[126:127], v252 offset:15360\n"
"v_exp_f32 v34, v34\n"
"v_mfma_f32_16x16x32_bf16 v[56:59], a[148:151], v[180:183], v[56:59]\n"
"v_cvt_pk_bf16_f32 v80, v32, v33\n"
"v_mfma_f32_16x16x32_bf16 v[56:59], a[152:155], v[184:187], v[56:59]\n"
"v_exp_f32 v35, v35\n"
"v_mfma_f32_16x16x32_bf16 v[56:59], a[156:159], v[188:191], v[56:59]\n"
"v_exp_f32 v36, v36\n"
"v_mfma_f32_16x16x32_bf16 v[60:63], a[160:163], v[176:179], v[204:207]\n"
"v_cvt_pk_bf16_f32 v81, v34, v35\n"
"v_mfma_f32_16x16x32_bf16 v[60:63], a[164:167], v[180:183], v[60:63]\n"
"v_exp_f32 v37, v37\n"
"v_mfma_f32_16x16x32_bf16 v[60:63], a[168:171], v[184:187], v[60:63]\n"
"v_cvt_pk_bf16_f32 v82, v36, v37\n"
"v_mfma_f32_16x16x32_bf16 v[60:63], a[172:175], v[188:191], v[60:63]\n"
"s_waitcnt vmcnt(14) lgkmcnt(0)\n"
"s_barrier\n"
"v_mfma_f32_16x16x32_bf16 a[0:3], v[96:99], v[64:67], a[0:3]\n"
"v_exp_f32 v38, v38\n"
"v_mfma_f32_16x16x32_bf16 a[16:19], v[100:103], v[64:67], a[16:19]\n"
"s_add_u32 s92, s92, 0x12000\n"
"v_exp_f32 v39, v39\n"
"v_mfma_f32_16x16x32_bf16 a[32:35], v[104:107], v[64:67], a[32:35]\n"
"s_add_u32 m0, s88, 0x8800\n"
"ds_read_b64_tr_b16 a[176:177], v247\n"
"v_cvt_pk_bf16_f32 v83, v38, v39\n"
"v_mfma_f32_16x16x32_bf16 a[48:51], v[108:111], v[64:67], a[48:51]\n"
"ds_read_b64_tr_b16 a[178:179], v247 offset:1024\n"
"v_mfma_f32_16x16x32_bf16 a[64:67], v[112:115], v[64:67], a[64:67]\n"
"s_mov_b32 exec_hi, 0\n"
"buffer_load_dwordx4 v233, s[56:59], s92 offen sc1 lds\n"
"s_mov_b32 exec_hi, -1\n"
"v_mfma_f32_16x16x32_bf16 a[80:83], v[116:119], v[64:67], a[80:83]\n"
"v_exp_f32 v40, v40\n"
"v_mfma_f32_16x16x32_bf16 a[96:99], v[120:123], v[64:67], a[96:99]\n"
"ds_read_b64_tr_b16 a[180:181], v248\n"
"v_mfma_f32_16x16x32_bf16 a[112:115], v[124:127], v[64:67], a[112:115]\n"
"v_exp_f32 v41, v41\n"
"v_mfma_f32_16x16x32_bf16 a[128:131], v[212:215], v[64:67], a[128:131]\n"
"buffer_load_dwordx4 v233, s[56:59], s92 offen offset:512 sc1 lds\n"
"v_mfma_f32_16x16x32_bf16 a[132:135], v[212:215], v[68:71], a[132:135]\n"
"v_exp_f32 v42, v42\n"
"v_mfma_f32_16x16x32_bf16 a[116:119], v[124:127], v[68:71], a[116:119]\n"
"buffer_load_dwordx4 v233, s[56:59], s92 offen offset:1536 sc1 lds\n"
"v_mfma_f32_16x16x32_bf16 a[100:103], v[120:123], v[68:71], a[100:103]\n"
"v_exp_f32 v43, v43\n"
"v_mfma_f32_16x16x32_bf16 a[84:87], v[116:119], v[68:71], a[84:87]\n"
"buffer_load_dwordx4 v233, s[56:59], s92 offen offset:2560 sc1 lds\n"
"v_mfma_f32_16x16x32_bf16 a[68:71], v[112:115], v[68:71], a[68:71]\n"
"v_exp_f32 v44, v44\n"
"v_mfma_f32_16x16x32_bf16 a[52:55], v[108:111], v[68:71], a[52:55]\n"
"buffer_load_dwordx4 v233, s[56:59], s92 offen offset:3584 sc1 lds\n"
"v_mfma_f32_16x16x32_bf16 a[36:39], v[104:107], v[68:71], a[36:39]\n"
"s_add_u32 s93, s93, 0x20000\n"
"v_exp_f32 v45, v45\n"
"v_mfma_f32_16x16x32_bf16 a[20:23], v[100:103], v[68:71], a[20:23]\n"
"s_add_u32 m0, s85, 0xd000\n"
"ds_read_b64_tr_b16 a[182:183], v248 offset:1024\n"
"v_cvt_pk_bf16_f32 v84, v40, v41\n"
"v_mfma_f32_16x16x32_bf16 a[4:7], v[96:99], v[68:71], a[4:7]\n"
"v_exp_f32 v46, v46\n"
"v_mfma_f32_16x16x32_bf16 a[8:11], v[96:99], v[72:75], a[8:11]\n"
"buffer_load_dwordx4 v225, s[52:55], s93 offen lds\n"
"v_mfma_f32_16x16x32_bf16 a[24:27], v[100:103], v[72:75], a[24:27]\n"
"s_addk_i32 m0, 1984\n"
"v_exp_f32 v47, v47\n"
"v_mfma_f32_16x16x32_bf16 a[40:43], v[104:107], v[72:75], a[40:43]\n"
"ds_read_b64_tr_b16 a[184:185], v247 offset:4096\n"
"v_cvt_pk_bf16_f32 v85, v42, v43\n"
"v_mfma_f32_16x16x32_bf16 a[56:59], v[108:111], v[72:75], a[56:59]\n"
"ds_read_b64_tr_b16 a[186:187], v247 offset:5120\n"
"v_cvt_pk_bf16_f32 v86, v44, v45\n"
"v_mfma_f32_16x16x32_bf16 a[72:75], v[112:115], v[72:75], a[72:75]\n"
"v_exp_f32 v48, v48\n"
"v_mfma_f32_16x16x32_bf16 a[88:91], v[116:119], v[72:75], a[88:91]\n"
"buffer_load_dwordx4 v225, s[52:55], s93 offen offset:64 lds\n"
"v_mfma_f32_16x16x32_bf16 a[104:107], v[120:123], v[72:75], a[104:107]\n"
"s_add_u32 s93, s93, 0x20000\n"
"v_exp_f32 v49, v49\n"
"v_mfma_f32_16x16x32_bf16 a[120:123], v[124:127], v[72:75], a[120:123]\n"
"s_addk_i32 m0, 6208\n"
"ds_read_b64_tr_b16 a[188:189], v248 offset:4096\n"
"v_cvt_pk_bf16_f32 v87, v46, v47\n"
"v_mfma_f32_16x16x32_bf16 a[136:139], v[212:215], v[72:75], a[136:139]\n"
"v_exp_f32 v50, v50\n"
"v_mfma_f32_16x16x32_bf16 a[140:143], v[212:215], v[76:79], a[140:143]\n"
"buffer_load_dwordx4 v225, s[52:55], s93 offen lds\n"
"v_mfma_f32_16x16x32_bf16 a[124:127], v[124:127], v[76:79], a[124:127]\n"
"s_addk_i32 m0, 1984\n"
"v_exp_f32 v51, v51\n"
"v_mfma_f32_16x16x32_bf16 a[108:111], v[120:123], v[76:79], a[108:111]\n"
"ds_read_b64_tr_b16 a[190:191], v248 offset:5120\n"
"v_cvt_pk_bf16_f32 v88, v48, v49\n"
"v_mfma_f32_16x16x32_bf16 a[92:95], v[116:119], v[76:79], a[92:95]\n"
"v_exp_f32 v52, v52\n"
"v_mfma_f32_16x16x32_bf16 a[76:79], v[112:115], v[76:79], a[76:79]\n"
"buffer_load_dwordx4 v225, s[52:55], s93 offen offset:64 lds\n"
"v_mfma_f32_16x16x32_bf16 a[60:63], v[108:111], v[76:79], a[60:63]\n"
"ds_read_b64_tr_b16 a[192:193], v247 offset:8192\n"
"ds_read_b64_tr_b16 a[194:195], v247 offset:9216\n"
"ds_read_b64_tr_b16 a[196:197], v248 offset:8192\n"
"ds_read_b64_tr_b16 a[198:199], v248 offset:9216\n"
"ds_read_b64_tr_b16 a[200:201], v247 offset:12288\n"
"ds_read_b64_tr_b16 a[202:203], v247 offset:13312\n"
"ds_read_b64_tr_b16 a[204:205], v248 offset:12288\n"
"ds_read_b64_tr_b16 a[206:207], v248 offset:13312\n"
"v_exp_f32 v53, v53\n"
"v_mfma_f32_16x16x32_bf16 a[44:47], v[104:107], v[76:79], a[44:47]\n"
"v_cvt_pk_bf16_f32 v89, v50, v51\n"
"v_mfma_f32_16x16x32_bf16 a[28:31], v[100:103], v[76:79], a[28:31]\n"
"v_exp_f32 v54, v54\n"
"v_mfma_f32_16x16x32_bf16 a[12:15], v[96:99], v[76:79], a[12:15]\n"
"s_waitcnt lgkmcnt(0)\n"
"s_barrier\n"
"v_cvt_pk_bf16_f32 v90, v52, v53\n"
"v_mfma_f32_16x16x32_bf16 a[0:3], a[176:179], v[80:83], a[0:3]\n"
"s_add_u32 s92, s92, 0x12000\n"
"v_exp_f32 v55, v55\n"
"v_mfma_f32_16x16x32_bf16 a[16:19], a[180:183], v[80:83], a[16:19]\n"
"s_add_u32 m0, s88, 0x11000\n"
"v_cvt_pk_bf16_f32 v91, v54, v55\n"
"v_exp_f32 v56, v56\n"
"v_mfma_f32_16x16x32_bf16 a[32:35], a[184:187], v[80:83], a[32:35]\n"
"s_mov_b32 exec_hi, 0\n"
"buffer_load_dwordx4 v233, s[56:59], s92 offen sc1 lds\n"
"s_mov_b32 exec_hi, -1\n"
"v_mfma_f32_16x16x32_bf16 a[48:51], a[188:191], v[80:83], a[48:51]\n"
"v_exp_f32 v57, v57\n"
"v_mfma_f32_16x16x32_bf16 a[64:67], a[192:195], v[80:83], a[64:67]\n"
"buffer_load_dwordx4 v233, s[56:59], s92 offen offset:512 sc1 lds\n"
"v_cvt_pk_bf16_f32 v92, v56, v57\n"
"v_mfma_f32_16x16x32_bf16 a[80:83], a[196:199], v[80:83], a[80:83]\n"
"buffer_load_dwordx4 v233, s[56:59], s92 offen offset:1536 sc1 lds\n"
"v_mfma_f32_16x16x32_bf16 a[96:99], a[200:203], v[80:83], a[96:99]\n"
"v_exp_f32 v58, v58\n"
"v_mfma_f32_16x16x32_bf16 a[112:115], a[204:207], v[80:83], a[112:115]\n"
"buffer_load_dwordx4 v233, s[56:59], s92 offen offset:2560 sc1 lds\n"
"v_mfma_f32_16x16x32_bf16 a[128:131], v[212:215], v[80:83], a[128:131]\n"
"buffer_load_dwordx4 v233, s[56:59], s92 offen offset:3584 sc1 lds\n"
"v_mfma_f32_16x16x32_bf16 a[132:135], v[212:215], v[84:87], a[132:135]\n"
"s_add_u32 s93, s93, 0x20000\n"
"v_exp_f32 v59, v59\n"
"v_mfma_f32_16x16x32_bf16 a[116:119], a[204:207], v[84:87], a[116:119]\n"
"s_add_u32 m0, s85, 0x15800\n"
"s_nop 0\n"
"buffer_load_dwordx4 v225, s[52:55], s93 offen lds\n"
"v_mfma_f32_16x16x32_bf16 a[100:103], a[200:203], v[84:87], a[100:103]\n"
"s_addk_i32 m0, 1984\n"
"v_exp_f32 v60, v60\n"
"v_cvt_pk_bf16_f32 v93, v58, v59\n"
"v_mfma_f32_16x16x32_bf16 a[84:87], a[196:199], v[84:87], a[84:87]\n"
"buffer_load_dwordx4 v225, s[52:55], s93 offen offset:64 lds\n"
"v_mfma_f32_16x16x32_bf16 a[68:71], a[192:195], v[84:87], a[68:71]\n"
"s_add_u32 s93, s93, 0x20000\n"
"v_exp_f32 v61, v61\n"
"v_mfma_f32_16x16x32_bf16 a[52:55], a[188:191], v[84:87], a[52:55]\n"
"s_addk_i32 m0, 6208\n"
"v_exp_f32 v62, v62\n"
"v_cvt_pk_bf16_f32 v94, v60, v61\n"
"v_mfma_f32_16x16x32_bf16 a[36:39], a[184:187], v[84:87], a[36:39]\n"
"buffer_load_dwordx4 v225, s[52:55], s93 offen lds\n"
"v_mfma_f32_16x16x32_bf16 a[20:23], a[180:183], v[84:87], a[20:23]\n"
"s_addk_i32 m0, 1984\n"
"v_exp_f32 v63, v63\n"
"v_mfma_f32_16x16x32_bf16 a[4:7], a[176:179], v[84:87], a[4:7]\n"
"buffer_load_dwordx4 v225, s[52:55], s93 offen offset:64 lds\n"
"v_cvt_pk_bf16_f32 v95, v62, v63\n"
"v_mfma_f32_16x16x32_bf16 a[8:11], a[176:179], v[88:91], a[8:11]\n"
"s_add_u32 s92, s92, 0x12000\n"
"v_accvgpr_read_b32 v201, a128\n"
"v_rcp_f32 v200, v201\n"
"v_mfma_f32_16x16x32_bf16 a[24:27], a[180:183], v[88:91], a[24:27]\n"
"s_add_u32 m0, s88, 0x19800\n"
"s_mov_b32 exec_hi, 0\n"
"buffer_load_dwordx4 v233, s[56:59], s92 offen sc1 lds\n"
"s_mov_b32 exec_hi, -1\n"
"v_mfma_f32_16x16x32_bf16 a[40:43], a[184:187], v[88:91], a[40:43]\n"
"buffer_load_dwordx4 v233, s[56:59], s92 offen offset:512 sc1 lds\n"
"v_mul_f32 v201, 0x43800000, v201\n"
"v_mfma_f32_16x16x32_bf16 a[56:59], a[188:191], v[88:91], a[56:59]\n"
"buffer_load_dwordx4 v233, s[56:59], s92 offen offset:1536 sc1 lds\n"
"v_mov_b32 v221, v201\n"
"v_mfma_f32_16x16x32_bf16 a[72:75], a[192:195], v[88:91], a[72:75]\n"
"s_mov_b32 s83, s1\n"
"buffer_load_dwordx4 v233, s[56:59], s92 offen offset:2560 sc1 lds\n"
"v_accvgpr_read_b32 v0, a0\n"
"v_mfma_f32_16x16x32_bf16 a[88:91], a[196:199], v[88:91], a[88:91]\n"
"buffer_load_dwordx4 v233, s[56:59], s92 offen offset:3584 sc1 lds\n"
"v_accvgpr_read_b32 v1, a1\n"
"v_mfma_f32_16x16x32_bf16 a[104:107], a[200:203], v[88:91], a[104:107]\n"
"s_add_u32 s93, s93, 0x20000\n"
"v_accvgpr_read_b32 v2, a2\n"
"v_accvgpr_read_b32 v3, a3\n"
"v_accvgpr_read_b32 v4, a16\n"
"v_mfma_f32_16x16x32_bf16 a[120:123], a[204:207], v[88:91], a[120:123]\n"
"s_add_u32 m0, s85, 0x1e000\n"
"s_nop 0\n"
"buffer_load_dwordx4 v225, s[52:55], s93 offen lds\n"
"v_mfma_f32_16x16x32_bf16 a[136:139], v[212:215], v[88:91], a[136:139]\n"
"s_addk_i32 m0, 1984\n"
"s_nop 0\n"
"buffer_load_dwordx4 v225, s[52:55], s93 offen offset:64 lds\n"
"v_mfma_f32_16x16x32_bf16 a[140:143], v[212:215], v[92:95], a[140:143]\n"
"s_add_u32 s93, s93, 0x20000\n"
"v_accvgpr_read_b32 v5, a17\n"
"v_accvgpr_read_b32 v6, a18\n"
"v_accvgpr_read_b32 v7, a19\n"
"v_mfma_f32_16x16x32_bf16 a[124:127], a[204:207], v[92:95], a[124:127]\n"
"s_addk_i32 m0, 6208\n"
"s_nop 0\n"
"buffer_load_dwordx4 v225, s[52:55], s93 offen lds\n"
"v_mfma_f32_16x16x32_bf16 a[108:111], a[200:203], v[92:95], a[108:111]\n"
"s_addk_i32 m0, 1984\n"
"v_maximum3_f32 v221, v221, |v0|, |v1|\n"
"v_maximum3_f32 v221, v221, |v2|, |v3|\n"
"v_maximum3_f32 v221, v221, |v4|, |v5|\n"
"v_mfma_f32_16x16x32_bf16 a[92:95], a[196:199], v[92:95], a[92:95]\n"
"buffer_load_dwordx4 v225, s[52:55], s93 offen offset:64 lds\n"
"v_maximum3_f32 v221, v221, |v6|, |v7|\n"
"v_mfma_f32_16x16x32_bf16 a[76:79], a[192:195], v[92:95], a[76:79]\n"
"v_mul_f32 v0, v0, v200\n"
"v_mul_f32 v1, v1, v200\n"
"v_mul_f32 v2, v2, v200\n"
"v_mfma_f32_16x16x32_bf16 a[60:63], a[188:191], v[92:95], a[60:63]\n"
"v_mul_f32 v3, v3, v200\n"
"v_mul_f32 v4, v4, v200\n"
"v_mul_f32 v5, v5, v200\n"
"v_mfma_f32_16x16x32_bf16 a[44:47], a[184:187], v[92:95], a[44:47]\n"
"v_mul_f32 v6, v6, v200\n"
"v_mul_f32 v7, v7, v200\n"
"v_mfma_f32_16x16x32_bf16 a[28:31], a[180:183], v[92:95], a[28:31]\n"
"v_cvt_pk_bf16_f32 v16, v0, v1\n"
"v_cvt_pk_bf16_f32 v17, v2, v3\n"
"v_mfma_f32_16x16x32_bf16 a[12:15], a[176:179], v[92:95], a[12:15]\n"
"v_cvt_pk_bf16_f32 v18, v4, v5\n"
"v_cvt_pk_bf16_f32 v19, v6, v7\n"
"buffer_store_dwordx4 v[16:19], v235, s[56:59], s83 offen sc1\n"
"L_ct2:\n"
"v_accvgpr_read_b32 v8, a32\n"
"v_accvgpr_read_b32 v9, a33\n"
"v_accvgpr_read_b32 v10, a34\n"
"v_accvgpr_read_b32 v11, a35\n"
"v_accvgpr_read_b32 v12, a48\n"
"v_accvgpr_read_b32 v13, a49\n"
"v_accvgpr_read_b32 v14, a50\n"
"v_accvgpr_read_b32 v15, a51\n"
"v_maximum3_f32 v221, v221, |v8|, |v9|\n"
"v_maximum3_f32 v221, v221, |v10|, |v11|\n"
"v_maximum3_f32 v221, v221, |v12|, |v13|\n"
"v_maximum3_f32 v221, v221, |v14|, |v15|\n"
"v_pk_mul_f32 v[8:9], v[8:9], v[200:201] op_sel_hi:[1,0]\n"
"v_pk_mul_f32 v[10:11], v[10:11], v[200:201] op_sel_hi:[1,0]\n"
"v_pk_mul_f32 v[12:13], v[12:13], v[200:201] op_sel_hi:[1,0]\n"
"v_pk_mul_f32 v[14:15], v[14:15], v[200:201] op_sel_hi:[1,0]\n"
"v_cvt_pk_bf16_f32 v20, v8, v9\n"
"v_cvt_pk_bf16_f32 v21, v10, v11\n"
"v_cvt_pk_bf16_f32 v22, v12, v13\n"
"v_cvt_pk_bf16_f32 v23, v14, v15\n"
"buffer_store_dwordx4 v[20:23], v235, s[56:59], s83 offen offset:1024 sc1\n"
"v_accvgpr_read_b32 v0, a64\n"
"v_accvgpr_read_b32 v1, a65\n"
"v_accvgpr_read_b32 v2, a66\n"
"v_accvgpr_read_b32 v3, a67\n"
"v_accvgpr_read_b32 v4, a80\n"
"v_accvgpr_read_b32 v5, a81\n"
"v_accvgpr_read_b32 v6, a82\n"
"v_accvgpr_read_b32 v7, a83\n"
"v_maximum3_f32 v221, v221, |v0|, |v1|\n"
"v_maximum3_f32 v221, v221, |v2|, |v3|\n"
"v_maximum3_f32 v221, v221, |v4|, |v5|\n"
"v_maximum3_f32 v221, v221, |v6|, |v7|\n"
"v_pk_mul_f32 v[0:1], v[0:1], v[200:201] op_sel_hi:[1,0]\n"
"v_pk_mul_f32 v[2:3], v[2:3], v[200:201] op_sel_hi:[1,0]\n"
"v_pk_mul_f32 v[4:5], v[4:5], v[200:201] op_sel_hi:[1,0]\n"
"v_pk_mul_f32 v[6:7], v[6:7], v[200:201] op_sel_hi:[1,0]\n"
"v_cvt_pk_bf16_f32 v24, v0, v1\n"
"v_cvt_pk_bf16_f32 v25, v2, v3\n"
"v_cvt_pk_bf16_f32 v26, v4, v5\n"
"v_cvt_pk_bf16_f32 v27, v6, v7\n"
"buffer_store_dwordx4 v[24:27], v235, s[56:59], s83 offen offset:2048 sc1\n"
"v_accvgpr_read_b32 v8, a96\n"
"v_accvgpr_read_b32 v9, a97\n"
"v_accvgpr_read_b32 v10, a98\n"
"v_accvgpr_read_b32 v11, a99\n"
"v_accvgpr_read_b32 v12, a112\n"
"v_accvgpr_read_b32 v13, a113\n"
"v_accvgpr_read_b32 v14, a114\n"
"v_accvgpr_read_b32 v15, a115\n"
"v_maximum3_f32 v221, v221, |v8|, |v9|\n"
"v_maximum3_f32 v221, v221, |v10|, |v11|\n"
"v_maximum3_f32 v221, v221, |v12|, |v13|\n"
"v_maximum3_f32 v221, v221, |v14|, |v15|\n"
"v_pk_mul_f32 v[8:9], v[8:9], v[200:201] op_sel_hi:[1,0]\n"
"v_pk_mul_f32 v[10:11], v[10:11], v[200:201] op_sel_hi:[1,0]\n"
"v_pk_mul_f32 v[12:13], v[12:13], v[200:201] op_sel_hi:[1,0]\n"
"v_pk_mul_f32 v[14:15], v[14:15], v[200:201] op_sel_hi:[1,0]\n"
"v_cvt_pk_bf16_f32 v16, v8, v9\n"
"v_cvt_pk_bf16_f32 v17, v10, v11\n"
"v_cvt_pk_bf16_f32 v18, v12, v13\n"
"v_cvt_pk_bf16_f32 v19, v14, v15\n"
"buffer_store_dwordx4 v[16:19], v235, s[56:59], s83 offen offset:3072 sc1\n"
"v_accvgpr_read_b32 v203, a132\n"
"v_rcp_f32 v202, v203\n"
"v_mul_f32 v203, 0x43800000, v203\n"
"v_maximum3_f32 v221, v221, v203, v203\n"
"s_add_u32 s83, s83, 0x20000\n"
"v_accvgpr_read_b32 v0, a4\n"
"v_accvgpr_read_b32 v1, a5\n"
"v_accvgpr_read_b32 v2, a6\n"
"v_accvgpr_read_b32 v3, a7\n"
"v_accvgpr_read_b32 v4, a20\n"
"v_accvgpr_read_b32 v5, a21\n"
"v_accvgpr_read_b32 v6, a22\n"
"v_accvgpr_read_b32 v7, a23\n"
"v_maximum3_f32 v221, v221, |v0|, |v1|\n"
"v_maximum3_f32 v221, v221, |v2|, |v3|\n"
"v_maximum3_f32 v221, v221, |v4|, |v5|\n"
"v_maximum3_f32 v221, v221, |v6|, |v7|\n"
"v_pk_mul_f32 v[0:1], v[0:1], v[202:203] op_sel_hi:[1,0]\n"
"v_pk_mul_f32 v[2:3], v[2:3], v[202:203] op_sel_hi:[1,0]\n"
"v_pk_mul_f32 v[4:5], v[4:5], v[202:203] op_sel_hi:[1,0]\n"
"v_pk_mul_f32 v[6:7], v[6:7], v[202:203] op_sel_hi:[1,0]\n"
"v_cvt_pk_bf16_f32 v20, v0, v1\n"
"v_cvt_pk_bf16_f32 v21, v2, v3\n"
"v_cvt_pk_bf16_f32 v22, v4, v5\n"
"v_cvt_pk_bf16_f32 v23, v6, v7\n"
"buffer_store_dwordx4 v[20:23], v235, s[56:59], s83 offen sc1\n"
"s_waitcnt vmcnt(32)\n"
"s_barrier\n"
"ds_read_b64_tr_b16 a[216:217], v238 offset:18432\n"
"v_accvgpr_read_b32 v8, a36\n"
"v_accvgpr_read_b32 v9, a37\n"
"ds_read_b64_tr_b16 a[218:219], v238 offset:19456\n"
"v_accvgpr_read_b32 v10, a38\n"
"v_accvgpr_read_b32 v11, a39\n"
"ds_read_b64_tr_b16 a[224:225], v238 offset:26624\n"
"v_accvgpr_read_b32 v12, a52\n"
"v_accvgpr_read_b32 v13, a53\n"
"ds_read_b64_tr_b16 a[226:227], v238 offset:27648\n"
"v_accvgpr_read_b32 v14, a54\n"
"v_accvgpr_read_b32 v15, a55\n"
"ds_read_b64_tr_b16 a[220:221], v239 offset:18432\n"
"v_maximum3_f32 v221, v221, |v8|, |v9|\n"
"v_maximum3_f32 v221, v221, |v10|, |v11|\n"
"ds_read_b64_tr_b16 a[222:223], v239 offset:19456\n"
"v_maximum3_f32 v221, v221, |v12|, |v13|\n"
"v_maximum3_f32 v221, v221, |v14|, |v15|\n"
"ds_read_b64_tr_b16 a[228:229], v239 offset:26624\n"
"v_pk_mul_f32 v[8:9], v[8:9], v[202:203] op_sel_hi:[1,0]\n"
"v_pk_mul_f32 v[10:11], v[10:11], v[202:203] op_sel_hi:[1,0]\n"
"ds_read_b64_tr_b16 a[230:231], v239 offset:27648\n"
"v_pk_mul_f32 v[12:13], v[12:13], v[202:203] op_sel_hi:[1,0]\n"
"v_pk_mul_f32 v[14:15], v[14:15], v[202:203] op_sel_hi:[1,0]\n"
"ds_read_b128 a[144:147], v235\n"
"v_cvt_pk_bf16_f32 v24, v8, v9\n"
"v_cvt_pk_bf16_f32 v25, v10, v11\n"
"ds_read_b128 a[180:183], v235 offset:1024\n"
"v_cvt_pk_bf16_f32 v26, v12, v13\n"
"v_cvt_pk_bf16_f32 v27, v14, v15\n"
"ds_read_b128 a[148:151], v235 offset:2048\n"
"buffer_store_dwordx4 v[24:27], v235, s[56:59], s83 offen offset:1024 sc1\n"
"v_accvgpr_read_b32 v0, a68\n"
"ds_read_b128 a[184:187], v235 offset:3072\n"
"v_accvgpr_read_b32 v1, a69\n"
"v_accvgpr_read_b32 v2, a70\n"
"ds_read_b128 a[152:155], v235 offset:4096\n"
"v_accvgpr_read_b32 v3, a71\n"
"v_accvgpr_read_b32 v4, a84\n"
"ds_read_b128 a[188:191], v235 offset:5120\n"
"v_accvgpr_read_b32 v5, a85\n"
"v_accvgpr_read_b32 v6, a86\n"
"ds_read_b128 a[156:159], v235 offset:6144\n"
"v_accvgpr_read_b32 v7, a87\n"
"v_maximum3_f32 v221, v221, |v0|, |v1|\n"
"ds_read_b128 a[192:195], v235 offset:7168\n"
"v_maximum3_f32 v221, v221, |v2|, |v3|\n"
"v_maximum3_f32 v221, v221, |v4|, |v5|\n"
"ds_read_b128 a[160:163], v235 offset:8192\n"
"v_maximum3_f32 v221, v221, |v6|, |v7|\n"
"v_pk_mul_f32 v[0:1], v[0:1], v[202:203] op_sel_hi:[1,0]\n"
"ds_read_b128 a[196:199], v235 offset:9216\n"
"v_pk_mul_f32 v[2:3], v[2:3], v[202:203] op_sel_hi:[1,0]\n"
"v_pk_mul_f32 v[4:5], v[4:5], v[202:203] op_sel_hi:[1,0]\n"
"ds_read_b128 a[164:167], v235 offset:10240\n"
"v_pk_mul_f32 v[6:7], v[6:7], v[202:203] op_sel_hi:[1,0]\n"
"v_cvt_pk_bf16_f32 v16, v0, v1\n"
"ds_read_b128 a[200:203], v235 offset:11264\n"
"v_cvt_pk_bf16_f32 v17, v2, v3\n"
"v_cvt_pk_bf16_f32 v18, v4, v5\n"
"ds_read_b128 a[168:171], v235 offset:12288\n"
"v_cvt_pk_bf16_f32 v19, v6, v7\n"
"buffer_store_dwordx4 v[16:19], v235, s[56:59], s83 offen offset:2048 sc1\n"
"ds_read_b128 a[204:207], v235 offset:13312\n"
"v_accvgpr_read_b32 v8, a100\n"
"v_accvgpr_read_b32 v9, a101\n"
"ds_read_b128 a[172:175], v235 offset:14336\n"
"v_accvgpr_read_b32 v10, a102\n"
"v_accvgpr_read_b32 v11, a103\n"
"ds_read_b128 a[208:211], v235 offset:15360\n"
"v_accvgpr_read_b32 v12, a116\n"
"v_accvgpr_read_b32 v13, a117\n"
"ds_read_b128 a[176:179], v235 offset:16384\n"
"v_accvgpr_read_b32 v14, a118\n"
"v_accvgpr_read_b32 v15, a119\n"
"ds_read_b128 a[212:215], v235 offset:17408\n"
"v_maximum3_f32 v221, v221, |v8|, |v9|\n"
"v_maximum3_f32 v221, v221, |v10|, |v11|\n"
"v_maximum3_f32 v221, v221, |v12|, |v13|\n"
"v_maximum3_f32 v221, v221, |v14|, |v15|\n"
"v_pk_mul_f32 v[8:9], v[8:9], v[202:203] op_sel_hi:[1,0]\n"
"v_pk_mul_f32 v[10:11], v[10:11], v[202:203] op_sel_hi:[1,0]\n"
"v_pk_mul_f32 v[12:13], v[12:13], v[202:203] op_sel_hi:[1,0]\n"
"v_pk_mul_f32 v[14:15], v[14:15], v[202:203] op_sel_hi:[1,0]\n"
"v_cvt_pk_bf16_f32 v20, v8, v9\n"
"v_cvt_pk_bf16_f32 v21, v10, v11\n"
"v_cvt_pk_bf16_f32 v22, v12, v13\n"
"v_cvt_pk_bf16_f32 v23, v14, v15\n"
"buffer_store_dwordx4 v[20:23], v235, s[56:59], s83 offen offset:3072 sc1\n"
"v_accvgpr_read_b32 v205, a136\n"
"v_rcp_f32 v204, v205\n"
"v_mul_f32 v205, 0x43800000, v205\n"
"v_maximum3_f32 v221, v221, v205, v205\n"
"s_add_u32 s83, s83, 0x20000\n"
"v_accvgpr_read_b32 v0, a8\n"
"v_accvgpr_read_b32 v1, a9\n"
"v_accvgpr_read_b32 v2, a10\n"
"v_accvgpr_read_b32 v3, a11\n"
"v_accvgpr_read_b32 v4, a24\n"
"v_accvgpr_read_b32 v5, a25\n"
"v_accvgpr_read_b32 v6, a26\n"
"v_accvgpr_read_b32 v7, a27\n"
"v_maximum3_f32 v221, v221, |v0|, |v1|\n"
"v_maximum3_f32 v221, v221, |v2|, |v3|\n"
"v_maximum3_f32 v221, v221, |v4|, |v5|\n"
"v_maximum3_f32 v221, v221, |v6|, |v7|\n"
"v_pk_mul_f32 v[0:1], v[0:1], v[204:205] op_sel_hi:[1,0]\n"
"v_pk_mul_f32 v[2:3], v[2:3], v[204:205] op_sel_hi:[1,0]\n"
"v_pk_mul_f32 v[4:5], v[4:5], v[204:205] op_sel_hi:[1,0]\n"
"v_pk_mul_f32 v[6:7], v[6:7], v[204:205] op_sel_hi:[1,0]\n"
"v_cvt_pk_bf16_f32 v24, v0, v1\n"
"v_cvt_pk_bf16_f32 v25, v2, v3\n"
"v_cvt_pk_bf16_f32 v26, v4, v5\n"
"v_cvt_pk_bf16_f32 v27, v6, v7\n"
"buffer_store_dwordx4 v[24:27], v235, s[56:59], s83 offen sc1\n"
"v_accvgpr_read_b32 v8, a40\n"
"v_accvgpr_read_b32 v9, a41\n"
"v_accvgpr_read_b32 v10, a42\n"
"v_accvgpr_read_b32 v11, a43\n"
"v_accvgpr_read_b32 v12, a56\n"
"v_accvgpr_read_b32 v13, a57\n"
"v_accvgpr_read_b32 v14, a58\n"
"v_accvgpr_read_b32 v15, a59\n"
"v_maximum3_f32 v221, v221, |v8|, |v9|\n"
"v_maximum3_f32 v221, v221, |v10|, |v11|\n"
"v_maximum3_f32 v221, v221, |v12|, |v13|\n"
"v_maximum3_f32 v221, v221, |v14|, |v15|\n"
"v_pk_mul_f32 v[8:9], v[8:9], v[204:205] op_sel_hi:[1,0]\n"
"v_pk_mul_f32 v[10:11], v[10:11], v[204:205] op_sel_hi:[1,0]\n"
"v_pk_mul_f32 v[12:13], v[12:13], v[204:205] op_sel_hi:[1,0]\n"
"v_pk_mul_f32 v[14:15], v[14:15], v[204:205] op_sel_hi:[1,0]\n"
"v_cvt_pk_bf16_f32 v16, v8, v9\n"
"v_cvt_pk_bf16_f32 v17, v10, v11\n"
"v_cvt_pk_bf16_f32 v18, v12, v13\n"
"v_cvt_pk_bf16_f32 v19, v14, v15\n"
"buffer_store_dwordx4 v[16:19], v235, s[56:59], s83 offen offset:1024 sc1\n"
"v_accvgpr_read_b32 v0, a72\n"
"v_accvgpr_read_b32 v1, a73\n"
"v_accvgpr_read_b32 v2, a74\n"
"v_accvgpr_read_b32 v3, a75\n"
"v_accvgpr_read_b32 v4, a88\n"
"v_accvgpr_read_b32 v5, a89\n"
"v_accvgpr_read_b32 v6, a90\n"
"v_accvgpr_read_b32 v7, a91\n"
"v_maximum3_f32 v221, v221, |v0|, |v1|\n"
"v_maximum3_f32 v221, v221, |v2|, |v3|\n"
"v_maximum3_f32 v221, v221, |v4|, |v5|\n"
"v_maximum3_f32 v221, v221, |v6|, |v7|\n"
"v_pk_mul_f32 v[0:1], v[0:1], v[204:205] op_sel_hi:[1,0]\n"
"v_pk_mul_f32 v[2:3], v[2:3], v[204:205] op_sel_hi:[1,0]\n"
"v_pk_mul_f32 v[4:5], v[4:5], v[204:205] op_sel_hi:[1,0]\n"
"v_pk_mul_f32 v[6:7], v[6:7], v[204:205] op_sel_hi:[1,0]\n"
"v_cvt_pk_bf16_f32 v20, v0, v1\n"
"v_cvt_pk_bf16_f32 v21, v2, v3\n"
"v_cvt_pk_bf16_f32 v22, v4, v5\n"
"v_cvt_pk_bf16_f32 v23, v6, v7\n"
"buffer_store_dwordx4 v[20:23], v235, s[56:59], s83 offen offset:2048 sc1\n"
"v_accvgpr_read_b32 v8, a104\n"
"v_accvgpr_read_b32 v9, a105\n"
"v_accvgpr_read_b32 v10, a106\n"
"v_accvgpr_read_b32 v11, a107\n"
"v_accvgpr_read_b32 v12, a120\n"
"v_accvgpr_read_b32 v13, a121\n"
"v_accvgpr_read_b32 v14, a122\n"
"v_accvgpr_read_b32 v15, a123\n"
"v_maximum3_f32 v221, v221, |v8|, |v9|\n"
"v_maximum3_f32 v221, v221, |v10|, |v11|\n"
"v_maximum3_f32 v221, v221, |v12|, |v13|\n"
"v_maximum3_f32 v221, v221, |v14|, |v15|\n"
"v_pk_mul_f32 v[8:9], v[8:9], v[204:205] op_sel_hi:[1,0]\n"
"v_pk_mul_f32 v[10:11], v[10:11], v[204:205] op_sel_hi:[1,0]\n"
"v_pk_mul_f32 v[12:13], v[12:13], v[204:205] op_sel_hi:[1,0]\n"
"v_pk_mul_f32 v[14:15], v[14:15], v[204:205] op_sel_hi:[1,0]\n"
"v_cvt_pk_bf16_f32 v24, v8, v9\n"
"v_cvt_pk_bf16_f32 v25, v10, v11\n"
"v_cvt_pk_bf16_f32 v26, v12, v13\n"
"v_cvt_pk_bf16_f32 v27, v14, v15\n"
"buffer_store_dwordx4 v[24:27], v235, s[56:59], s83 offen offset:3072 sc1\n"
"v_accvgpr_read_b32 v207, a140\n"
"v_rcp_f32 v206, v207\n"
"v_mul_f32 v207, 0x43800000, v207\n"
"v_maximum3_f32 v221, v221, v207, v207\n"
"s_add_u32 s83, s83, 0x20000\n"
"v_accvgpr_read_b32 v0, a12\n"
"v_accvgpr_read_b32 v1, a13\n"
"v_accvgpr_read_b32 v2, a14\n"
"v_accvgpr_read_b32 v3, a15\n"
"v_accvgpr_read_b32 v4, a28\n"
"v_accvgpr_read_b32 v5, a29\n"
"v_accvgpr_read_b32 v6, a30\n"
"v_accvgpr_read_b32 v7, a31\n"
"v_maximum3_f32 v221, v221, |v0|, |v1|\n"
"v_maximum3_f32 v221, v221, |v2|, |v3|\n"
"v_maximum3_f32 v221, v221, |v4|, |v5|\n"
"v_maximum3_f32 v221, v221, |v6|, |v7|\n"
"v_pk_mul_f32 v[0:1], v[0:1], v[206:207] op_sel_hi:[1,0]\n"
"v_pk_mul_f32 v[2:3], v[2:3], v[206:207] op_sel_hi:[1,0]\n"
"v_pk_mul_f32 v[4:5], v[4:5], v[206:207] op_sel_hi:[1,0]\n"
"v_pk_mul_f32 v[6:7], v[6:7], v[206:207] op_sel_hi:[1,0]\n"
"v_cvt_pk_bf16_f32 v16, v0, v1\n"
"v_cvt_pk_bf16_f32 v17, v2, v3\n"
"v_cvt_pk_bf16_f32 v18, v4, v5\n"
"v_cvt_pk_bf16_f32 v19, v6, v7\n"
"buffer_store_dwordx4 v[16:19], v235, s[56:59], s83 offen sc1\n"
"v_accvgpr_read_b32 v8, a44\n"
"v_accvgpr_read_b32 v9, a45\n"
"v_accvgpr_read_b32 v10, a46\n"
"v_accvgpr_read_b32 v11, a47\n"
"v_accvgpr_read_b32 v12, a60\n"
"v_accvgpr_read_b32 v13, a61\n"
"v_accvgpr_read_b32 v14, a62\n"
"v_accvgpr_read_b32 v15, a63\n"
"v_maximum3_f32 v221, v221, |v8|, |v9|\n"
"v_maximum3_f32 v221, v221, |v10|, |v11|\n"
"v_maximum3_f32 v221, v221, |v12|, |v13|\n"
"v_maximum3_f32 v221, v221, |v14|, |v15|\n"
"v_pk_mul_f32 v[8:9], v[8:9], v[206:207] op_sel_hi:[1,0]\n"
"v_pk_mul_f32 v[10:11], v[10:11], v[206:207] op_sel_hi:[1,0]\n"
"v_pk_mul_f32 v[12:13], v[12:13], v[206:207] op_sel_hi:[1,0]\n"
"v_pk_mul_f32 v[14:15], v[14:15], v[206:207] op_sel_hi:[1,0]\n"
"v_cvt_pk_bf16_f32 v20, v8, v9\n"
"v_cvt_pk_bf16_f32 v21, v10, v11\n"
"v_cvt_pk_bf16_f32 v22, v12, v13\n"
"v_cvt_pk_bf16_f32 v23, v14, v15\n"
"buffer_store_dwordx4 v[20:23], v235, s[56:59], s83 offen offset:1024 sc1\n"
"v_accvgpr_read_b32 v0, a76\n"
"v_accvgpr_read_b32 v1, a77\n"
"v_accvgpr_read_b32 v2, a78\n"
"v_accvgpr_read_b32 v3, a79\n"
"v_accvgpr_read_b32 v4, a92\n"
"v_accvgpr_read_b32 v5, a93\n"
"v_accvgpr_read_b32 v6, a94\n"
"v_accvgpr_read_b32 v7, a95\n"
"v_maximum3_f32 v221, v221, |v0|, |v1|\n"
"v_maximum3_f32 v221, v221, |v2|, |v3|\n"
"v_maximum3_f32 v221, v221, |v4|, |v5|\n"
"v_maximum3_f32 v221, v221, |v6|, |v7|\n"
"v_pk_mul_f32 v[0:1], v[0:1], v[206:207] op_sel_hi:[1,0]\n"
"v_pk_mul_f32 v[2:3], v[2:3], v[206:207] op_sel_hi:[1,0]\n"
"v_pk_mul_f32 v[4:5], v[4:5], v[206:207] op_sel_hi:[1,0]\n"
"v_pk_mul_f32 v[6:7], v[6:7], v[206:207] op_sel_hi:[1,0]\n"
"v_cvt_pk_bf16_f32 v24, v0, v1\n"
"v_cvt_pk_bf16_f32 v25, v2, v3\n"
"v_cvt_pk_bf16_f32 v26, v4, v5\n"
"v_cvt_pk_bf16_f32 v27, v6, v7\n"
"buffer_store_dwordx4 v[24:27], v235, s[56:59], s83 offen offset:2048 sc1\n"
"v_accvgpr_read_b32 v8, a108\n"
"v_accvgpr_read_b32 v9, a109\n"
"v_accvgpr_read_b32 v10, a110\n"
"v_accvgpr_read_b32 v11, a111\n"
"v_accvgpr_read_b32 v12, a124\n"
"v_accvgpr_read_b32 v13, a125\n"
"v_accvgpr_read_b32 v14, a126\n"
"v_accvgpr_read_b32 v15, a127\n"
"v_maximum3_f32 v221, v221, |v8|, |v9|\n"
"v_maximum3_f32 v221, v221, |v10|, |v11|\n"
"v_maximum3_f32 v221, v221, |v12|, |v13|\n"
"v_maximum3_f32 v221, v221, |v14|, |v15|\n"
"v_pk_mul_f32 v[8:9], v[8:9], v[206:207] op_sel_hi:[1,0]\n"
"v_pk_mul_f32 v[10:11], v[10:11], v[206:207] op_sel_hi:[1,0]\n"
"v_pk_mul_f32 v[12:13], v[12:13], v[206:207] op_sel_hi:[1,0]\n"
"v_pk_mul_f32 v[14:15], v[14:15], v[206:207] op_sel_hi:[1,0]\n"
"v_cvt_pk_bf16_f32 v16, v8, v9\n"
"v_cvt_pk_bf16_f32 v17, v10, v11\n"
"v_cvt_pk_bf16_f32 v18, v12, v13\n"
"v_cvt_pk_bf16_f32 v19, v14, v15\n"
"buffer_store_dwordx4 v[16:19], v235, s[56:59], s83 offen offset:3072 sc1\n"
"v_mov_b32 v208, 0x7f800000\n"
"s_lshl_b32 s82, s76, 2\n"
"s_add_u32 s82, s82, 0x27ff0\n"
"v_mov_b32 v209, s82\n"
"v_cmp_nlt_f32 vcc, v221, v208\n"
"s_nop 0\n"
"s_cmp_lg_u64 vcc, 0\n"
"s_cselect_b32 s81, 1, 0\n"
"v_mov_b32 v208, s81\n"
"ds_write_b32 v209, v208\n"
"s_waitcnt vmcnt(34) lgkmcnt(0)\n"
"s_barrier\n"
"v_mov_b32 v210, 0x27ff0\n"
"ds_read_b128 v[212:215], v210\n"
"s_waitcnt lgkmcnt(0)\n"
"v_or3_b32 v212, v212, v213, v214\n"
"v_or_b32 v212, v212, v215\n"
"v_sub_u32 v212, v212, s97\n"
"v_cmp_eq_u32 vcc, 1, v212\n"
"s_cbranch_vccnz L_redo\n"
"s_mov_b32 s94, 3\n"
"v_mfma_f32_16x16x32_bf16 v[0:3], a[144:147], a[216:219], 0\n"
"s_add_u32 s92, s92, 0x12000\n"
"s_add_u32 m0, s88, 0\n"
"ds_read_b64_tr_b16 a[72:73], v238 offset:53248\n"
"v_mfma_f32_16x16x32_bf16 v[0:3], a[180:183], a[224:227], v[0:3]\n"
"s_mov_b32 exec_hi, 0\n"
"buffer_load_dwordx4 v233, s[56:59], s92 offen sc1 lds\n"
"s_mov_b32 exec_hi, -1\n"
"v_mfma_f32_16x16x32_bf16 v[4:7], a[144:147], a[220:223], 0\n"
"ds_read_b64_tr_b16 a[74:75], v238 offset:54272\n"
"v_mfma_f32_16x16x32_bf16 v[4:7], a[180:183], a[228:231], v[4:7]\n"
"ds_read_b64_tr_b16 a[80:81], v238 offset:61440\n"
"v_mfma_f32_16x16x32_bf16 v[12:15], a[148:151], a[220:223], 0\n"
"ds_read_b64_tr_b16 a[82:83], v238 offset:62464\n"
"v_mfma_f32_16x16x32_bf16 v[12:15], a[184:187], a[228:231], v[12:15]\n"
"buffer_load_dwordx4 v233, s[56:59], s92 offen offset:512 sc1 lds\n"
"v_mfma_f32_16x16x32_bf16 v[8:11], a[148:151], a[216:219], 0\n"
"ds_read_b64_tr_b16 a[76:77], v239 offset:53248\n"
"v_mfma_f32_16x16x32_bf16 v[8:11], a[184:187], a[224:227], v[8:11]\n"
"ds_read_b64_tr_b16 a[78:79], v239 offset:54272\n"
"v_mfma_f32_16x16x32_bf16 v[16:19], a[152:155], a[216:219], 0\n"
"ds_read_b64_tr_b16 a[84:85], v239 offset:61440\n"
"v_mfma_f32_16x16x32_bf16 v[16:19], a[188:191], a[224:227], v[16:19]\n"
"buffer_load_dwordx4 v233, s[56:59], s92 offen offset:1536 sc1 lds\n"
"v_mfma_f32_16x16x32_bf16 v[20:23], a[152:155], a[220:223], 0\n"
"ds_read_b64_tr_b16 a[86:87], v239 offset:62464\n"
"v_mfma_f32_16x16x32_bf16 v[20:23], a[188:191], a[228:231], v[20:23]\n"
"ds_read_b128 a[0:3], v235 offset:34816\n"
"v_mfma_f32_16x16x32_bf16 v[28:31], a[156:159], a[220:223], 0\n"
"ds_read_b128 a[36:39], v235 offset:35840\n"
"v_mfma_f32_16x16x32_bf16 v[28:31], a[192:195], a[228:231], v[28:31]\n"
"buffer_load_dwordx4 v233, s[56:59], s92 offen offset:2560 sc1 lds\n"
"v_mfma_f32_16x16x32_bf16 v[24:27], a[156:159], a[216:219], 0\n"
"ds_read_b128 a[4:7], v235 offset:36864\n"
"v_mfma_f32_16x16x32_bf16 v[24:27], a[192:195], a[224:227], v[24:27]\n"
"ds_read_b128 a[40:43], v235 offset:37888\n"
"v_mfma_f32_16x16x32_bf16 v[32:35], a[160:163], a[216:219], 0\n"
"ds_read_b128 a[8:11], v235 offset:38912\n"
"v_mfma_f32_16x16x32_bf16 v[32:35], a[196:199], a[224:227], v[32:35]\n"
"buffer_load_dwordx4 v233, s[56:59], s92 offen offset:3584 sc1 lds\n"
"v_mfma_f32_16x16x32_bf16 v[36:39], a[160:163], a[220:223], 0\n"
"s_add_u32 s93, s93, 0x20000\n"
"ds_read_b128 a[44:47], v235 offset:39936\n"
"v_mfma_f32_16x16x32_bf16 v[36:39], a[196:199], a[228:231], v[36:39]\n"
"s_add_u32 m0, s85, 0x4800\n"
"ds_read_b128 a[12:15], v235 offset:40960\n"
"v_mfma_f32_16x16x32_bf16 v[44:47], a[164:167], a[220:223], 0\n"
"ds_read_b128 a[48:51], v235 offset:41984\n"
"v_mfma_f32_16x16x32_bf16 v[44:47], a[200:203], a[228:231], v[44:47]\n"
"buffer_load_dwordx4 v225, s[52:55], s93 offen lds\n"
"v_mfma_f32_16x16x32_bf16 v[40:43], a[164:167], a[216:219], 0\n"
"s_addk_i32 m0, 1984\n"
"ds_read_b128 a[16:19], v235 offset:43008\n"
"v_mfma_f32_16x16x32_bf16 v[40:43], a[200:203], a[224:227], v[40:43]\n"
"ds_read_b128 a[52:55], v235 offset:44032\n"
"v_mfma_f32_16x16x32_bf16 v[48:51], a[168:171], a[216:219], 0\n"
"ds_read_b128 a[20:23], v235 offset:45056\n"
"v_mfma_f32_16x16x32_bf16 v[48:51], a[204:207], a[224:227], v[48:51]\n"
"buffer_load_dwordx4 v225, s[52:55], s93 offen offset:64 lds\n"
"v_mfma_f32_16x16x32_bf16 v[52:55], a[168:171], a[220:223], 0\n"
"s_add_u32 s93, s93, 0x20000\n"
"ds_read_b128 a[56:59], v235 offset:46080\n"
"v_mfma_f32_16x16x32_bf16 v[52:55], a[204:207], a[228:231], v[52:55]\n"
"s_addk_i32 m0, 6208\n"
"ds_read_b128 a[24:27], v235 offset:47104\n"
"v_mfma_f32_16x16x32_bf16 v[60:63], a[172:175], a[220:223], 0\n"
"ds_read_b128 a[60:63], v235 offset:48128\n"
"v_mfma_f32_16x16x32_bf16 v[60:63], a[208:211], a[228:231], v[60:63]\n"
"buffer_load_dwordx4 v225, s[52:55], s93 offen lds\n"
"v_mfma_f32_16x16x32_bf16 v[56:59], a[172:175], a[216:219], 0\n"
"s_addk_i32 m0, 1984\n"
"ds_read_b128 a[28:31], v235 offset:49152\n"
"ds_read_b128 a[64:67], v235 offset:50176\n"
"v_mfma_f32_16x16x32_bf16 v[56:59], a[208:211], a[224:227], v[56:59]\n"
"ds_read_b128 a[32:35], v235 offset:51200\n"
"ds_read_b128 a[68:71], v235 offset:52224\n"
"v_mfma_f32_16x16x32_bf16 v[64:67], a[176:179], a[216:219], 0\n"
"v_mfma_f32_16x16x32_bf16 v[64:67], a[212:215], a[224:227], v[64:67]\n"
"buffer_load_dwordx4 v225, s[52:55], s93 offen offset:64 lds\n"
"v_mfma_f32_16x16x32_bf16 v[68:71], a[176:179], a[220:223], 0\n"
"v_mfma_f32_16x16x32_bf16 v[68:71], a[212:215], a[228:231], v[68:71]\n"
"s_waitcnt vmcnt(34) lgkmcnt(0)\n"
"s_barrier\n"
"L_b2:\n"
"v_mfma_f32_16x16x32_bf16 v[0:3], a[0:3], a[72:75], v[0:3]\n"
"s_add_u32 s92, s92, 0x12000\n"
"s_add_u32 m0, s88, 0x8800\n"
"ds_read_b64_tr_b16 a[216:217], v240 offset:22528\n"
"v_mfma_f32_16x16x32_bf16 v[0:3], a[36:39], a[80:83], v[0:3]\n"
"s_mov_b32 exec_hi, 0\n"
"buffer_load_dwordx4 v233, s[56:59], s92 offen sc1 lds\n"
"s_mov_b32 exec_hi, -1\n"
"v_mfma_f32_16x16x32_bf16 v[4:7], a[0:3], a[76:79], v[4:7]\n"
"ds_read_b64_tr_b16 a[218:219], v240 offset:23552\n"
"v_mfma_f32_16x16x32_bf16 v[4:7], a[36:39], a[84:87], v[4:7]\n"
"ds_read_b64_tr_b16 a[224:225], v240 offset:30720\n"
"v_mfma_f32_16x16x32_bf16 v[12:15], a[4:7], a[76:79], v[12:15]\n"
"ds_read_b64_tr_b16 a[226:227], v240 offset:31744\n"
"v_mfma_f32_16x16x32_bf16 v[12:15], a[40:43], a[84:87], v[12:15]\n"
"buffer_load_dwordx4 v233, s[56:59], s92 offen offset:512 sc1 lds\n"
"v_mfma_f32_16x16x32_bf16 v[8:11], a[4:7], a[72:75], v[8:11]\n"
"ds_read_b64_tr_b16 a[220:221], v241 offset:22528\n"
"v_mfma_f32_16x16x32_bf16 v[8:11], a[40:43], a[80:83], v[8:11]\n"
"ds_read_b64_tr_b16 a[222:223], v241 offset:23552\n"
"v_mfma_f32_16x16x32_bf16 v[16:19], a[8:11], a[72:75], v[16:19]\n"
"ds_read_b64_tr_b16 a[228:229], v241 offset:30720\n"
"v_mfma_f32_16x16x32_bf16 v[16:19], a[44:47], a[80:83], v[16:19]\n"
"buffer_load_dwordx4 v233, s[56:59], s92 offen offset:1536 sc1 lds\n"
"v_mfma_f32_16x16x32_bf16 v[20:23], a[8:11], a[76:79], v[20:23]\n"
"ds_read_b64_tr_b16 a[230:231], v241 offset:31744\n"
"v_mfma_f32_16x16x32_bf16 v[20:23], a[44:47], a[84:87], v[20:23]\n"
"ds_read_b128 a[144:147], v236 offset:4096\n"
"v_mfma_f32_16x16x32_bf16 v[28:31], a[12:15], a[76:79], v[28:31]\n"
"ds_read_b128 a[180:183], v236 offset:5120\n"
"v_mfma_f32_16x16x32_bf16 v[28:31], a[48:51], a[84:87], v[28:31]\n"
"buffer_load_dwordx4 v233, s[56:59], s92 offen offset:2560 sc1 lds\n"
"v_mfma_f32_16x16x32_bf16 v[24:27], a[12:15], a[72:75], v[24:27]\n"
"ds_read_b128 a[148:151], v236 offset:6144\n"
"v_mfma_f32_16x16x32_bf16 v[24:27], a[48:51], a[80:83], v[24:27]\n"
"ds_read_b128 a[184:187], v236 offset:7168\n"
"v_mfma_f32_16x16x32_bf16 v[32:35], a[16:19], a[72:75], v[32:35]\n"
"ds_read_b128 a[152:155], v236 offset:8192\n"
"v_mfma_f32_16x16x32_bf16 v[32:35], a[52:55], a[80:83], v[32:35]\n"
"buffer_load_dwordx4 v233, s[56:59], s92 offen offset:3584 sc1 lds\n"
"v_mfma_f32_16x16x32_bf16 v[36:39], a[16:19], a[76:79], v[36:39]\n"
"s_add_u32 s93, s93, 0x20000\n"
"ds_read_b128 a[188:191], v236 offset:9216\n"
"v_mfma_f32_16x16x32_bf16 v[36:39], a[52:55], a[84:87], v[36:39]\n"
"s_add_u32 m0, s85, 0xd000\n"
"ds_read_b128 a[156:159], v236 offset:10240\n"
"v_mfma_f32_16x16x32_bf16 v[44:47], a[20:23], a[76:79], v[44:47]\n"
"ds_read_b128 a[192:195], v236 offset:11264\n"
"v_mfma_f32_16x16x32_bf16 v[44:47], a[56:59], a[84:87], v[44:47]\n"
"buffer_load_dwordx4 v225, s[52:55], s93 offen lds\n"
"v_mfma_f32_16x16x32_bf16 v[40:43], a[20:23], a[72:75], v[40:43]\n"
"s_addk_i32 m0, 1984\n"
"ds_read_b128 a[160:163], v236 offset:12288\n"
"v_mfma_f32_16x16x32_bf16 v[40:43], a[56:59], a[80:83], v[40:43]\n"
"ds_read_b128 a[196:199], v236 offset:13312\n"
"v_mfma_f32_16x16x32_bf16 v[48:51], a[24:27], a[72:75], v[48:51]\n"
"ds_read_b128 a[164:167], v236 offset:14336\n"
"v_mfma_f32_16x16x32_bf16 v[48:51], a[60:63], a[80:83], v[48:51]\n"
"buffer_load_dwordx4 v225, s[52:55], s93 offen offset:64 lds\n"
"v_mfma_f32_16x16x32_bf16 v[52:55], a[24:27], a[76:79], v[52:55]\n"
"s_add_u32 s93, s93, 0x20000\n"
"ds_read_b128 a[200:203], v236 offset:15360\n"
"v_mfma_f32_16x16x32_bf16 v[52:55], a[60:63], a[84:87], v[52:55]\n"
"s_addk_i32 m0, 6208\n"
"ds_read_b128 a[168:171], v236 offset:16384\n"
"v_mfma_f32_16x16x32_bf16 v[60:63], a[28:31], a[76:79], v[60:63]\n"
"ds_read_b128 a[204:207], v236 offset:17408\n"
"v_mfma_f32_16x16x32_bf16 v[60:63], a[64:67], a[84:87], v[60:63]\n"
"buffer_load_dwordx4 v225, s[52:55], s93 offen lds\n"
"v_mfma_f32_16x16x32_bf16 v[56:59], a[28:31], a[72:75], v[56:59]\n"
"s_addk_i32 m0, 1984\n"
"ds_read_b128 a[172:175], v236 offset:18432\n"
"ds_read_b128 a[208:211], v236 offset:19456\n"
"v_mfma_f32_16x16x32_bf16 v[56:59], a[64:67], a[80:83], v[56:59]\n"
"ds_read_b128 a[176:179], v236 offset:20480\n"
"ds_read_b128 a[212:215], v236 offset:21504\n"
"v_mfma_f32_16x16x32_bf16 v[64:67], a[32:35], a[72:75], v[64:67]\n"
"v_mfma_f32_16x16x32_bf16 v[64:67], a[68:71], a[80:83], v[64:67]\n"
"buffer_load_dwordx4 v225, s[52:55], s93 offen offset:64 lds\n"
"v_mfma_f32_16x16x32_bf16 v[68:71], a[32:35], a[76:79], v[68:71]\n"
"v_mfma_f32_16x16x32_bf16 v[68:71], a[68:71], a[84:87], v[68:71]\n"
"s_waitcnt vmcnt(20) lgkmcnt(0)\n"
"s_barrier\n"
"v_mfma_f32_16x16x32_bf16 v[0:3], a[144:147], a[216:219], v[0:3]\n"
"s_add_u32 s92, s92, 0x12000\n"
"s_add_u32 m0, s88, 0x11000\n"
"ds_read_b64_tr_b16 a[72:73], v240 offset:57344\n"
"v_mfma_f32_16x16x32_bf16 v[0:3], a[180:183], a[224:227], v[0:3]\n"
"s_mov_b32 exec_hi, 0\n"
"buffer_load_dwordx4 v233, s[56:59], s92 offen sc1 lds\n"
"s_mov_b32 exec_hi, -1\n"
"v_mfma_f32_16x16x32_bf16 v[4:7], a[144:147], a[220:223], v[4:7]\n"
"ds_read_b64_tr_b16 a[74:75], v240 offset:58368\n"
"v_mfma_f32_16x16x32_bf16 v[4:7], a[180:183], a[228:231], v[4:7]\n"
"ds_read_b64_tr_b16 a[80:81], v242\n"
"v_mfma_f32_16x16x32_bf16 v[12:15], a[148:151], a[220:223], v[12:15]\n"
"ds_read_b64_tr_b16 a[82:83], v242 offset:1024\n"
"v_mfma_f32_16x16x32_bf16 v[12:15], a[184:187], a[228:231], v[12:15]\n"
"buffer_load_dwordx4 v233, s[56:59], s92 offen offset:512 sc1 lds\n"
"v_mfma_f32_16x16x32_bf16 v[8:11], a[148:151], a[216:219], v[8:11]\n"
"ds_read_b64_tr_b16 a[76:77], v241 offset:57344\n"
"v_mfma_f32_16x16x32_bf16 v[8:11], a[184:187], a[224:227], v[8:11]\n"
"ds_read_b64_tr_b16 a[78:79], v241 offset:58368\n"
"v_mfma_f32_16x16x32_bf16 v[16:19], a[152:155], a[216:219], v[16:19]\n"
"ds_read_b64_tr_b16 a[84:85], v243\n"
"v_mfma_f32_16x16x32_bf16 v[16:19], a[188:191], a[224:227], v[16:19]\n"
"buffer_load_dwordx4 v233, s[56:59], s92 offen offset:1536 sc1 lds\n"
"v_mfma_f32_16x16x32_bf16 v[20:23], a[152:155], a[220:223], v[20:23]\n"
"ds_read_b64_tr_b16 a[86:87], v243 offset:1024\n"
"v_mfma_f32_16x16x32_bf16 v[20:23], a[188:191], a[228:231], v[20:23]\n"
"ds_read_b128 a[0:3], v236 offset:38912\n"
"v_mfma_f32_16x16x32_bf16 v[28:31], a[156:159], a[220:223], v[28:31]\n"
"ds_read_b128 a[36:39], v236 offset:39936\n"
"v_mfma_f32_16x16x32_bf16 v[28:31], a[192:195], a[228:231], v[28:31]\n"
"buffer_load_dwordx4 v233, s[56:59], s92 offen offset:2560 sc1 lds\n"
"v_mfma_f32_16x16x32_bf16 v[24:27], a[156:159], a[216:219], v[24:27]\n"
"ds_read_b128 a[4:7], v236 offset:40960\n"
"v_mfma_f32_16x16x32_bf16 v[24:27], a[192:195], a[224:227], v[24:27]\n"
"ds_read_b128 a[40:43], v236 offset:41984\n"
"v_mfma_f32_16x16x32_bf16 v[32:35], a[160:163], a[216:219], v[32:35]\n"
"ds_read_b128 a[8:11], v236 offset:43008\n"
"v_mfma_f32_16x16x32_bf16 v[32:35], a[196:199], a[224:227], v[32:35]\n"
"buffer_load_dwordx4 v233, s[56:59], s92 offen offset:3584 sc1 lds\n"
"v_mfma_f32_16x16x32_bf16 v[36:39], a[160:163], a[220:223], v[36:39]\n"
"s_add_u32 s93, s93, 0x20000\n"
"ds_read_b128 a[44:47], v236 offset:44032\n"
"v_mfma_f32_16x16x32_bf16 v[36:39], a[196:199], a[228:231], v[36:39]\n"
"s_add_u32 m0, s85, 0x15800\n"
"ds_read_b128 a[12:15], v236 offset:45056\n"
"v_mfma_f32_16x16x32_bf16 v[44:47], a[164:167], a[220:223], v[44:47]\n"
"ds_read_b128 a[48:51], v236 offset:46080\n"
"v_mfma_f32_16x16x32_bf16 v[44:47], a[200:203], a[228:231], v[44:47]\n"
"buffer_load_dwordx4 v225, s[52:55], s93 offen lds\n"
"v_mfma_f32_16x16x32_bf16 v[40:43], a[164:167], a[216:219], v[40:43]\n"
"s_addk_i32 m0, 1984\n"
"ds_read_b128 a[16:19], v236 offset:47104\n"
"v_mfma_f32_16x16x32_bf16 v[40:43], a[200:203], a[224:227], v[40:43]\n"
"ds_read_b128 a[52:55], v236 offset:48128\n"
"v_mfma_f32_16x16x32_bf16 v[48:51], a[168:171], a[216:219], v[48:51]\n"
"ds_read_b128 a[20:23], v236 offset:49152\n"
"v_mfma_f32_16x16x32_bf16 v[48:51], a[204:207], a[224:227], v[48:51]\n"
"buffer_load_dwordx4 v225, s[52:55], s93 offen offset:64 lds\n"
"v_mfma_f32_16x16x32_bf16 v[52:55], a[168:171], a[220:223], v[52:55]\n"
"s_add_u32 s93, s93, 0x20000\n"
"ds_read_b128 a[56:59], v236 offset:50176\n"
"v_mfma_f32_16x16x32_bf16 v[52:55], a[204:207], a[228:231], v[52:55]\n"
"s_addk_i32 m0, 6208\n"
"ds_read_b128 a[24:27], v236 offset:51200\n"
"v_mfma_f32_16x16x32_bf16 v[60:63], a[172:175], a[220:223], v[60:63]\n"
"ds_read_b128 a[60:63], v236 offset:52224\n"
"v_mfma_f32_16x16x32_bf16 v[60:63], a[208:211], a[228:231], v[60:63]\n"
"buffer_load_dwordx4 v225, s[52:55], s93 offen lds\n"
"v_mfma_f32_16x16x32_bf16 v[56:59], a[172:175], a[216:219], v[56:59]\n"
"s_addk_i32 m0, 1984\n"
"ds_read_b128 a[28:31], v236 offset:53248\n"
"ds_read_b128 a[64:67], v236 offset:54272\n"
"v_mfma_f32_16x16x32_bf16 v[56:59], a[208:211], a[224:227], v[56:59]\n"
"ds_read_b128 a[32:35], v236 offset:55296\n"
"ds_read_b128 a[68:71], v236 offset:56320\n"
"v_mfma_f32_16x16x32_bf16 v[64:67], a[176:179], a[216:219], v[64:67]\n"
"v_mfma_f32_16x16x32_bf16 v[64:67], a[212:215], a[224:227], v[64:67]\n"
"buffer_load_dwordx4 v225, s[52:55], s93 offen offset:64 lds\n"
"v_mfma_f32_16x16x32_bf16 v[68:71], a[176:179], a[220:223], v[68:71]\n"
"v_mfma_f32_16x16x32_bf16 v[68:71], a[212:215], a[228:231], v[68:71]\n"
"s_waitcnt vmcnt(18) lgkmcnt(0)\n"
"s_barrier\n"
"v_mfma_f32_16x16x32_bf16 v[0:3], a[0:3], a[72:75], v[0:3]\n"
"s_add_u32 s92, s92, 0x12000\n"
"s_add_u32 m0, s88, 0x19800\n"
"ds_read_b64_tr_b16 a[216:217], v238 offset:18432\n"
"v_mfma_f32_16x16x32_bf16 v[0:3], a[36:39], a[80:83], v[0:3]\n"
"s_mov_b32 exec_hi, 0\n"
"buffer_load_dwordx4 v233, s[56:59], s92 offen sc1 lds\n"
"s_mov_b32 exec_hi, -1\n"
"v_mfma_f32_16x16x32_bf16 v[4:7], a[0:3], a[76:79], v[4:7]\n"
"ds_read_b64_tr_b16 a[218:219], v238 offset:19456\n"
"v_mfma_f32_16x16x32_bf16 v[4:7], a[36:39], a[84:87], v[4:7]\n"
"ds_read_b64_tr_b16 a[224:225], v238 offset:26624\n"
"v_mfma_f32_16x16x32_bf16 v[12:15], a[4:7], a[76:79], v[12:15]\n"
"ds_read_b64_tr_b16 a[226:227], v238 offset:27648\n"
"v_mfma_f32_16x16x32_bf16 v[12:15], a[40:43], a[84:87], v[12:15]\n"
"buffer_load_dwordx4 v233, s[56:59], s92 offen offset:512 sc1 lds\n"
"v_mfma_f32_16x16x32_bf16 v[8:11], a[4:7], a[72:75], v[8:11]\n"
"ds_read_b64_tr_b16 a[220:221], v239 offset:18432\n"
"v_mfma_f32_16x16x32_bf16 v[8:11], a[40:43], a[80:83], v[8:11]\n"
"ds_read_b64_tr_b16 a[222:223], v239 offset:19456\n"
"v_mfma_f32_16x16x32_bf16 v[16:19], a[8:11], a[72:75], v[16:19]\n"
"buffer_load_dwordx4 v233, s[56:59], s92 offen offset:1536 sc1 lds\n"
"v_mfma_f32_16x16x32_bf16 v[16:19], a[44:47], a[80:83], v[16:19]\n"
"ds_read_b64_tr_b16 a[228:229], v239 offset:26624\n"
"v_mfma_f32_16x16x32_bf16 v[20:23], a[8:11], a[76:79], v[20:23]\n"
"ds_read_b64_tr_b16 a[230:231], v239 offset:27648\n"
"v_mfma_f32_16x16x32_bf16 v[20:23], a[44:47], a[84:87], v[20:23]\n"
"ds_read_b128 a[144:147], v235\n"
"v_mfma_f32_16x16x32_bf16 v[28:31], a[12:15], a[76:79], v[28:31]\n"
"buffer_load_dwordx4 v233, s[56:59], s92 offen offset:2560 sc1 lds\n"
"v_mfma_f32_16x16x32_bf16 v[28:31], a[48:51], a[84:87], v[28:31]\n"
"ds_read_b128 a[180:183], v235 offset:1024\n"
"v_mfma_f32_16x16x32_bf16 v[24:27], a[12:15], a[72:75], v[24:27]\n"
"ds_read_b128 a[148:151], v235 offset:2048\n"
"v_mfma_f32_16x16x32_bf16 v[24:27], a[48:51], a[80:83], v[24:27]\n"
"buffer_load_dwordx4 v233, s[56:59], s92 offen offset:3584 sc1 lds\n"
"v_mfma_f32_16x16x32_bf16 v[32:35], a[16:19], a[72:75], v[32:35]\n"
"s_add_u32 s93, s93, 0x20000\n"
"ds_read_b128 a[184:187], v235 offset:3072\n"
"v_mfma_f32_16x16x32_bf16 v[32:35], a[52:55], a[80:83], v[32:35]\n"
"s_add_u32 m0, s85, 0x1e000\n"
"ds_read_b128 a[152:155], v235 offset:4096\n"
"v_mfma_f32_16x16x32_bf16 v[36:39], a[16:19], a[76:79], v[36:39]\n"
"ds_read_b128 a[188:191], v235 offset:5120\n"
"v_mfma_f32_16x16x32_bf16 v[36:39], a[52:55], a[84:87], v[36:39]\n"
"buffer_load_dwordx4 v225, s[52:55], s93 offen lds\n"
"v_mfma_f32_16x16x32_bf16 v[44:47], a[20:23], a[76:79], v[44:47]\n"
"s_addk_i32 m0, 1984\n"
"ds_read_b128 a[156:159], v235 offset:6144\n"
"v_mfma_f32_16x16x32_bf16 v[44:47], a[56:59], a[84:87], v[44:47]\n"
"ds_read_b128 a[192:195], v235 offset:7168\n"
"v_mfma_f32_16x16x32_bf16 v[40:43], a[20:23], a[72:75], v[40:43]\n"
"buffer_load_dwordx4 v225, s[52:55], s93 offen offset:64 lds\n"
"v_mfma_f32_16x16x32_bf16 v[40:43], a[56:59], a[80:83], v[40:43]\n"
"s_add_u32 s93, s93, 0x20000\n"
"ds_read_b128 a[160:163], v235 offset:8192\n"
"v_mfma_f32_16x16x32_bf16 v[48:51], a[24:27], a[72:75], v[48:51]\n"
"s_addk_i32 m0, 6208\n"
"ds_read_b128 a[196:199], v235 offset:9216\n"
"v_mfma_f32_16x16x32_bf16 v[48:51], a[60:63], a[80:83], v[48:51]\n"
"ds_read_b128 a[164:167], v235 offset:10240\n"
"v_mfma_f32_16x16x32_bf16 v[52:55], a[24:27], a[76:79], v[52:55]\n"
"buffer_load_dwordx4 v225, s[52:55], s93 offen lds\n"
"v_mfma_f32_16x16x32_bf16 v[52:55], a[60:63], a[84:87], v[52:55]\n"
"s_addk_i32 m0, 1984\n"
"ds_read_b128 a[200:203], v235 offset:11264\n"
"v_mfma_f32_16x16x32_bf16 v[60:63], a[28:31], a[76:79], v[60:63]\n"
"ds_read_b128 a[168:171], v235 offset:12288\n"
"v_mfma_f32_16x16x32_bf16 v[60:63], a[64:67], a[84:87], v[60:63]\n"
"buffer_load_dwordx4 v225, s[52:55], s93 offen offset:64 lds\n"
"v_mfma_f32_16x16x32_bf16 v[56:59], a[28:31], a[72:75], v[56:59]\n"
"s_add_u32 s84, s8, 0x18000\n"
"s_cmp_eq_u32 s94, 3\n"
"s_cselect_b32 s84, s84, 0x7ffffff0\n"
"v_mfma_f32_16x16x32_bf16 v[56:59], a[64:67], a[80:83], v[56:59]\n"
"ds_read_b128 a[204:207], v235 offset:13312\n"
"v_mov_b32 v254, s95\n"
"v_mfma_f32_16x16x32_bf16 v[64:67], a[32:35], a[72:75], v[64:67]\n"
"ds_read_b128 a[172:175], v235 offset:14336\n"
"ds_read_b128 a[208:211], v235 offset:15360\n"
"ds_read_b128 a[176:179], v235 offset:16384\n"
"ds_read_b128 a[212:215], v235 offset:17408\n"
"v_mfma_f32_16x16x32_bf16 v[64:67], a[68:71], a[80:83], v[64:67]\n"
"buffer_store_dword v254, v234, s[56:59], s84 offen sc1\n"
"v_mfma_f32_16x16x32_bf16 v[68:71], a[32:35], a[76:79], v[68:71]\n"
"v_mfma_f32_16x16x32_bf16 v[68:71], a[68:71], a[84:87], v[68:71]\n"
"s_waitcnt vmcnt(19) lgkmcnt(0)\n"
"s_barrier\n"
"v_mfma_f32_16x16x32_bf16 v[0:3], a[144:147], a[216:219], v[0:3]\n"
"s_lshl_b32 s84, s78, 2\n"
"s_add_u32 s84, s84, s76\n"
"s_lshl_b32 s84, s84, 11\n"
"v_and_b32 v254, 15, v224\n"
"v_lshlrev_b32 v254, 7, v254\n"
"s_add_u32 s84, s84, 0x18000\n"
"s_cmp_eq_u32 s94, 1\n"
"s_cselect_b32 s84, s84, 0x7ffffff0\n"
"v_mfma_f32_16x16x32_bf16 v[0:3], a[180:183], a[224:227], v[0:3]\n"
"buffer_load_dword v249, v254, s[56:59], s84 offen sc1\n"
"v_mfma_f32_16x16x32_bf16 v[4:7], a[144:147], a[220:223], v[4:7]\n"
"s_add_u32 s92, s92, 0x12000\n"
"ds_read_b64_tr_b16 a[72:73], v238 offset:53248\n"
"v_mfma_f32_16x16x32_bf16 v[4:7], a[180:183], a[228:231], v[4:7]\n"
"s_add_u32 m0, s88, 0\n"
"ds_read_b64_tr_b16 a[74:75], v238 offset:54272\n"
"v_mfma_f32_16x16x32_bf16 v[12:15], a[148:151], a[220:223], v[12:15]\n"
"ds_read_b64_tr_b16 a[80:81], v238 offset:61440\n"
"v_mfma_f32_16x16x32_bf16 v[12:15], a[184:187], a[228:231], v[12:15]\n"
"s_mov_b32 exec_hi, 0\n"
"buffer_load_dwordx4 v233, s[56:59], s92 offen sc1 lds\n"
"s_mov_b32 exec_hi, -1\n"
"v_mfma_f32_16x16x32_bf16 v[8:11], a[148:151], a[216:219], v[8:11]\n"
"ds_read_b64_tr_b16 a[82:83], v238 offset:62464\n"
"v_mfma_f32_16x16x32_bf16 v[8:11], a[184:187], a[224:227], v[8:11]\n"
"ds_read_b64_tr_b16 a[76:77], v239 offset:53248\n"
"v_mfma_f32_16x16x32_bf16 v[16:19], a[152:155], a[216:219], v[16:19]\n"
"buffer_load_dwordx4 v233, s[56:59], s92 offen offset:512 sc1 lds\n"
"v_mfma_f32_16x16x32_bf16 v[16:19], a[188:191], a[224:227], v[16:19]\n"
"ds_read_b64_tr_b16 a[78:79], v239 offset:54272\n"
"v_mfma_f32_16x16x32_bf16 v[20:23], a[152:155], a[220:223], v[20:23]\n"
"ds_read_b64_tr_b16 a[84:85], v239 offset:61440\n"
"v_mfma_f32_16x16x32_bf16 v[20:23], a[188:191], a[228:231], v[20:23]\n"
"ds_read_b64_tr_b16 a[86:87], v239 offset:62464\n"
"v_mfma_f32_16x16x32_bf16 v[28:31], a[156:159], a[220:223], v[28:31]\n"
"buffer_load_dwordx4 v233, s[56:59], s92 offen offset:1536 sc1 lds\n"
"v_mfma_f32_16x16x32_bf16 v[28:31], a[192:195], a[228:231], v[28:31]\n"
"ds_read_b128 a[0:3], v235 offset:34816\n"
"v_mfma_f32_16x16x32_bf16 v[24:27], a[156:159], a[216:219], v[24:27]\n"
"ds_read_b128 a[36:39], v235 offset:35840\n"
"v_mfma_f32_16x16x32_bf16 v[24:27], a[192:195], a[224:227], v[24:27]\n"
"buffer_load_dwordx4 v233, s[56:59], s92 offen offset:2560 sc1 lds\n"
"v_mfma_f32_16x16x32_bf16 v[32:35], a[160:163], a[216:219], v[32:35]\n"
"ds_read_b128 a[4:7], v235 offset:36864\n"
"v_mfma_f32_16x16x32_bf16 v[32:35], a[196:199], a[224:227], v[32:35]\n"
"ds_read_b128 a[40:43], v235 offset:37888\n"
"v_mfma_f32_16x16x32_bf16 v[36:39], a[160:163], a[220:223], v[36:39]\n"
"ds_read_b128 a[8:11], v235 offset:38912\n"
"v_mfma_f32_16x16x32_bf16 v[36:39], a[196:199], a[228:231], v[36:39]\n"
"buffer_load_dwordx4 v233, s[56:59], s92 offen offset:3584 sc1 lds\n"
"v_mfma_f32_16x16x32_bf16 v[44:47], a[164:167], a[220:223], v[44:47]\n"
"s_add_u32 s93, s93, 0x20000\n"
"ds_read_b128 a[44:47], v235 offset:39936\n"
"s_add_u32 m0, s85, 0x4800\n"
"v_mfma_f32_16x16x32_bf16 v[44:47], a[200:203], a[228:231], v[44:47]\n"
"ds_read_b128 a[12:15], v235 offset:40960\n"
"v_mfma_f32_16x16x32_bf16 v[40:43], a[164:167], a[216:219], v[40:43]\n"
"buffer_load_dwordx4 v225, s[52:55], s93 offen lds\n"
"v_mfma_f32_16x16x32_bf16 v[40:43], a[200:203], a[224:227], v[40:43]\n"
"s_addk_i32 m0, 1984\n"
"ds_read_b128 a[48:51], v235 offset:41984\n"
"ds_read_b128 a[16:19], v235 offset:43008\n"
"v_mfma_f32_16x16x32_bf16 v[48:51], a[168:171], a[216:219], v[48:51]\n"
"ds_read_b128 a[52:55], v235 offset:44032\n"
"v_mfma_f32_16x16x32_bf16 v[48:51], a[204:207], a[224:227], v[48:51]\n"
"ds_read_b128 a[20:23], v235 offset:45056\n"
"v_mfma_f32_16x16x32_bf16 v[52:55], a[168:171], a[220:223], v[52:55]\n"
"buffer_load_dwordx4 v225, s[52:55], s93 offen offset:64 lds\n"
"v_mfma_f32_16x16x32_bf16 v[52:55], a[204:207], a[228:231], v[52:55]\n"
"s_add_u32 s93, s93, 0x20000\n"
"ds_read_b128 a[56:59], v235 offset:46080\n"
"s_addk_i32 m0, 6208\n"
"v_mfma_f32_16x16x32_bf16 v[60:63], a[172:175], a[220:223], v[60:63]\n"
"s_sub_u32 s94, s94, 1\n"
"ds_read_b128 a[24:27], v235 offset:47104\n"
"v_mfma_f32_16x16x32_bf16 v[60:63], a[208:211], a[228:231], v[60:63]\n"
"buffer_load_dwordx4 v225, s[52:55], s93 offen lds\n"
"v_mfma_f32_16x16x32_bf16 v[56:59], a[172:175], a[216:219], v[56:59]\n"
"s_addk_i32 m0, 1984\n"
"ds_read_b128 a[60:63], v235 offset:48128\n"
"ds_read_b128 a[28:31], v235 offset:49152\n"
"v_mfma_f32_16x16x32_bf16 v[56:59], a[208:211], a[224:227], v[56:59]\n"
"ds_read_b128 a[64:67], v235 offset:50176\n"
"ds_read_b128 a[32:35], v235 offset:51200\n"
"v_mfma_f32_16x16x32_bf16 v[64:67], a[176:179], a[216:219], v[64:67]\n"
"ds_read_b128 a[68:71], v235 offset:52224\n"
"v_mfma_f32_16x16x32_bf16 v[64:67], a[212:215], a[224:227], v[64:67]\n"
"buffer_load_dwordx4 v225, s[52:55], s93 offen offset:64 lds\n"
"v_mfma_f32_16x16x32_bf16 v[68:71], a[176:179], a[220:223], v[68:71]\n"
"s_cmp_lg_u32 s94, 0\n"
"v_mfma_f32_16x16x32_bf16 v[68:71], a[212:215], a[228:231], v[68:71]\n"
"s_waitcnt vmcnt(20) lgkmcnt(0)\n"
"s_barrier\n"
"s_cbranch_scc1 L_b2\n"
"v_mfma_f32_16x16x32_bf16 v[0:3], a[0:3], a[72:75], v[0:3]\n"
"s_add_u32 s92, s92, 0x12000\n"
"s_add_u32 m0, s88, 0x8800\n"
"ds_read_b64_tr_b16 a[216:217], v240 offset:22528\n"
"v_mfma_f32_16x16x32_bf16 v[0:3], a[36:39], a[80:83], v[0:3]\n"
"s_mov_b32 exec_hi, 0\n"
"buffer_load_dwordx4 v233, s[56:59], s92 offen sc1 lds\n"
"s_mov_b32 exec_hi, -1\n"
"v_mfma_f32_16x16x32_bf16 v[4:7], a[0:3], a[76:79], v[4:7]\n"
"ds_read_b64_tr_b16 a[218:219], v240 offset:23552\n"
"v_mfma_f32_16x16x32_bf16 v[4:7], a[36:39], a[84:87], v[4:7]\n"
"ds_read_b64_tr_b16 a[224:225], v240 offset:30720\n"
"v_mfma_f32_16x16x32_bf16 v[12:15], a[4:7], a[76:79], v[12:15]\n"
"ds_read_b64_tr_b16 a[226:227], v240 offset:31744\n"
"v_mfma_f32_16x16x32_bf16 v[12:15], a[40:43], a[84:87], v[12:15]\n"
"buffer_load_dwordx4 v233, s[56:59], s92 offen offset:512 sc1 lds\n"
"v_mfma_f32_16x16x32_bf16 v[8:11], a[4:7], a[72:75], v[8:11]\n"
"ds_read_b64_tr_b16 a[220:221], v241 offset:22528\n"
"v_mfma_f32_16x16x32_bf16 v[8:11], a[40:43], a[80:83], v[8:11]\n"
"ds_read_b64_tr_b16 a[222:223], v241 offset:23552\n"
"v_mfma_f32_16x16x32_bf16 v[16:19], a[8:11], a[72:75], v[16:19]\n"
"ds_read_b64_tr_b16 a[228:229], v241 offset:30720\n"
"v_mfma_f32_16x16x32_bf16 v[16:19], a[44:47], a[80:83], v[16:19]\n"
"buffer_load_dwordx4 v233, s[56:59], s92 offen offset:1536 sc1 lds\n"
"v_mfma_f32_16x16x32_bf16 v[20:23], a[8:11], a[76:79], v[20:23]\n"
"ds_read_b64_tr_b16 a[230:231], v241 offset:31744\n"
"v_mfma_f32_16x16x32_bf16 v[20:23], a[44:47], a[84:87], v[20:23]\n"
"ds_read_b128 a[144:147], v236 offset:4096\n"
"v_mfma_f32_16x16x32_bf16 v[28:31], a[12:15], a[76:79], v[28:31]\n"
"ds_read_b128 a[180:183], v236 offset:5120\n"
"v_mfma_f32_16x16x32_bf16 v[28:31], a[48:51], a[84:87], v[28:31]\n"
"buffer_load_dwordx4 v233, s[56:59], s92 offen offset:2560 sc1 lds\n"
"v_mfma_f32_16x16x32_bf16 v[24:27], a[12:15], a[72:75], v[24:27]\n"
"ds_read_b128 a[148:151], v236 offset:6144\n"
"v_mfma_f32_16x16x32_bf16 v[24:27], a[48:51], a[80:83], v[24:27]\n"
"ds_read_b128 a[184:187], v236 offset:7168\n"
"v_mfma_f32_16x16x32_bf16 v[32:35], a[16:19], a[72:75], v[32:35]\n"
"ds_read_b128 a[152:155], v236 offset:8192\n"
"v_mfma_f32_16x16x32_bf16 v[32:35], a[52:55], a[80:83], v[32:35]\n"
"buffer_load_dwordx4 v233, s[56:59], s92 offen offset:3584 sc1 lds\n"
"v_mfma_f32_16x16x32_bf16 v[36:39], a[16:19], a[76:79], v[36:39]\n"
"s_add_u32 s93, s93, 0x20000\n"
"ds_read_b128 a[188:191], v236 offset:9216\n"
"v_mfma_f32_16x16x32_bf16 v[36:39], a[52:55], a[84:87], v[36:39]\n"
"s_add_u32 m0, s85, 0xd000\n"
"ds_read_b128 a[156:159], v236 offset:10240\n"
"v_mfma_f32_16x16x32_bf16 v[44:47], a[20:23], a[76:79], v[44:47]\n"
"ds_read_b128 a[192:195], v236 offset:11264\n"
"v_mfma_f32_16x16x32_bf16 v[44:47], a[56:59], a[84:87], v[44:47]\n"
"buffer_load_dwordx4 v225, s[52:55], s93 offen lds\n"
"v_mfma_f32_16x16x32_bf16 v[40:43], a[20:23], a[72:75], v[40:43]\n"
"s_addk_i32 m0, 1984\n"
"ds_read_b128 a[160:163], v236 offset:12288\n"
"v_mfma_f32_16x16x32_bf16 v[40:43], a[56:59], a[80:83], v[40:43]\n"
"ds_read_b128 a[196:199], v236 offset:13312\n"
"v_mfma_f32_16x16x32_bf16 v[48:51], a[24:27], a[72:75], v[48:51]\n"
"ds_read_b128 a[164:167], v236 offset:14336\n"
"v_mfma_f32_16x16x32_bf16 v[48:51], a[60:63], a[80:83], v[48:51]\n"
"buffer_load_dwordx4 v225, s[52:55], s93 offen offset:64 lds\n"
"v_mfma_f32_16x16x32_bf16 v[52:55], a[24:27], a[76:79], v[52:55]\n"
"s_add_u32 s93, s93, 0x20000\n"
"ds_read_b128 a[200:203], v236 offset:15360\n"
"v_mfma_f32_16x16x32_bf16 v[52:55], a[60:63], a[84:87], v[52:55]\n"
"s_addk_i32 m0, 6208\n"
"ds_read_b128 a[168:171], v236 offset:16384\n"
"v_mfma_f32_16x16x32_bf16 v[60:63], a[28:31], a[76:79], v[60:63]\n"
"ds_read_b128 a[204:207], v236 offset:17408\n"
"v_mfma_f32_16x16x32_bf16 v[60:63], a[64:67], a[84:87], v[60:63]\n"
"buffer_load_dwordx4 v225, s[52:55], s93 offen lds\n"
"v_mfma_f32_16x16x32_bf16 v[56:59], a[28:31], a[72:75], v[56:59]\n"
"s_addk_i32 m0, 1984\n"
"ds_read_b128 a[172:175], v236 offset:18432\n"
"ds_read_b128 a[208:211], v236 offset:19456\n"
"v_mfma_f32_16x16x32_bf16 v[56:59], a[64:67], a[80:83], v[56:59]\n"
"ds_read_b128 a[176:179], v236 offset:20480\n"
"ds_read_b128 a[212:215], v236 offset:21504\n"
"v_mfma_f32_16x16x32_bf16 v[64:67], a[32:35], a[72:75], v[64:67]\n"
"v_mfma_f32_16x16x32_bf16 v[64:67], a[68:71], a[80:83], v[64:67]\n"
"buffer_load_dwordx4 v225, s[52:55], s93 offen offset:64 lds\n"
"v_mfma_f32_16x16x32_bf16 v[68:71], a[32:35], a[76:79], v[68:71]\n"
"v_mfma_f32_16x16x32_bf16 v[68:71], a[68:71], a[84:87], v[68:71]\n"
"s_waitcnt vmcnt(20) lgkmcnt(0)\n"
"s_waitcnt vmcnt(18)\n"
"v_cmp_ne_u32 vcc, s95, v249\n"
"s_cbranch_vccnz L_slow3\n"
"L_back3:\n"
"s_barrier\n"
"v_mfma_f32_16x16x32_bf16 v[0:3], a[144:147], a[216:219], v[0:3]\n"
"s_mov_b32 s90, s5\n"
"ds_read_b64_tr_b16 a[72:73], v240 offset:57344\n"
"v_mfma_f32_16x16x32_bf16 v[0:3], a[180:183], a[224:227], v[0:3]\n"
"s_add_u32 m0, s88, 0x22000\n"
"ds_read_b64_tr_b16 a[74:75], v240 offset:58368\n"
"v_mfma_f32_16x16x32_bf16 v[4:7], a[144:147], a[220:223], v[4:7]\n"
"ds_read_b64_tr_b16 a[80:81], v242\n"
"v_mfma_f32_16x16x32_bf16 v[4:7], a[180:183], a[228:231], v[4:7]\n"
"s_mov_b32 exec_hi, 0\n"
"buffer_load_dwordx4 v233, s[56:59], s90 offen sc1 lds\n"
"s_mov_b32 exec_hi, -1\n"
"v_mfma_f32_16x16x32_bf16 v[12:15], a[148:151], a[220:223], v[12:15]\n"
"ds_read_b64_tr_b16 a[82:83], v242 offset:1024\n"
"v_mfma_f32_16x16x32_bf16 v[12:15], a[184:187], a[228:231], v[12:15]\n"
"ds_read_b64_tr_b16 a[76:77], v241 offset:57344\n"
"v_mfma_f32_16x16x32_bf16 v[8:11], a[148:151], a[216:219], v[8:11]\n"
"ds_read_b64_tr_b16 a[78:79], v241 offset:58368\n"
"v_mfma_f32_16x16x32_bf16 v[8:11], a[184:187], a[224:227], v[8:11]\n"
"ds_read_b64_tr_b16 a[84:85], v243\n"
"v_mfma_f32_16x16x32_bf16 v[16:19], a[152:155], a[216:219], v[16:19]\n"
"ds_read_b64_tr_b16 a[86:87], v243 offset:1024\n"
"v_mfma_f32_16x16x32_bf16 v[16:19], a[188:191], a[224:227], v[16:19]\n"
"ds_read_b128 a[0:3], v236 offset:38912\n"
"v_mfma_f32_16x16x32_bf16 v[20:23], a[152:155], a[220:223], v[20:23]\n"
"buffer_load_dwordx4 v233, s[56:59], s90 offen offset:512 sc1 lds\n"
"v_mfma_f32_16x16x32_bf16 v[20:23], a[188:191], a[228:231], v[20:23]\n"
"ds_read_b128 a[36:39], v236 offset:39936\n"
"v_mfma_f32_16x16x32_bf16 v[28:31], a[156:159], a[220:223], v[28:31]\n"
"ds_read_b128 a[4:7], v236 offset:40960\n"
"v_mfma_f32_16x16x32_bf16 v[28:31], a[192:195], a[228:231], v[28:31]\n"
"ds_read_b128 a[40:43], v236 offset:41984\n"
"v_mfma_f32_16x16x32_bf16 v[24:27], a[156:159], a[216:219], v[24:27]\n"
"ds_read_b128 a[8:11], v236 offset:43008\n"
"v_mfma_f32_16x16x32_bf16 v[24:27], a[192:195], a[224:227], v[24:27]\n"
"v_mfma_f32_16x16x32_bf16 v[32:35], a[160:163], a[216:219], v[32:35]\n"
"ds_read_b128 a[44:47], v236 offset:44032\n"
"v_mfma_f32_16x16x32_bf16 v[32:35], a[196:199], a[224:227], v[32:35]\n"
"buffer_load_dwordx4 v233, s[56:59], s90 offen offset:1536 sc1 lds\n"
"v_mfma_f32_16x16x32_bf16 v[36:39], a[160:163], a[220:223], v[36:39]\n"
"ds_read_b128 a[12:15], v236 offset:45056\n"
"v_mfma_f32_16x16x32_bf16 v[36:39], a[196:199], a[228:231], v[36:39]\n"
"ds_read_b128 a[48:51], v236 offset:46080\n"
"v_mfma_f32_16x16x32_bf16 v[44:47], a[164:167], a[220:223], v[44:47]\n"
"ds_read_b128 a[16:19], v236 offset:47104\n"
"v_mfma_f32_16x16x32_bf16 v[44:47], a[200:203], a[228:231], v[44:47]\n"
"ds_read_b128 a[52:55], v236 offset:48128\n"
"v_mfma_f32_16x16x32_bf16 v[40:43], a[164:167], a[216:219], v[40:43]\n"
"ds_read_b128 a[20:23], v236 offset:49152\n"
"v_mfma_f32_16x16x32_bf16 v[40:43], a[200:203], a[224:227], v[40:43]\n"
"ds_read_b128 a[56:59], v236 offset:50176\n"
"v_mfma_f32_16x16x32_bf16 v[48:51], a[168:171], a[216:219], v[48:51]\n"
"buffer_load_dwordx4 v233, s[56:59], s90 offen offset:2560 sc1 lds\n"
"v_mfma_f32_16x16x32_bf16 v[48:51], a[204:207], a[224:227], v[48:51]\n"
"ds_read_b128 a[24:27], v236 offset:51200\n"
"v_mfma_f32_16x16x32_bf16 v[52:55], a[168:171], a[220:223], v[52:55]\n"
"ds_read_b128 a[60:63], v236 offset:52224\n"
"v_mfma_f32_16x16x32_bf16 v[52:55], a[204:207], a[228:231], v[52:55]\n"
"ds_read_b128 a[28:31], v236 offset:53248\n"
"v_mfma_f32_16x16x32_bf16 v[60:63], a[172:175], a[220:223], v[60:63]\n"
"ds_read_b128 a[64:67], v236 offset:54272\n"
"v_mfma_f32_16x16x32_bf16 v[60:63], a[208:211], a[228:231], v[60:63]\n"
"ds_read_b128 a[32:35], v236 offset:55296\n"
"v_mfma_f32_16x16x32_bf16 v[56:59], a[172:175], a[216:219], v[56:59]\n"
"v_mfma_f32_16x16x32_bf16 v[56:59], a[208:211], a[224:227], v[56:59]\n"
"buffer_load_dwordx4 v233, s[56:59], s90 offen offset:3584 sc1 lds\n"
"v_mfma_f32_16x16x32_bf16 v[64:67], a[176:179], a[216:219], v[64:67]\n"
"ds_read_b128 a[68:71], v236 offset:56320\n"
"v_mfma_f32_16x16x32_bf16 v[64:67], a[212:215], a[224:227], v[64:67]\n"
"v_mfma_f32_16x16x32_bf16 v[68:71], a[176:179], a[220:223], v[68:71]\n"
"v_mfma_f32_16x16x32_bf16 v[68:71], a[212:215], a[228:231], v[68:71]\n"
"s_waitcnt vmcnt(14) lgkmcnt(0)\n"
"s_barrier\n"
"v_mfma_f32_16x16x32_bf16 v[0:3], a[0:3], a[72:75], v[0:3]\n"
"s_add_u32 s90, s90, 0x12000\n"
"s_add_u32 m0, s88, 0x11000\n"
"ds_read_b64_tr_b16 a[216:217], v238 offset:18432\n"
"v_mfma_f32_16x16x32_bf16 v[0:3], a[36:39], a[80:83], v[0:3]\n"
"s_mov_b32 exec_hi, 0\n"
"buffer_load_dwordx4 v233, s[56:59], s90 offen sc1 lds\n"
"s_mov_b32 exec_hi, -1\n"
"v_mfma_f32_16x16x32_bf16 v[4:7], a[0:3], a[76:79], v[4:7]\n"
"ds_read_b64_tr_b16 a[218:219], v238 offset:19456\n"
"v_mfma_f32_16x16x32_bf16 v[4:7], a[36:39], a[84:87], v[4:7]\n"
"ds_read_b64_tr_b16 a[224:225], v238 offset:26624\n"
"v_mfma_f32_16x16x32_bf16 v[12:15], a[4:7], a[76:79], v[12:15]\n"
"ds_read_b64_tr_b16 a[226:227], v238 offset:27648\n"
"v_mfma_f32_16x16x32_bf16 v[12:15], a[40:43], a[84:87], v[12:15]\n"
"buffer_load_dwordx4 v233, s[56:59], s90 offen offset:512 sc1 lds\n"
"v_mfma_f32_16x16x32_bf16 v[8:11], a[4:7], a[72:75], v[8:11]\n"
"ds_read_b64_tr_b16 a[220:221], v239 offset:18432\n"
"v_mfma_f32_16x16x32_bf16 v[8:11], a[40:43], a[80:83], v[8:11]\n"
"ds_read_b64_tr_b16 a[222:223], v239 offset:19456\n"
"v_mfma_f32_16x16x32_bf16 v[16:19], a[8:11], a[72:75], v[16:19]\n"
"ds_read_b64_tr_b16 a[228:229], v239 offset:26624\n"
"v_mfma_f32_16x16x32_bf16 v[16:19], a[44:47], a[80:83], v[16:19]\n"
"buffer_load_dwordx4 v233, s[56:59], s90 offen offset:1536 sc1 lds\n"
"v_mfma_f32_16x16x32_bf16 v[20:23], a[8:11], a[76:79], v[20:23]\n"
"ds_read_b64_tr_b16 a[230:231], v239 offset:27648\n"
"v_mfma_f32_16x16x32_bf16 v[20:23], a[44:47], a[84:87], v[20:23]\n"
"ds_read_b128 a[144:147], v235\n"
"v_mfma_f32_16x16x32_bf16 v[28:31], a[12:15], a[76:79], v[28:31]\n"
"ds_read_b128 a[180:183], v235 offset:1024\n"
"v_mfma_f32_16x16x32_bf16 v[28:31], a[48:51], a[84:87], v[28:31]\n"
"buffer_load_dwordx4 v233, s[56:59], s90 offen offset:2560 sc1 lds\n"
"v_mfma_f32_16x16x32_bf16 v[24:27], a[12:15], a[72:75], v[24:27]\n"
"ds_read_b128 a[148:151], v235 offset:2048\n"
"v_mfma_f32_16x16x32_bf16 v[24:27], a[48:51], a[80:83], v[24:27]\n"
"ds_read_b128 a[184:187], v235 offset:3072\n"
"v_mfma_f32_16x16x32_bf16 v[32:35], a[16:19], a[72:75], v[32:35]\n"
"ds_read_b128 a[152:155], v235 offset:4096\n"
"v_mfma_f32_16x16x32_bf16 v[32:35], a[52:55], a[80:83], v[32:35]\n"
"buffer_load_dwordx4 v233, s[56:59], s90 offen offset:3584 sc1 lds\n"
"v_mfma_f32_16x16x32_bf16 v[36:39], a[16:19], a[76:79], v[36:39]\n"
"s_mov_b32 s91, 0\n"
"ds_read_b128 a[188:191], v235 offset:5120\n"
"v_mfma_f32_16x16x32_bf16 v[36:39], a[52:55], a[84:87], v[36:39]\n"
"ds_read_b128 a[156:159], v235 offset:6144\n"
"v_mfma_f32_16x16x32_bf16 v[44:47], a[20:23], a[76:79], v[44:47]\n"
"ds_read_b128 a[192:195], v235 offset:7168\n"
"v_mfma_f32_16x16x32_bf16 v[44:47], a[56:59], a[84:87], v[44:47]\n"
"buffer_load_dwordx4 a[88:91], v226, s[56:59], s91 offen sc1\n"
"v_mfma_f32_16x16x32_bf16 v[40:43], a[20:23], a[72:75], v[40:43]\n"
"ds_read_b128 a[160:163], v235 offset:8192\n"
"v_mfma_f32_16x16x32_bf16 v[40:43], a[56:59], a[80:83], v[40:43]\n"
"ds_read_b128 a[196:199], v235 offset:9216\n"
"v_mfma_f32_16x16x32_bf16 v[48:51], a[24:27], a[72:75], v[48:51]\n"
"ds_read_b128 a[164:167], v235 offset:10240\n"
"v_mfma_f32_16x16x32_bf16 v[48:51], a[60:63], a[80:83], v[48:51]\n"
"buffer_load_dwordx4 a[96:99], v226, s[56:59], s91 offen offset:1024 sc1\n"
"v_mfma_f32_16x16x32_bf16 v[52:55], a[24:27], a[76:79], v[52:55]\n"
"ds_read_b128 a[200:203], v235 offset:11264\n"
"v_mfma_f32_16x16x32_bf16 v[52:55], a[60:63], a[84:87], v[52:55]\n"
"ds_read_b128 a[168:171], v235 offset:12288\n"
"v_mfma_f32_16x16x32_bf16 v[60:63], a[28:31], a[76:79], v[60:63]\n"
"ds_read_b128 a[204:207], v235 offset:13312\n"
"v_mfma_f32_16x16x32_bf16 v[60:63], a[64:67], a[84:87], v[60:63]\n"
"buffer_load_dwordx4 a[92:95], v227, s[56:59], s91 offen sc1\n"
"v_mfma_f32_16x16x32_bf16 v[56:59], a[28:31], a[72:75], v[56:59]\n"
"ds_read_b128 a[172:175], v235 offset:14336\n"
"ds_read_b128 a[208:211], v235 offset:15360\n"
"v_mfma_f32_16x16x32_bf16 v[56:59], a[64:67], a[80:83], v[56:59]\n"
"ds_read_b128 a[176:179], v235 offset:16384\n"
"ds_read_b128 a[212:215], v235 offset:17408\n"
"v_mfma_f32_16x16x32_bf16 v[64:67], a[32:35], a[72:75], v[64:67]\n"
"v_mfma_f32_16x16x32_bf16 v[64:67], a[68:71], a[80:83], v[64:67]\n"
"buffer_load_dwordx4 a[100:103], v227, s[56:59], s91 offen offset:1024 sc1\n"
"v_mfma_f32_16x16x32_bf16 v[68:71], a[32:35], a[76:79], v[68:71]\n"
"v_mfma_f32_16x16x32_bf16 v[68:71], a[68:71], a[84:87], v[68:71]\n"
"s_waitcnt vmcnt(14) lgkmcnt(0)\n"
"s_barrier\n"
"v_mfma_f32_16x16x32_bf16 v[0:3], a[144:147], a[216:219], v[0:3]\n"
"s_add_u32 s90, s90, 0x12000\n"
"s_add_u32 m0, s88, 0x19800\n"
"ds_read_b64_tr_b16 a[72:73], v238 offset:53248\n"
"v_mfma_f32_16x16x32_bf16 v[0:3], a[180:183], a[224:227], v[0:3]\n"
"s_mov_b32 exec_hi, 0\n"
"buffer_load_dwordx4 v233, s[56:59], s90 offen sc1 lds\n"
"s_mov_b32 exec_hi, -1\n"
"v_mfma_f32_16x16x32_bf16 v[4:7], a[144:147], a[220:223], v[4:7]\n"
"ds_read_b64_tr_b16 a[74:75], v238 offset:54272\n"
"v_mfma_f32_16x16x32_bf16 v[4:7], a[180:183], a[228:231], v[4:7]\n"
"ds_read_b64_tr_b16 a[80:81], v238 offset:61440\n"
"v_mfma_f32_16x16x32_bf16 v[12:15], a[148:151], a[220:223], v[12:15]\n"
"ds_read_b64_tr_b16 a[82:83], v238 offset:62464\n"
"v_mfma_f32_16x16x32_bf16 v[12:15], a[184:187], a[228:231], v[12:15]\n"
"buffer_load_dwordx4 v233, s[56:59], s90 offen offset:512 sc1 lds\n"
"v_mfma_f32_16x16x32_bf16 v[8:11], a[148:151], a[216:219], v[8:11]\n"
"ds_read_b64_tr_b16 a[76:77], v239 offset:53248\n"
"v_mfma_f32_16x16x32_bf16 v[8:11], a[184:187], a[224:227], v[8:11]\n"
"ds_read_b64_tr_b16 a[78:79], v239 offset:54272\n"
"v_mfma_f32_16x16x32_bf16 v[16:19], a[152:155], a[216:219], v[16:19]\n"
"ds_read_b64_tr_b16 a[84:85], v239 offset:61440\n"
"v_mfma_f32_16x16x32_bf16 v[16:19], a[188:191], a[224:227], v[16:19]\n"
"buffer_load_dwordx4 v233, s[56:59], s90 offen offset:1536 sc1 lds\n"
"v_mfma_f32_16x16x32_bf16 v[20:23], a[152:155], a[220:223], v[20:23]\n"
"ds_read_b64_tr_b16 a[86:87], v239 offset:62464\n"
"v_mfma_f32_16x16x32_bf16 v[20:23], a[188:191], a[228:231], v[20:23]\n"
"ds_read_b128 a[0:3], v235 offset:34816\n"
"v_mfma_f32_16x16x32_bf16 v[28:31], a[156:159], a[220:223], v[28:31]\n"
"ds_read_b128 a[36:39], v235 offset:35840\n"
"v_mfma_f32_16x16x32_bf16 v[28:31], a[192:195], a[228:231], v[28:31]\n"
"buffer_load_dwordx4 v233, s[56:59], s90 offen offset:2560 sc1 lds\n"
"v_mfma_f32_16x16x32_bf16 v[24:27], a[156:159], a[216:219], v[24:27]\n"
"ds_read_b128 a[4:7], v235 offset:36864\n"
"v_mfma_f32_16x16x32_bf16 v[24:27], a[192:195], a[224:227], v[24:27]\n"
"ds_read_b128 a[40:43], v235 offset:37888\n"
"v_mfma_f32_16x16x32_bf16 v[32:35], a[160:163], a[216:219], v[32:35]\n"
"ds_read_b128 a[8:11], v235 offset:38912\n"
"v_mfma_f32_16x16x32_bf16 v[32:35], a[196:199], a[224:227], v[32:35]\n"
"buffer_load_dwordx4 v233, s[56:59], s90 offen offset:3584 sc1 lds\n"
"v_mfma_f32_16x16x32_bf16 v[36:39], a[160:163], a[220:223], v[36:39]\n"
"s_add_u32 s91, s91, 0x800\n"
"ds_read_b128 a[44:47], v235 offset:39936\n"
"v_mfma_f32_16x16x32_bf16 v[36:39], a[196:199], a[228:231], v[36:39]\n"
"ds_read_b128 a[12:15], v235 offset:40960\n"
"v_mfma_f32_16x16x32_bf16 v[44:47], a[164:167], a[220:223], v[44:47]\n"
"ds_read_b128 a[48:51], v235 offset:41984\n"
"v_mfma_f32_16x16x32_bf16 v[44:47], a[200:203], a[228:231], v[44:47]\n"
"buffer_load_dwordx4 a[104:107], v226, s[56:59], s91 offen sc1\n"
"v_mfma_f32_16x16x32_bf16 v[40:43], a[164:167], a[216:219], v[40:43]\n"
"ds_read_b128 a[16:19], v235 offset:43008\n"
"v_mfma_f32_16x16x32_bf16 v[40:43], a[200:203], a[224:227], v[40:43]\n"
"ds_read_b128 a[52:55], v235 offset:44032\n"
"v_mfma_f32_16x16x32_bf16 v[48:51], a[168:171], a[216:219], v[48:51]\n"
"ds_read_b128 a[20:23], v235 offset:45056\n"
"v_mfma_f32_16x16x32_bf16 v[48:51], a[204:207], a[224:227], v[48:51]\n"
"buffer_load_dwordx4 a[112:115], v226, s[56:59], s91 offen offset:1024 sc1\n"
"v_mfma_f32_16x16x32_bf16 v[52:55], a[168:171], a[220:223], v[52:55]\n"
"ds_read_b128 a[56:59], v235 offset:46080\n"
"v_mfma_f32_16x16x32_bf16 v[52:55], a[204:207], a[228:231], v[52:55]\n"
"ds_read_b128 a[24:27], v235 offset:47104\n"
"v_mfma_f32_16x16x32_bf16 v[60:63], a[172:175], a[220:223], v[60:63]\n"
"ds_read_b128 a[60:63], v235 offset:48128\n"
"v_mfma_f32_16x16x32_bf16 v[60:63], a[208:211], a[228:231], v[60:63]\n"
"buffer_load_dwordx4 a[108:111], v227, s[56:59], s91 offen sc1\n"
"v_mfma_f32_16x16x32_bf16 v[56:59], a[172:175], a[216:219], v[56:59]\n"
"ds_read_b128 a[28:31], v235 offset:49152\n"
"ds_read_b128 a[64:67], v235 offset:50176\n"
"v_mfma_f32_16x16x32_bf16 v[56:59], a[208:211], a[224:227], v[56:59]\n"
"ds_read_b128 a[32:35], v235 offset:51200\n"
"ds_read_b128 a[68:71], v235 offset:52224\n"
"v_mfma_f32_16x16x32_bf16 v[64:67], a[176:179], a[216:219], v[64:67]\n"
"v_mfma_f32_16x16x32_bf16 v[64:67], a[212:215], a[224:227], v[64:67]\n"
"buffer_load_dwordx4 a[116:119], v227, s[56:59], s91 offen offset:1024 sc1\n"
"v_mfma_f32_16x16x32_bf16 v[68:71], a[176:179], a[220:223], v[68:71]\n"
"v_mfma_f32_16x16x32_bf16 v[68:71], a[212:215], a[228:231], v[68:71]\n"
"s_waitcnt vmcnt(18) lgkmcnt(0)\n"
"s_barrier\n"
"v_mfma_f32_16x16x32_bf16 v[0:3], a[0:3], a[72:75], v[0:3]\n"
"s_add_u32 s90, s90, 0x12000\n"
"s_add_u32 m0, s88, 0\n"
"ds_read_b128 a[144:147], v237 offset:8192\n"
"v_mfma_f32_16x16x32_bf16 v[0:3], a[36:39], a[80:83], v[0:3]\n"
"s_mov_b32 exec_hi, 0\n"
"buffer_load_dwordx4 v233, s[56:59], s90 offen sc1 lds\n"
"s_mov_b32 exec_hi, -1\n"
"v_mfma_f32_16x16x32_bf16 v[4:7], a[0:3], a[76:79], v[4:7]\n"
"ds_read_b128 a[180:183], v237 offset:9216\n"
"v_mfma_f32_16x16x32_bf16 v[4:7], a[36:39], a[84:87], v[4:7]\n"
"ds_read_b128 a[148:151], v237 offset:10240\n"
"v_mfma_f32_16x16x32_bf16 v[12:15], a[4:7], a[76:79], v[12:15]\n"
"v_mfma_f32_16x16x32_bf16 v[12:15], a[40:43], a[84:87], v[12:15]\n"
"buffer_load_dwordx4 v233, s[56:59], s90 offen offset:512 sc1 lds\n"
"v_mfma_f32_16x16x32_bf16 v[8:11], a[4:7], a[72:75], v[8:11]\n"
"ds_read_b128 a[184:187], v237 offset:11264\n"
"v_mfma_f32_16x16x32_bf16 v[8:11], a[40:43], a[80:83], v[8:11]\n"
"ds_read_b128 a[152:155], v237 offset:12288\n"
"v_mfma_f32_16x16x32_bf16 v[16:19], a[8:11], a[72:75], v[16:19]\n"
"v_mfma_f32_16x16x32_bf16 v[16:19], a[44:47], a[80:83], v[16:19]\n"
"buffer_load_dwordx4 v233, s[56:59], s90 offen offset:1536 sc1 lds\n"
"v_mfma_f32_16x16x32_bf16 v[20:23], a[8:11], a[76:79], v[20:23]\n"
"ds_read_b128 a[188:191], v237 offset:13312\n"
"v_mfma_f32_16x16x32_bf16 v[20:23], a[44:47], a[84:87], v[20:23]\n"
"ds_read_b128 a[156:159], v237 offset:14336\n"
"v_mfma_f32_16x16x32_bf16 v[28:31], a[12:15], a[76:79], v[28:31]\n"
"ds_read_b128 a[192:195], v237 offset:15360\n"
"v_mfma_f32_16x16x32_bf16 v[28:31], a[48:51], a[84:87], v[28:31]\n"
"buffer_load_dwordx4 v233, s[56:59], s90 offen offset:2560 sc1 lds\n"
"v_mfma_f32_16x16x32_bf16 v[24:27], a[12:15], a[72:75], v[24:27]\n"
"ds_read_b128 a[160:163], v237 offset:16384\n"
"v_mfma_f32_16x16x32_bf16 v[24:27], a[48:51], a[80:83], v[24:27]\n"
"v_mfma_f32_16x16x32_bf16 v[32:35], a[16:19], a[72:75], v[32:35]\n"
"ds_read_b128 a[196:199], v237 offset:17408\n"
"v_mfma_f32_16x16x32_bf16 v[32:35], a[52:55], a[80:83], v[32:35]\n"
"buffer_load_dwordx4 v233, s[56:59], s90 offen offset:3584 sc1 lds\n"
"v_mfma_f32_16x16x32_bf16 v[36:39], a[16:19], a[76:79], v[36:39]\n"
"s_add_u32 s91, s91, 0x800\n"
"ds_read_b128 a[164:167], v237 offset:18432\n"
"v_mfma_f32_16x16x32_bf16 v[36:39], a[52:55], a[84:87], v[36:39]\n"
"ds_read_b128 a[200:203], v237 offset:19456\n"
"v_mfma_f32_16x16x32_bf16 v[44:47], a[20:23], a[76:79], v[44:47]\n"
"v_mfma_f32_16x16x32_bf16 v[44:47], a[56:59], a[84:87], v[44:47]\n"
"buffer_load_dwordx4 a[120:123], v226, s[56:59], s91 offen sc1\n"
"v_mfma_f32_16x16x32_bf16 v[40:43], a[20:23], a[72:75], v[40:43]\n"
"ds_read_b128 a[168:171], v237 offset:20480\n"
"v_mfma_f32_16x16x32_bf16 v[40:43], a[56:59], a[80:83], v[40:43]\n"
"ds_read_b128 a[204:207], v237 offset:21504\n"
"v_mfma_f32_16x16x32_bf16 v[48:51], a[24:27], a[72:75], v[48:51]\n"
"v_mfma_f32_16x16x32_bf16 v[48:51], a[60:63], a[80:83], v[48:51]\n"
"buffer_load_dwordx4 a[128:131], v226, s[56:59], s91 offen offset:1024 sc1\n"
"v_mfma_f32_16x16x32_bf16 v[52:55], a[24:27], a[76:79], v[52:55]\n"
"ds_read_b128 a[172:175], v237 offset:22528\n"
"v_mfma_f32_16x16x32_bf16 v[52:55], a[60:63], a[84:87], v[52:55]\n"
"ds_read_b128 a[208:211], v237 offset:23552\n"
"v_mfma_f32_16x16x32_bf16 v[60:63], a[28:31], a[76:79], v[60:63]\n"
"ds_read_b128 a[176:179], v237 offset:24576\n"
"v_mfma_f32_16x16x32_bf16 v[60:63], a[64:67], a[84:87], v[60:63]\n"
"buffer_load_dwordx4 a[124:127], v227, s[56:59], s91 offen sc1\n"
"v_mfma_f32_16x16x32_bf16 v[56:59], a[28:31], a[72:75], v[56:59]\n"
"ds_read_b128 a[212:215], v237 offset:25600\n"
"v_mfma_f32_16x16x32_bf16 v[56:59], a[64:67], a[80:83], v[56:59]\n"
"v_mfma_f32_16x16x32_bf16 v[64:67], a[32:35], a[72:75], v[64:67]\n"
"v_mfma_f32_16x16x32_bf16 v[64:67], a[68:71], a[80:83], v[64:67]\n"
"buffer_load_dwordx4 a[132:135], v227, s[56:59], s91 offen offset:1024 sc1\n"
"v_mfma_f32_16x16x32_bf16 v[68:71], a[32:35], a[76:79], v[68:71]\n"
"v_mfma_f32_16x16x32_bf16 v[68:71], a[68:71], a[84:87], v[68:71]\n"
"s_mov_b32 s94, 3\n"
"s_waitcnt vmcnt(18) lgkmcnt(0)\n"
"s_barrier\n"
"v_mfma_f32_16x16x32_bf16 v[72:75], a[144:147], a[88:91], 0\n"
"s_add_u32 s90, s90, 0x12000\n"
"s_add_u32 m0, s88, 0x22000\n"
"ds_read_b128 a[0:3], v236 offset:4096\n"
"v_mfma_f32_16x16x32_bf16 v[72:75], a[180:183], a[96:99], v[72:75]\n"
"s_mov_b32 exec_hi, 0\n"
"buffer_load_dwordx4 v233, s[56:59], s90 offen sc1 lds\n"
"s_mov_b32 exec_hi, -1\n"
"v_mfma_f32_16x16x32_bf16 v[76:79], a[144:147], a[92:95], 0\n"
"ds_read_b128 a[36:39], v236 offset:5120\n"
"v_mfma_f32_16x16x32_bf16 v[76:79], a[180:183], a[100:103], v[76:79]\n"
"ds_read_b128 a[4:7], v236 offset:6144\n"
"v_mfma_f32_16x16x32_bf16 v[84:87], a[148:151], a[92:95], 0\n"
"v_mfma_f32_16x16x32_bf16 v[84:87], a[184:187], a[100:103], v[84:87]\n"
"s_add_u32 s83, s2, 0x1c28000\n"
"buffer_load_dwordx4 v233, s[56:59], s90 offen offset:512 sc1 lds\n"
"v_mfma_f32_16x16x32_bf16 v[80:83], a[148:151], a[88:91], 0\n"
"ds_read_b128 a[40:43], v236 offset:7168\n"
"v_cvt_pk_bf16_f32 v144, v0, v1\n"
"v_mfma_f32_16x16x32_bf16 v[80:83], a[184:187], a[96:99], v[80:83]\n"
"ds_read_b128 a[8:11], v236 offset:8192\n"
"v_cvt_pk_bf16_f32 v145, v2, v3\n"
"v_cvt_pk_bf16_f32 v146, v4, v5\n"
"v_mfma_f32_16x16x32_bf16 v[88:91], a[152:155], a[88:91], 0\n"
"v_cvt_pk_bf16_f32 v147, v6, v7\n"
"buffer_store_dwordx4 v[144:147], v218, s[56:59], s83 offen sc1\n"
"v_mfma_f32_16x16x32_bf16 v[88:91], a[188:191], a[96:99], v[88:91]\n"
"buffer_load_dwordx4 v233, s[56:59], s90 offen offset:1536 sc1 lds\n"
"v_cvt_pk_bf16_f32 v148, v8, v9\n"
"v_mfma_f32_16x16x32_bf16 v[92:95], a[152:155], a[92:95], 0\n"
"ds_read_b128 a[44:47], v236 offset:9216\n"
"v_cvt_pk_bf16_f32 v149, v10, v11\n"
"v_cvt_pk_bf16_f32 v150, v12, v13\n"
"v_mfma_f32_16x16x32_bf16 v[92:95], a[188:191], a[100:103], v[92:95]\n"
"ds_read_b128 a[12:15], v236 offset:10240\n"
"v_cvt_pk_bf16_f32 v151, v14, v15\n"
"v_mfma_f32_16x16x32_bf16 v[100:103], a[156:159], a[92:95], 0\n"
"ds_read_b128 a[48:51], v236 offset:11264\n"
"buffer_store_dwordx4 v[148:151], v218, s[56:59], s83 offen offset:1024 sc1\n"
"v_mfma_f32_16x16x32_bf16 v[100:103], a[192:195], a[100:103], v[100:103]\n"
"buffer_load_dwordx4 v233, s[56:59], s90 offen offset:2560 sc1 lds\n"
"v_cvt_pk_bf16_f32 v152, v16, v17\n"
"v_mfma_f32_16x16x32_bf16 v[96:99], a[156:159], a[88:91], 0\n"
"ds_read_b128 a[16:19], v236 offset:12288\n"
"v_cvt_pk_bf16_f32 v153, v18, v19\n"
"v_cvt_pk_bf16_f32 v154, v20, v21\n"
"v_mfma_f32_16x16x32_bf16 v[96:99], a[192:195], a[96:99], v[96:99]\n"
"v_cvt_pk_bf16_f32 v155, v22, v23\n"
"buffer_store_dwordx4 v[152:155], v218, s[56:59], s83 offen offset:2048 sc1\n"
"v_mfma_f32_16x16x32_bf16 v[104:107], a[160:163], a[88:91], 0\n"
"ds_read_b128 a[52:55], v236 offset:13312\n"
"v_cvt_pk_bf16_f32 v144, v24, v25\n"
"v_cvt_pk_bf16_f32 v145, v26, v27\n"
"v_mfma_f32_16x16x32_bf16 v[104:107], a[196:199], a[96:99], v[104:107]\n"
"buffer_load_dwordx4 v233, s[56:59], s90 offen offset:3584 sc1 lds\n"
"v_cvt_pk_bf16_f32 v146, v28, v29\n"
"v_mfma_f32_16x16x32_bf16 v[108:111], a[160:163], a[92:95], 0\n"
"s_add_u32 s91, s91, 0x800\n"
"ds_read_b128 a[20:23], v236 offset:14336\n"
"v_cvt_pk_bf16_f32 v147, v30, v31\n"
"v_mfma_f32_16x16x32_bf16 v[108:111], a[196:199], a[100:103], v[108:111]\n"
"buffer_store_dwordx4 v[144:147], v218, s[56:59], s83 offen offset:3072 sc1\n"
"v_cvt_pk_bf16_f32 v148, v32, v33\n"
"v_mfma_f32_16x16x32_bf16 v[116:119], a[164:167], a[92:95], 0\n"
"ds_read_b128 a[56:59], v236 offset:15360\n"
"v_cvt_pk_bf16_f32 v149, v34, v35\n"
"v_cvt_pk_bf16_f32 v150, v36, v37\n"
"v_mfma_f32_16x16x32_bf16 v[116:119], a[200:203], a[100:103], v[116:119]\n"
"buffer_load_dwordx4 a[232:235], v226, s[56:59], s91 offen sc1\n"
"v_cvt_pk_bf16_f32 v151, v38, v39\n"
"v_mfma_f32_16x16x32_bf16 v[112:115], a[164:167], a[88:91], 0\n"
"s_add_u32 s83, s83, 0x1000\n"
"ds_read_b128 a[24:27], v236 offset:16384\n"
"buffer_store_dwordx4 v[148:151], v218, s[56:59], s83 offen sc1\n"
"v_mfma_f32_16x16x32_bf16 v[112:115], a[200:203], a[96:99], v[112:115]\n"
"v_cvt_pk_bf16_f32 v152, v40, v41\n"
"v_cvt_pk_bf16_f32 v153, v42, v43\n"
"v_cvt_pk_bf16_f32 v154, v44, v45\n"
"v_mfma_f32_16x16x32_bf16 v[120:123], a[168:171], a[88:91], 0\n"
"ds_read_b128 a[60:63], v236 offset:17408\n"
"v_cvt_pk_bf16_f32 v155, v46, v47\n"
"v_mfma_f32_16x16x32_bf16 v[120:123], a[204:207], a[96:99], v[120:123]\n"
"buffer_load_dwordx4 a[240:243], v226, s[56:59], s91 offen offset:1024 sc1\n"
"v_mfma_f32_16x16x32_bf16 v[124:127], a[168:171], a[92:95], 0\n"
"buffer_store_dwordx4 v[152:155], v218, s[56:59], s83 offen offset:1024 sc1\n"
"ds_read_b128 a[28:31], v236 offset:18432\n"
"v_mfma_f32_16x16x32_bf16 v[124:127], a[204:207], a[100:103], v[124:127]\n"
"v_cvt_pk_bf16_f32 v144, v48, v49\n"
"v_cvt_pk_bf16_f32 v145, v50, v51\n"
"v_cvt_pk_bf16_f32 v146, v52, v53\n"
"v_mfma_f32_16x16x32_bf16 v[132:135], a[172:175], a[92:95], 0\n"
"ds_read_b128 a[64:67], v236 offset:19456\n"
"v_cvt_pk_bf16_f32 v147, v54, v55\n"
"v_mfma_f32_16x16x32_bf16 v[132:135], a[208:211], a[100:103], v[132:135]\n"
"buffer_load_dwordx4 a[236:239], v227, s[56:59], s91 offen sc1\n"
"ds_read_b128 a[32:35], v236 offset:20480\n"
"v_mfma_f32_16x16x32_bf16 v[128:131], a[172:175], a[88:91], 0\n"
"buffer_store_dwordx4 v[144:147], v218, s[56:59], s83 offen offset:2048 sc1\n"
"v_cvt_pk_bf16_f32 v148, v56, v57\n"
"v_mfma_f32_16x16x32_bf16 v[128:131], a[208:211], a[96:99], v[128:131]\n"
"ds_read_b128 a[68:71], v236 offset:21504\n"
"v_cvt_pk_bf16_f32 v149, v58, v59\n"
"v_cvt_pk_bf16_f32 v150, v60, v61\n"
"v_mfma_f32_16x16x32_bf16 v[136:139], a[176:179], a[88:91], 0\n"
"v_cvt_pk_bf16_f32 v151, v62, v63\n"
"buffer_store_dwordx4 v[148:151], v218, s[56:59], s83 offen offset:3072 sc1\n"
"v_mfma_f32_16x16x32_bf16 v[136:139], a[212:215], a[96:99], v[136:139]\n"
"buffer_load_dwordx4 a[244:247], v227, s[56:59], s91 offen offset:1024 sc1\n"
"v_cvt_pk_bf16_f32 v152, v64, v65\n"
"v_mfma_f32_16x16x32_bf16 v[140:143], a[176:179], a[92:95], 0\n"
"v_cvt_pk_bf16_f32 v153, v66, v67\n"
"v_cvt_pk_bf16_f32 v154, v68, v69\n"
"s_add_u32 s83, s83, 0x1000\n"
"v_cvt_pk_bf16_f32 v155, v70, v71\n"
"buffer_store_dwordx4 v[152:155], v218, s[56:59], s83 offen sc1\n"
"v_mfma_f32_16x16x32_bf16 v[140:143], a[212:215], a[100:103], v[140:143]\n"
"s_waitcnt vmcnt(27) lgkmcnt(0)\n"
"s_barrier\n"
"L_a3:\n"
"v_mfma_f32_16x16x32_bf16 v[72:75], a[0:3], a[104:107], v[72:75]\n"
"s_add_u32 s90, s90, 0x12000\n"
"s_add_u32 m0, s88, 0x11000\n"
"ds_read_b128 a[144:147], v236 offset:38912\n"
"v_mfma_f32_16x16x32_bf16 v[72:75], a[36:39], a[112:115], v[72:75]\n"
"s_mov_b32 exec_hi, 0\n"
"buffer_load_dwordx4 v233, s[56:59], s90 offen sc1 lds\n"
"s_mov_b32 exec_hi, -1\n"
"v_mfma_f32_16x16x32_bf16 v[76:79], a[0:3], a[108:111], v[76:79]\n"
"ds_read_b128 a[180:183], v236 offset:39936\n"
"v_mfma_f32_16x16x32_bf16 v[76:79], a[36:39], a[116:119], v[76:79]\n"
"ds_read_b128 a[148:151], v236 offset:40960\n"
"v_mfma_f32_16x16x32_bf16 v[84:87], a[4:7], a[108:111], v[84:87]\n"
"v_mfma_f32_16x16x32_bf16 v[84:87], a[40:43], a[116:119], v[84:87]\n"
"buffer_load_dwordx4 v233, s[56:59], s90 offen offset:512 sc1 lds\n"
"v_mfma_f32_16x16x32_bf16 v[80:83], a[4:7], a[104:107], v[80:83]\n"
"ds_read_b128 a[184:187], v236 offset:41984\n"
"v_mfma_f32_16x16x32_bf16 v[80:83], a[40:43], a[112:115], v[80:83]\n"
"ds_read_b128 a[152:155], v236 offset:43008\n"
"v_mfma_f32_16x16x32_bf16 v[88:91], a[8:11], a[104:107], v[88:91]\n"
"v_mfma_f32_16x16x32_bf16 v[88:91], a[44:47], a[112:115], v[88:91]\n"
"buffer_load_dwordx4 v233, s[56:59], s90 offen offset:1536 sc1 lds\n"
"v_mfma_f32_16x16x32_bf16 v[92:95], a[8:11], a[108:111], v[92:95]\n"
"ds_read_b128 a[188:191], v236 offset:44032\n"
"v_mfma_f32_16x16x32_bf16 v[92:95], a[44:47], a[116:119], v[92:95]\n"
"ds_read_b128 a[156:159], v236 offset:45056\n"
"v_mfma_f32_16x16x32_bf16 v[100:103], a[12:15], a[108:111], v[100:103]\n"
"ds_read_b128 a[192:195], v236 offset:46080\n"
"v_mfma_f32_16x16x32_bf16 v[100:103], a[48:51], a[116:119], v[100:103]\n"
"buffer_load_dwordx4 v233, s[56:59], s90 offen offset:2560 sc1 lds\n"
"v_mfma_f32_16x16x32_bf16 v[96:99], a[12:15], a[104:107], v[96:99]\n"
"ds_read_b128 a[160:163], v236 offset:47104\n"
"v_mfma_f32_16x16x32_bf16 v[96:99], a[48:51], a[112:115], v[96:99]\n"
"v_mfma_f32_16x16x32_bf16 v[104:107], a[16:19], a[104:107], v[104:107]\n"
"ds_read_b128 a[196:199], v236 offset:48128\n"
"v_mfma_f32_16x16x32_bf16 v[104:107], a[52:55], a[112:115], v[104:107]\n"
"buffer_load_dwordx4 v233, s[56:59], s90 offen offset:3584 sc1 lds\n"
"v_mfma_f32_16x16x32_bf16 v[108:111], a[16:19], a[108:111], v[108:111]\n"
"s_add_u32 s91, s91, 0x800\n"
"ds_read_b128 a[164:167], v236 offset:49152\n"
"v_mfma_f32_16x16x32_bf16 v[108:111], a[52:55], a[116:119], v[108:111]\n"
"ds_read_b128 a[200:203], v236 offset:50176\n"
"v_mfma_f32_16x16x32_bf16 v[116:119], a[20:23], a[108:111], v[116:119]\n"
"v_mfma_f32_16x16x32_bf16 v[116:119], a[56:59], a[116:119], v[116:119]\n"
"buffer_load_dwordx4 a[88:91], v226, s[56:59], s91 offen sc1\n"
"v_mfma_f32_16x16x32_bf16 v[112:115], a[20:23], a[104:107], v[112:115]\n"
"ds_read_b128 a[168:171], v236 offset:51200\n"
"v_mfma_f32_16x16x32_bf16 v[112:115], a[56:59], a[112:115], v[112:115]\n"
"ds_read_b128 a[204:207], v236 offset:52224\n"
"v_mfma_f32_16x16x32_bf16 v[120:123], a[24:27], a[104:107], v[120:123]\n"
"v_mfma_f32_16x16x32_bf16 v[120:123], a[60:63], a[112:115], v[120:123]\n"
"buffer_load_dwordx4 a[96:99], v226, s[56:59], s91 offen offset:1024 sc1\n"
"v_mfma_f32_16x16x32_bf16 v[124:127], a[24:27], a[108:111], v[124:127]\n"
"ds_read_b128 a[172:175], v236 offset:53248\n"
"v_mfma_f32_16x16x32_bf16 v[124:127], a[60:63], a[116:119], v[124:127]\n"
"ds_read_b128 a[208:211], v236 offset:54272\n"
"v_mfma_f32_16x16x32_bf16 v[132:135], a[28:31], a[108:111], v[132:135]\n"
"ds_read_b128 a[176:179], v236 offset:55296\n"
"v_mfma_f32_16x16x32_bf16 v[132:135], a[64:67], a[116:119], v[132:135]\n"
"buffer_load_dwordx4 a[92:95], v227, s[56:59], s91 offen sc1\n"
"v_mfma_f32_16x16x32_bf16 v[128:131], a[28:31], a[104:107], v[128:131]\n"
"ds_read_b128 a[212:215], v236 offset:56320\n"
"v_mfma_f32_16x16x32_bf16 v[128:131], a[64:67], a[112:115], v[128:131]\n"
"v_mfma_f32_16x16x32_bf16 v[136:139], a[32:35], a[104:107], v[136:139]\n"
"v_mfma_f32_16x16x32_bf16 v[136:139], a[68:71], a[112:115], v[136:139]\n"
"buffer_load_dwordx4 a[100:103], v227, s[56:59], s91 offen offset:1024 sc1\n"
"v_mfma_f32_16x16x32_bf16 v[140:143], a[32:35], a[108:111], v[140:143]\n"
"v_mfma_f32_16x16x32_bf16 v[140:143], a[68:71], a[116:119], v[140:143]\n"
"s_waitcnt vmcnt(20) lgkmcnt(0)\n"
"s_barrier\n"
"v_mfma_f32_16x16x32_bf16 v[72:75], a[144:147], a[120:123], v[72:75]\n"
"s_add_u32 s90, s90, 0x12000\n"
"s_add_u32 m0, s88, 0x19800\n"
"ds_read_b128 a[0:3], v235\n"
"v_mfma_f32_16x16x32_bf16 v[72:75], a[180:183], a[128:131], v[72:75]\n"
"s_mov_b32 exec_hi, 0\n"
"buffer_load_dwordx4 v233, s[56:59], s90 offen sc1 lds\n"
"s_mov_b32 exec_hi, -1\n"
"v_mfma_f32_16x16x32_bf16 v[76:79], a[144:147], a[124:127], v[76:79]\n"
"ds_read_b128 a[36:39], v235 offset:1024\n"
"v_mfma_f32_16x16x32_bf16 v[76:79], a[180:183], a[132:135], v[76:79]\n"
"ds_read_b128 a[4:7], v235 offset:2048\n"
"v_mfma_f32_16x16x32_bf16 v[84:87], a[148:151], a[124:127], v[84:87]\n"
"v_mfma_f32_16x16x32_bf16 v[84:87], a[184:187], a[132:135], v[84:87]\n"
"buffer_load_dwordx4 v233, s[56:59], s90 offen offset:512 sc1 lds\n"
"v_mfma_f32_16x16x32_bf16 v[80:83], a[148:151], a[120:123], v[80:83]\n"
"ds_read_b128 a[40:43], v235 offset:3072\n"
"v_mfma_f32_16x16x32_bf16 v[80:83], a[184:187], a[128:131], v[80:83]\n"
"ds_read_b128 a[8:11], v235 offset:4096\n"
"v_mfma_f32_16x16x32_bf16 v[88:91], a[152:155], a[120:123], v[88:91]\n"
"v_mfma_f32_16x16x32_bf16 v[88:91], a[188:191], a[128:131], v[88:91]\n"
"buffer_load_dwordx4 v233, s[56:59], s90 offen offset:1536 sc1 lds\n"
"v_mfma_f32_16x16x32_bf16 v[92:95], a[152:155], a[124:127], v[92:95]\n"
"ds_read_b128 a[44:47], v235 offset:5120\n"
"v_mfma_f32_16x16x32_bf16 v[92:95], a[188:191], a[132:135], v[92:95]\n"
"ds_read_b128 a[12:15], v235 offset:6144\n"
"v_mfma_f32_16x16x32_bf16 v[100:103], a[156:159], a[124:127], v[100:103]\n"
"ds_read_b128 a[48:51], v235 offset:7168\n"
"v_mfma_f32_16x16x32_bf16 v[100:103], a[192:195], a[132:135], v[100:103]\n"
"buffer_load_dwordx4 v233, s[56:59], s90 offen offset:2560 sc1 lds\n"
"v_mfma_f32_16x16x32_bf16 v[96:99], a[156:159], a[120:123], v[96:99]\n"
"ds_read_b128 a[16:19], v235 offset:8192\n"
"v_mfma_f32_16x16x32_bf16 v[96:99], a[192:195], a[128:131], v[96:99]\n"
"v_mfma_f32_16x16x32_bf16 v[104:107], a[160:163], a[120:123], v[104:107]\n"
"ds_read_b128 a[52:55], v235 offset:9216\n"
"v_mfma_f32_16x16x32_bf16 v[104:107], a[196:199], a[128:131], v[104:107]\n"
"buffer_load_dwordx4 v233, s[56:59], s90 offen offset:3584 sc1 lds\n"
"v_mfma_f32_16x16x32_bf16 v[108:111], a[160:163], a[124:127], v[108:111]\n"
"s_add_u32 s91, s91, 0x800\n"
"ds_read_b128 a[20:23], v235 offset:10240\n"
"v_mfma_f32_16x16x32_bf16 v[108:111], a[196:199], a[132:135], v[108:111]\n"
"ds_read_b128 a[56:59], v235 offset:11264\n"
"v_mfma_f32_16x16x32_bf16 v[116:119], a[164:167], a[124:127], v[116:119]\n"
"v_mfma_f32_16x16x32_bf16 v[116:119], a[200:203], a[132:135], v[116:119]\n"
"buffer_load_dwordx4 a[104:107], v226, s[56:59], s91 offen sc1\n"
"v_mfma_f32_16x16x32_bf16 v[112:115], a[164:167], a[120:123], v[112:115]\n"
"ds_read_b128 a[24:27], v235 offset:12288\n"
"v_mfma_f32_16x16x32_bf16 v[112:115], a[200:203], a[128:131], v[112:115]\n"
"ds_read_b128 a[60:63], v235 offset:13312\n"
"v_mfma_f32_16x16x32_bf16 v[120:123], a[168:171], a[120:123], v[120:123]\n"
"v_mfma_f32_16x16x32_bf16 v[120:123], a[204:207], a[128:131], v[120:123]\n"
"buffer_load_dwordx4 a[112:115], v226, s[56:59], s91 offen offset:1024 sc1\n"
"v_mfma_f32_16x16x32_bf16 v[124:127], a[168:171], a[124:127], v[124:127]\n"
"ds_read_b128 a[28:31], v235 offset:14336\n"
"v_mfma_f32_16x16x32_bf16 v[124:127], a[204:207], a[132:135], v[124:127]\n"
"ds_read_b128 a[64:67], v235 offset:15360\n"
"v_mfma_f32_16x16x32_bf16 v[132:135], a[172:175], a[124:127], v[132:135]\n"
"ds_read_b128 a[32:35], v235 offset:16384\n"
"v_mfma_f32_16x16x32_bf16 v[132:135], a[208:211], a[132:135], v[132:135]\n"
"buffer_load_dwordx4 a[108:111], v227, s[56:59], s91 offen sc1\n"
"v_mfma_f32_16x16x32_bf16 v[128:131], a[172:175], a[120:123], v[128:131]\n"
"ds_read_b128 a[68:71], v235 offset:17408\n"
"v_mfma_f32_16x16x32_bf16 v[128:131], a[208:211], a[128:131], v[128:131]\n"
"v_mfma_f32_16x16x32_bf16 v[136:139], a[176:179], a[120:123], v[136:139]\n"
"v_mfma_f32_16x16x32_bf16 v[136:139], a[212:215], a[128:131], v[136:139]\n"
"buffer_load_dwordx4 a[116:119], v227, s[56:59], s91 offen offset:1024 sc1\n"
"v_mfma_f32_16x16x32_bf16 v[140:143], a[176:179], a[124:127], v[140:143]\n"
"v_mfma_f32_16x16x32_bf16 v[140:143], a[212:215], a[132:135], v[140:143]\n"
"s_waitcnt vmcnt(18) lgkmcnt(0)\n"
"s_barrier\n"
"v_mfma_f32_16x16x32_bf16 v[72:75], a[0:3], a[232:235], v[72:75]\n"
"s_add_u32 s90, s90, 0x12000\n"
"s_add_u32 m0, s88, 0\n"
"ds_read_b128 a[144:147], v237 offset:8192\n"
"v_mfma_f32_16x16x32_bf16 v[72:75], a[36:39], a[240:243], v[72:75]\n"
"s_mov_b32 exec_hi, 0\n"
"buffer_load_dwordx4 v233, s[56:59], s90 offen sc1 lds\n"
"s_mov_b32 exec_hi, -1\n"
"v_mfma_f32_16x16x32_bf16 v[76:79], a[0:3], a[236:239], v[76:79]\n"
"ds_read_b128 a[180:183], v237 offset:9216\n"
"v_mfma_f32_16x16x32_bf16 v[76:79], a[36:39], a[244:247], v[76:79]\n"
"ds_read_b128 a[148:151], v237 offset:10240\n"
"v_mfma_f32_16x16x32_bf16 v[84:87], a[4:7], a[236:239], v[84:87]\n"
"v_mfma_f32_16x16x32_bf16 v[84:87], a[40:43], a[244:247], v[84:87]\n"
"buffer_load_dwordx4 v233, s[56:59], s90 offen offset:512 sc1 lds\n"
"v_mfma_f32_16x16x32_bf16 v[80:83], a[4:7], a[232:235], v[80:83]\n"
"ds_read_b128 a[184:187], v237 offset:11264\n"
"v_mfma_f32_16x16x32_bf16 v[80:83], a[40:43], a[240:243], v[80:83]\n"
"ds_read_b128 a[152:155], v237 offset:12288\n"
"v_mfma_f32_16x16x32_bf16 v[88:91], a[8:11], a[232:235], v[88:91]\n"
"v_mfma_f32_16x16x32_bf16 v[88:91], a[44:47], a[240:243], v[88:91]\n"
"buffer_load_dwordx4 v233, s[56:59], s90 offen offset:1536 sc1 lds\n"
"v_mfma_f32_16x16x32_bf16 v[92:95], a[8:11], a[236:239], v[92:95]\n"
"ds_read_b128 a[188:191], v237 offset:13312\n"
"v_mfma_f32_16x16x32_bf16 v[92:95], a[44:47], a[244:247], v[92:95]\n"
"ds_read_b128 a[156:159], v237 offset:14336\n"
"v_mfma_f32_16x16x32_bf16 v[100:103], a[12:15], a[236:239], v[100:103]\n"
"ds_read_b128 a[192:195], v237 offset:15360\n"
"v_mfma_f32_16x16x32_bf16 v[100:103], a[48:51], a[244:247], v[100:103]\n"
"buffer_load_dwordx4 v233, s[56:59], s90 offen offset:2560 sc1 lds\n"
"v_mfma_f32_16x16x32_bf16 v[96:99], a[12:15], a[232:235], v[96:99]\n"
"ds_read_b128 a[160:163], v237 offset:16384\n"
"v_mfma_f32_16x16x32_bf16 v[96:99], a[48:51], a[240:243], v[96:99]\n"
"v_mfma_f32_16x16x32_bf16 v[104:107], a[16:19], a[232:235], v[104:107]\n"
"ds_read_b128 a[196:199], v237 offset:17408\n"
"v_mfma_f32_16x16x32_bf16 v[104:107], a[52:55], a[240:243], v[104:107]\n"
"buffer_load_dwordx4 v233, s[56:59], s90 offen offset:3584 sc1 lds\n"
"v_mfma_f32_16x16x32_bf16 v[108:111], a[16:19], a[236:239], v[108:111]\n"
"s_add_u32 s91, s91, 0x800\n"
"ds_read_b128 a[164:167], v237 offset:18432\n"
"v_mfma_f32_16x16x32_bf16 v[108:111], a[52:55], a[244:247], v[108:111]\n"
"ds_read_b128 a[200:203], v237 offset:19456\n"
"v_mfma_f32_16x16x32_bf16 v[116:119], a[20:23], a[236:239], v[116:119]\n"
"v_mfma_f32_16x16x32_bf16 v[116:119], a[56:59], a[244:247], v[116:119]\n"
"buffer_load_dwordx4 a[120:123], v226, s[56:59], s91 offen sc1\n"
"v_mfma_f32_16x16x32_bf16 v[112:115], a[20:23], a[232:235], v[112:115]\n"
"ds_read_b128 a[168:171], v237 offset:20480\n"
"v_mfma_f32_16x16x32_bf16 v[112:115], a[56:59], a[240:243], v[112:115]\n"
"ds_read_b128 a[204:207], v237 offset:21504\n"
"v_mfma_f32_16x16x32_bf16 v[120:123], a[24:27], a[232:235], v[120:123]\n"
"v_mfma_f32_16x16x32_bf16 v[120:123], a[60:63], a[240:243], v[120:123]\n"
"buffer_load_dwordx4 a[128:131], v226, s[56:59], s91 offen offset:1024 sc1\n"
"v_mfma_f32_16x16x32_bf16 v[124:127], a[24:27], a[236:239], v[124:127]\n"
"ds_read_b128 a[172:175], v237 offset:22528\n"
"v_mfma_f32_16x16x32_bf16 v[124:127], a[60:63], a[244:247], v[124:127]\n"
"ds_read_b128 a[208:211], v237 offset:23552\n"
"v_mfma_f32_16x16x32_bf16 v[132:135], a[28:31], a[236:239], v[132:135]\n"
"ds_read_b128 a[176:179], v237 offset:24576\n"
"v_mfma_f32_16x16x32_bf16 v[132:135], a[64:67], a[244:247], v[132:135]\n"
"buffer_load_dwordx4 a[124:127], v227, s[56:59], s91 offen sc1\n"
"v_mfma_f32_16x16x32_bf16 v[128:131], a[28:31], a[232:235], v[128:131]\n"
"ds_read_b128 a[212:215], v237 offset:25600\n"
"v_mfma_f32_16x16x32_bf16 v[128:131], a[64:67], a[240:243], v[128:131]\n"
"v_mfma_f32_16x16x32_bf16 v[136:139], a[32:35], a[232:235], v[136:139]\n"
"v_mfma_f32_16x16x32_bf16 v[136:139], a[68:71], a[240:243], v[136:139]\n"
"buffer_load_dwordx4 a[132:135], v227, s[56:59], s91 offen offset:1024 sc1\n"
"v_mfma_f32_16x16x32_bf16 v[140:143], a[32:35], a[236:239], v[140:143]\n"
"v_mfma_f32_16x16x32_bf16 v[140:143], a[68:71], a[244:247], v[140:143]\n"
"s_waitcnt vmcnt(18) lgkmcnt(0)\n"
"s_barrier\n"
"v_mfma_f32_16x16x32_bf16 v[72:75], a[144:147], a[88:91], v[72:75]\n"
"s_add_u32 s84, s9, 0x20000\n"
"s_cmp_eq_u32 s94, 3\n"
"s_cselect_b32 s84, s84, 0x7ffffff0\n"
"v_mov_b32 v254, s95\n"
"v_mfma_f32_16x16x32_bf16 v[72:75], a[180:183], a[96:99], v[72:75]\n"
"buffer_store_dword v254, v234, s[56:59], s84 offen sc1\n"
"v_mfma_f32_16x16x32_bf16 v[76:79], a[144:147], a[92:95], v[76:79]\n"
"s_and_b32 s84, s9, 0xfffffe00\n"
"ds_read_b128 a[0:3], v236 offset:4096\n"
"v_and_b32 v254, 3, v224\n"
"v_mfma_f32_16x16x32_bf16 v[76:79], a[180:183], a[100:103], v[76:79]\n"
"v_lshlrev_b32 v254, 7, v254\n"
"s_add_u32 s84, s84, 0x20000\n"
"s_cmp_eq_u32 s94, 1\n"
"s_cselect_b32 s84, s84, 0x7ffffff0\n"
"v_mfma_f32_16x16x32_bf16 v[84:87], a[148:151], a[92:95], v[84:87]\n"
"buffer_load_dword v249, v254, s[56:59], s84 offen sc1\n"
"v_mfma_f32_16x16x32_bf16 v[84:87], a[184:187], a[100:103], v[84:87]\n"
"s_cmp_eq_u32 s94, 1\n"
"s_cselect_b32 s90, 0x70000000, s90\n"
"ds_read_b128 a[36:39], v236 offset:5120\n"
"v_mfma_f32_16x16x32_bf16 v[80:83], a[148:151], a[88:91], v[80:83]\n"
"s_add_u32 s90, s90, 0x12000\n"
"s_add_u32 m0, s88, 0x22000\n"
"ds_read_b128 a[4:7], v236 offset:6144\n"
"v_mfma_f32_16x16x32_bf16 v[80:83], a[184:187], a[96:99], v[80:83]\n"
"s_mov_b32 exec_hi, 0\n"
"buffer_load_dwordx4 v233, s[56:59], s90 offen sc1 lds\n"
"s_mov_b32 exec_hi, -1\n"
"v_mfma_f32_16x16x32_bf16 v[88:91], a[152:155], a[88:91], v[88:91]\n"
"ds_read_b128 a[40:43], v236 offset:7168\n"
"v_mfma_f32_16x16x32_bf16 v[88:91], a[188:191], a[96:99], v[88:91]\n"
"ds_read_b128 a[8:11], v236 offset:8192\n"
"v_mfma_f32_16x16x32_bf16 v[92:95], a[152:155], a[92:95], v[92:95]\n"
"ds_read_b128 a[44:47], v236 offset:9216\n"
"v_mfma_f32_16x16x32_bf16 v[92:95], a[188:191], a[100:103], v[92:95]\n"
"buffer_load_dwordx4 v233, s[56:59], s90 offen offset:512 sc1 lds\n"
"v_mfma_f32_16x16x32_bf16 v[100:103], a[156:159], a[92:95], v[100:103]\n"
"ds_read_b128 a[12:15], v236 offset:10240\n"
"v_mfma_f32_16x16x32_bf16 v[100:103], a[192:195], a[100:103], v[100:103]\n"
"ds_read_b128 a[48:51], v236 offset:11264\n"
"v_mfma_f32_16x16x32_bf16 v[96:99], a[156:159], a[88:91], v[96:99]\n"
"buffer_load_dwordx4 v233, s[56:59], s90 offen offset:1536 sc1 lds\n"
"v_mfma_f32_16x16x32_bf16 v[96:99], a[192:195], a[96:99], v[96:99]\n"
"ds_read_b128 a[16:19], v236 offset:12288\n"
"v_mfma_f32_16x16x32_bf16 v[104:107], a[160:163], a[88:91], v[104:107]\n"
"ds_read_b128 a[52:55], v236 offset:13312\n"
"v_mfma_f32_16x16x32_bf16 v[104:107], a[196:199], a[96:99], v[104:107]\n"
"buffer_load_dwordx4 v233, s[56:59], s90 offen offset:2560 sc1 lds\n"
"v_mfma_f32_16x16x32_bf16 v[108:111], a[160:163], a[92:95], v[108:111]\n"
"ds_read_b128 a[20:23], v236 offset:14336\n"
"v_mfma_f32_16x16x32_bf16 v[108:111], a[196:199], a[100:103], v[108:111]\n"
"ds_read_b128 a[56:59], v236 offset:15360\n"
"v_mfma_f32_16x16x32_bf16 v[116:119], a[164:167], a[92:95], v[116:119]\n"
"buffer_load_dwordx4 v233, s[56:59], s90 offen offset:3584 sc1 lds\n"
"v_mfma_f32_16x16x32_bf16 v[116:119], a[200:203], a[100:103], v[116:119]\n"
"s_add_u32 s91, s91, 0x800\n"
"ds_read_b128 a[24:27], v236 offset:16384\n"
"v_mfma_f32_16x16x32_bf16 v[112:115], a[164:167], a[88:91], v[112:115]\n"
"v_mfma_f32_16x16x32_bf16 v[112:115], a[200:203], a[96:99], v[112:115]\n"
"buffer_load_dwordx4 a[232:235], v226, s[56:59], s91 offen sc1\n"
"v_mfma_f32_16x16x32_bf16 v[120:123], a[168:171], a[88:91], v[120:123]\n"
"ds_read_b128 a[60:63], v236 offset:17408\n"
"v_mfma_f32_16x16x32_bf16 v[120:123], a[204:207], a[96:99], v[120:123]\n"
"ds_read_b128 a[28:31], v236 offset:18432\n"
"v_mfma_f32_16x16x32_bf16 v[124:127], a[168:171], a[92:95], v[124:127]\n"
"v_mfma_f32_16x16x32_bf16 v[124:127], a[204:207], a[100:103], v[124:127]\n"
"buffer_load_dwordx4 a[240:243], v226, s[56:59], s91 offen offset:1024 sc1\n"
"v_mfma_f32_16x16x32_bf16 v[132:135], a[172:175], a[92:95], v[132:135]\n"
"s_sub_u32 s94, s94, 1\n"
"ds_read_b128 a[64:67], v236 offset:19456\n"
"v_mfma_f32_16x16x32_bf16 v[132:135], a[208:211], a[100:103], v[132:135]\n"
"ds_read_b128 a[32:35], v236 offset:20480\n"
"v_mfma_f32_16x16x32_bf16 v[128:131], a[172:175], a[88:91], v[128:131]\n"
"buffer_load_dwordx4 a[236:239], v227, s[56:59], s91 offen sc1\n"
"v_mfma_f32_16x16x32_bf16 v[128:131], a[208:211], a[96:99], v[128:131]\n"
"ds_read_b128 a[68:71], v236 offset:21504\n"
"v_mfma_f32_16x16x32_bf16 v[136:139], a[176:179], a[88:91], v[136:139]\n"
"v_mfma_f32_16x16x32_bf16 v[136:139], a[212:215], a[96:99], v[136:139]\n"
"buffer_load_dwordx4 a[244:247], v227, s[56:59], s91 offen offset:1024 sc1\n"
"v_mfma_f32_16x16x32_bf16 v[140:143], a[176:179], a[92:95], v[140:143]\n"
"s_cmp_lg_u32 s94, 0\n"
"v_mfma_f32_16x16x32_bf16 v[140:143], a[212:215], a[100:103], v[140:143]\n"
"s_waitcnt vmcnt(20) lgkmcnt(0)\n"
"s_barrier\n"
"s_cbranch_scc1 L_a3\n"
"L_ct3:\n"
"v_mfma_f32_16x16x32_bf16 v[72:75], a[0:3], a[104:107], v[72:75]\n"
"ds_read_b128 a[144:147], v236 offset:38912\n"
"v_mfma_f32_16x16x32_bf16 v[72:75], a[36:39], a[112:115], v[72:75]\n"
"ds_read_b128 a[180:183], v236 offset:39936\n"
"v_mfma_f32_16x16x32_bf16 v[76:79], a[0:3], a[108:111], v[76:79]\n"
"v_mfma_f32_16x16x32_bf16 v[76:79], a[36:39], a[116:119], v[76:79]\n"
"buffer_load_dwordx4 v[180:183], v228, s[32:35], 0 offen\n"
"v_mfma_f32_16x16x32_bf16 v[84:87], a[4:7], a[108:111], v[84:87]\n"
"ds_read_b128 a[148:151], v236 offset:40960\n"
"v_mfma_f32_16x16x32_bf16 v[84:87], a[40:43], a[116:119], v[84:87]\n"
"ds_read_b128 a[184:187], v236 offset:41984\n"
"v_mfma_f32_16x16x32_bf16 v[80:83], a[4:7], a[104:107], v[80:83]\n"
"v_mfma_f32_16x16x32_bf16 v[80:83], a[40:43], a[112:115], v[80:83]\n"
"ds_read_b128 a[152:155], v236 offset:43008\n"
"v_mfma_f32_16x16x32_bf16 v[88:91], a[8:11], a[104:107], v[88:91]\n"
"v_mfma_f32_16x16x32_bf16 v[88:91], a[44:47], a[112:115], v[88:91]\n"
"ds_read_b128 a[188:191], v236 offset:44032\n"
"v_mfma_f32_16x16x32_bf16 v[92:95], a[8:11], a[108:111], v[92:95]\n"
"buffer_load_dwordx4 v[184:187], v228, s[32:35], 0 offen offset:128\n"
"v_mfma_f32_16x16x32_bf16 v[92:95], a[44:47], a[116:119], v[92:95]\n"
"ds_read_b128 a[156:159], v236 offset:45056\n"
"v_mfma_f32_16x16x32_bf16 v[100:103], a[12:15], a[108:111], v[100:103]\n"
"ds_read_b128 a[192:195], v236 offset:46080\n"
"v_mfma_f32_16x16x32_bf16 v[100:103], a[48:51], a[116:119], v[100:103]\n"
"v_mfma_f32_16x16x32_bf16 v[96:99], a[12:15], a[104:107], v[96:99]\n"
"ds_read_b128 a[160:163], v236 offset:47104\n"
"v_mfma_f32_16x16x32_bf16 v[96:99], a[48:51], a[112:115], v[96:99]\n"
"v_mfma_f32_16x16x32_bf16 v[104:107], a[16:19], a[104:107], v[104:107]\n"
"ds_read_b128 a[196:199], v236 offset:48128\n"
"v_mfma_f32_16x16x32_bf16 v[104:107], a[52:55], a[112:115], v[104:107]\n"
"buffer_load_dword v188, v229, s[32:35], 0 offen\n"
"v_mfma_f32_16x16x32_bf16 v[108:111], a[16:19], a[108:111], v[108:111]\n"
"ds_read_b128 a[164:167], v236 offset:49152\n"
"v_and_b32 v254, 15, v224\n"
"v_mfma_f32_16x16x32_bf16 v[108:111], a[52:55], a[116:119], v[108:111]\n"
"ds_read_b128 a[200:203], v236 offset:50176\n"
"v_lshlrev_b32 v254, 1, v254\n"
"v_mfma_f32_16x16x32_bf16 v[116:119], a[20:23], a[108:111], v[116:119]\n"
"s_lshl_b32 s84, s11, 2\n"
"v_mfma_f32_16x16x32_bf16 v[116:119], a[56:59], a[116:119], v[116:119]\n"
"s_add_u32 s84, s84, s78\n"
"ds_read_b128 a[168:171], v236 offset:51200\n"
"v_mfma_f32_16x16x32_bf16 v[112:115], a[20:23], a[104:107], v[112:115]\n"
"s_lshl_b32 s84, s84, 6\n"
"v_mfma_f32_16x16x32_bf16 v[112:115], a[56:59], a[112:115], v[112:115]\n"
"ds_read_b128 a[204:207], v236 offset:52224\n"
"v_mfma_f32_16x16x32_bf16 v[120:123], a[24:27], a[104:107], v[120:123]\n"
"buffer_load_ushort v190, v254, s[68:71], s84 offen\n"
"v_mfma_f32_16x16x32_bf16 v[120:123], a[60:63], a[112:115], v[120:123]\n"
"ds_read_b128 a[172:175], v236 offset:53248\n"
"v_mfma_f32_16x16x32_bf16 v[124:127], a[24:27], a[108:111], v[124:127]\n"
"v_mfma_f32_16x16x32_bf16 v[124:127], a[60:63], a[116:119], v[124:127]\n"
"ds_read_b128 a[208:211], v236 offset:54272\n"
"v_mfma_f32_16x16x32_bf16 v[132:135], a[28:31], a[108:111], v[132:135]\n"
"ds_read_b128 a[176:179], v236 offset:55296\n"
"v_mfma_f32_16x16x32_bf16 v[132:135], a[64:67], a[116:119], v[132:135]\n"
"v_mfma_f32_16x16x32_bf16 v[128:131], a[28:31], a[104:107], v[128:131]\n"
"ds_read_b128 a[212:215], v236 offset:56320\n"
"v_mfma_f32_16x16x32_bf16 v[128:131], a[64:67], a[112:115], v[128:131]\n"
"buffer_load_ushort v191, v254, s[68:71], s84 offen offset:32\n"
"v_mfma_f32_16x16x32_bf16 v[136:139], a[32:35], a[104:107], v[136:139]\n"
"v_add_u32 v196, s86, v235\n"
"v_mfma_f32_16x16x32_bf16 v[136:139], a[68:71], a[112:115], v[136:139]\n"
"s_lshl_b32 s84, s76, 2\n"
"v_mfma_f32_16x16x32_bf16 v[140:143], a[32:35], a[108:111], v[140:143]\n"
"s_add_u32 s84, s84, 0x2000\n"
"v_add_u32 v197, s84, v235\n"
"v_mfma_f32_16x16x32_bf16 v[140:143], a[68:71], a[116:119], v[140:143]\n"
"s_waitcnt vmcnt(16) lgkmcnt(0)\n"
"s_barrier\n"
"s_waitcnt vmcnt(14)\n"
"v_cmp_ne_u32 vcc, s95, v249\n"
"s_cbranch_vccnz L_slow4\n"
"L_back4:\n"
"v_mfma_f32_16x16x32_bf16 v[72:75], a[144:147], a[120:123], v[72:75]\n"
"s_add_u32 s84, s3, 0x1c28000\n"
"ds_read_b128 a[0:3], v235\n"
"v_mfma_f32_16x16x32_bf16 v[72:75], a[180:183], a[128:131], v[72:75]\n"
"buffer_load_dwordx4 v[144:147], v196, s[56:59], s84 offen sc1\n"
"v_mfma_f32_16x16x32_bf16 v[76:79], a[144:147], a[124:127], v[76:79]\n"
"s_add_u32 s84, s17, 0x1c28000\n"
"ds_read_b128 a[36:39], v235 offset:1024\n"
"v_mfma_f32_16x16x32_bf16 v[76:79], a[180:183], a[132:135], v[76:79]\n"
"ds_read_b128 a[4:7], v235 offset:2048\n"
"v_mfma_f32_16x16x32_bf16 v[84:87], a[148:151], a[124:127], v[84:87]\n"
"v_mfma_f32_16x16x32_bf16 v[84:87], a[184:187], a[132:135], v[84:87]\n"
"buffer_load_dwordx4 v[148:151], v196, s[56:59], s84 offen sc1\n"
"v_mfma_f32_16x16x32_bf16 v[80:83], a[148:151], a[120:123], v[80:83]\n"
"s_add_u32 s84, s15, 0x1c28000\n"
"ds_read_b128 a[40:43], v235 offset:3072\n"
"v_mfma_f32_16x16x32_bf16 v[80:83], a[184:187], a[128:131], v[80:83]\n"
"ds_read_b128 a[8:11], v235 offset:4096\n"
"v_mfma_f32_16x16x32_bf16 v[88:91], a[152:155], a[120:123], v[88:91]\n"
"v_mfma_f32_16x16x32_bf16 v[88:91], a[188:191], a[128:131], v[88:91]\n"
"buffer_load_dwordx4 v[152:155], v196, s[56:59], s84 offen sc1\n"
"v_mfma_f32_16x16x32_bf16 v[92:95], a[152:155], a[124:127], v[92:95]\n"
"s_add_u32 s84, s3, 0x1c29000\n"
"ds_read_b128 a[44:47], v235 offset:5120\n"
"v_mfma_f32_16x16x32_bf16 v[92:95], a[188:191], a[132:135], v[92:95]\n"
"ds_read_b128 a[12:15], v235 offset:6144\n"
"v_mfma_f32_16x16x32_bf16 v[100:103], a[156:159], a[124:127], v[100:103]\n"
"ds_read_b128 a[48:51], v235 offset:7168\n"
"v_mfma_f32_16x16x32_bf16 v[100:103], a[192:195], a[132:135], v[100:103]\n"
"buffer_load_dwordx4 v[160:163], v196, s[56:59], s84 offen sc1\n"
"v_mfma_f32_16x16x32_bf16 v[96:99], a[156:159], a[120:123], v[96:99]\n"
"s_add_u32 s84, s17, 0x1c29000\n"
"ds_read_b128 a[16:19], v235 offset:8192\n"
"v_mfma_f32_16x16x32_bf16 v[96:99], a[192:195], a[128:131], v[96:99]\n"
"v_mfma_f32_16x16x32_bf16 v[104:107], a[160:163], a[120:123], v[104:107]\n"
"ds_read_b128 a[52:55], v235 offset:9216\n"
"v_mfma_f32_16x16x32_bf16 v[104:107], a[196:199], a[128:131], v[104:107]\n"
"buffer_load_dwordx4 v[164:167], v196, s[56:59], s84 offen sc1\n"
"v_mfma_f32_16x16x32_bf16 v[108:111], a[160:163], a[124:127], v[108:111]\n"
"s_add_u32 s84, s15, 0x1c29000\n"
"ds_read_b128 a[20:23], v235 offset:10240\n"
"v_mfma_f32_16x16x32_bf16 v[108:111], a[196:199], a[132:135], v[108:111]\n"
"ds_read_b128 a[56:59], v235 offset:11264\n"
"v_mfma_f32_16x16x32_bf16 v[116:119], a[164:167], a[124:127], v[116:119]\n"
"v_mfma_f32_16x16x32_bf16 v[116:119], a[200:203], a[132:135], v[116:119]\n"
"buffer_load_dwordx4 v[168:171], v196, s[56:59], s84 offen sc1\n"
"v_mfma_f32_16x16x32_bf16 v[112:115], a[164:167], a[120:123], v[112:115]\n"
"s_add_u32 s84, s3, 0x1c28000\n"
"ds_read_b128 a[24:27], v235 offset:12288\n"
"v_mfma_f32_16x16x32_bf16 v[112:115], a[200:203], a[128:131], v[112:115]\n"
"ds_read_b128 a[60:63], v235 offset:13312\n"
"v_mfma_f32_16x16x32_bf16 v[120:123], a[168:171], a[120:123], v[120:123]\n"
"v_mfma_f32_16x16x32_bf16 v[120:123], a[204:207], a[128:131], v[120:123]\n"
"buffer_load_dword v176, v197, s[56:59], s84 offen sc1\n"
"v_mfma_f32_16x16x32_bf16 v[124:127], a[168:171], a[124:127], v[124:127]\n"
"s_add_u32 s84, s17, 0x1c28000\n"
"ds_read_b128 a[28:31], v235 offset:14336\n"
"v_mfma_f32_16x16x32_bf16 v[124:127], a[204:207], a[132:135], v[124:127]\n"
"ds_read_b128 a[64:67], v235 offset:15360\n"
"v_mfma_f32_16x16x32_bf16 v[132:135], a[172:175], a[124:127], v[132:135]\n"
"ds_read_b128 a[32:35], v235 offset:16384\n"
"v_mfma_f32_16x16x32_bf16 v[132:135], a[208:211], a[132:135], v[132:135]\n"
"buffer_load_dword v177, v197, s[56:59], s84 offen sc1\n"
"v_mfma_f32_16x16x32_bf16 v[128:131], a[172:175], a[120:123], v[128:131]\n"
"s_add_u32 s84, s15, 0x1c28000\n"
"ds_read_b128 a[68:71], v235 offset:17408\n"
"v_mfma_f32_16x16x32_bf16 v[128:131], a[208:211], a[128:131], v[128:131]\n"
"v_mfma_f32_16x16x32_bf16 v[136:139], a[176:179], a[120:123], v[136:139]\n"
"v_mfma_f32_16x16x32_bf16 v[136:139], a[212:215], a[128:131], v[136:139]\n"
"buffer_load_dword v178, v197, s[56:59], s84 offen sc1\n"
"v_mfma_f32_16x16x32_bf16 v[140:143], a[176:179], a[124:127], v[140:143]\n"
"v_mfma_f32_16x16x32_bf16 v[140:143], a[212:215], a[132:135], v[140:143]\n"
"s_waitcnt vmcnt(14) lgkmcnt(0)\n"
"s_barrier\n"
"v_mfma_f32_16x16x32_bf16 v[72:75], a[0:3], a[232:235], v[72:75]\n"
"v_mfma_f32_16x16x32_bf16 v[72:75], a[36:39], a[240:243], v[72:75]\n"
"v_mfma_f32_16x16x32_bf16 v[76:79], a[0:3], a[236:239], v[76:79]\n"
"v_mfma_f32_16x16x32_bf16 v[76:79], a[36:39], a[244:247], v[76:79]\n"
"v_mfma_f32_16x16x32_bf16 v[84:87], a[4:7], a[236:239], v[84:87]\n"
"v_mfma_f32_16x16x32_bf16 v[84:87], a[40:43], a[244:247], v[84:87]\n"
"v_mfma_f32_16x16x32_bf16 v[80:83], a[4:7], a[232:235], v[80:83]\n"
"v_mfma_f32_16x16x32_bf16 v[80:83], a[40:43], a[240:243], v[80:83]\n"
"v_mfma_f32_16x16x32_bf16 v[88:91], a[8:11], a[232:235], v[88:91]\n"
"v_mfma_f32_16x16x32_bf16 v[88:91], a[44:47], a[240:243], v[88:91]\n"
"v_mfma_f32_16x16x32_bf16 v[92:95], a[8:11], a[236:239], v[92:95]\n"
"v_mfma_f32_16x16x32_bf16 v[92:95], a[44:47], a[244:247], v[92:95]\n"
"v_mfma_f32_16x16x32_bf16 v[100:103], a[12:15], a[236:239], v[100:103]\n"
"v_mfma_f32_16x16x32_bf16 v[100:103], a[48:51], a[244:247], v[100:103]\n"
"v_mfma_f32_16x16x32_bf16 v[96:99], a[12:15], a[232:235], v[96:99]\n"
"v_mfma_f32_16x16x32_bf16 v[96:99], a[48:51], a[240:243], v[96:99]\n"
"v_mfma_f32_16x16x32_bf16 v[104:107], a[16:19], a[232:235], v[104:107]\n"
"v_mfma_f32_16x16x32_bf16 v[104:107], a[52:55], a[240:243], v[104:107]\n"
"v_mfma_f32_16x16x32_bf16 v[108:111], a[16:19], a[236:239], v[108:111]\n"
"v_mfma_f32_16x16x32_bf16 v[108:111], a[52:55], a[244:247], v[108:111]\n"
"v_mfma_f32_16x16x32_bf16 v[116:119], a[20:23], a[236:239], v[116:119]\n"
"v_mfma_f32_16x16x32_bf16 v[116:119], a[56:59], a[244:247], v[116:119]\n"
"v_mfma_f32_16x16x32_bf16 v[112:115], a[20:23], a[232:235], v[112:115]\n"
"v_mfma_f32_16x16x32_bf16 v[112:115], a[56:59], a[240:243], v[112:115]\n"
"v_mfma_f32_16x16x32_bf16 v[120:123], a[24:27], a[232:235], v[120:123]\n"
"v_mfma_f32_16x16x32_bf16 v[120:123], a[60:63], a[240:243], v[120:123]\n"
"v_mfma_f32_16x16x32_bf16 v[124:127], a[24:27], a[236:239], v[124:127]\n"
"v_mfma_f32_16x16x32_bf16 v[124:127], a[60:63], a[244:247], v[124:127]\n"
"v_mfma_f32_16x16x32_bf16 v[132:135], a[28:31], a[236:239], v[132:135]\n"
"v_mfma_f32_16x16x32_bf16 v[132:135], a[64:67], a[244:247], v[132:135]\n"
"v_mfma_f32_16x16x32_bf16 v[128:131], a[28:31], a[232:235], v[128:131]\n"
"v_mfma_f32_16x16x32_bf16 v[128:131], a[64:67], a[240:243], v[128:131]\n"
"v_mfma_f32_16x16x32_bf16 v[136:139], a[32:35], a[232:235], v[136:139]\n"
"v_mfma_f32_16x16x32_bf16 v[136:139], a[68:71], a[240:243], v[136:139]\n"
"v_mfma_f32_16x16x32_bf16 v[140:143], a[32:35], a[236:239], v[140:143]\n"
"v_mfma_f32_16x16x32_bf16 v[140:143], a[68:71], a[244:247], v[140:143]\n"
"s_nop 7\n"
"s_cmp_eq_u32 s76, s78\n"
"s_cbranch_scc1 L_own\n"
"s_add_u32 s83, s2, 0x2528000\n"
"v_cvt_pk_bf16_f32 v200, v72, v73\n"
"v_cvt_pk_bf16_f32 v201, v74, v75\n"
"v_cvt_pk_bf16_f32 v202, v76, v77\n"
"v_cvt_pk_bf16_f32 v203, v78, v79\n"
"buffer_store_dwordx4 v[200:203], v235, s[56:59], s83 offen sc1\n"
"v_cvt_pk_bf16_f32 v204, v80, v81\n"
"v_cvt_pk_bf16_f32 v205, v82, v83\n"
"v_cvt_pk_bf16_f32 v206, v84, v85\n"
"v_cvt_pk_bf16_f32 v207, v86, v87\n"
"buffer_store_dwordx4 v[204:207], v235, s[56:59], s83 offen offset:1024 sc1\n"
"v_cvt_pk_bf16_f32 v208, v88, v89\n"
"v_cvt_pk_bf16_f32 v209, v90, v91\n"
"v_cvt_pk_bf16_f32 v210, v92, v93\n"
"v_cvt_pk_bf16_f32 v211, v94, v95\n"
"buffer_store_dwordx4 v[208:211], v235, s[56:59], s83 offen offset:2048 sc1\n"
"v_cvt_pk_bf16_f32 v200, v96, v97\n"
"v_cvt_pk_bf16_f32 v201, v98, v99\n"
"v_cvt_pk_bf16_f32 v202, v100, v101\n"
"v_cvt_pk_bf16_f32 v203, v102, v103\n"
"buffer_store_dwordx4 v[200:203], v235, s[56:59], s83 offen offset:3072 sc1\n"
"v_cvt_pk_bf16_f32 v204, v104, v105\n"
"v_cvt_pk_bf16_f32 v205, v106, v107\n"
"v_cvt_pk_bf16_f32 v206, v108, v109\n"
"v_cvt_pk_bf16_f32 v207, v110, v111\n"
"s_add_u32 s83, s83, 0x1000\n"
"buffer_store_dwordx4 v[204:207], v235, s[56:59], s83 offen sc1\n"
"v_cvt_pk_bf16_f32 v208, v112, v113\n"
"v_cvt_pk_bf16_f32 v209, v114, v115\n"
"v_cvt_pk_bf16_f32 v210, v116, v117\n"
"v_cvt_pk_bf16_f32 v211, v118, v119\n"
"buffer_store_dwordx4 v[208:211], v235, s[56:59], s83 offen offset:1024 sc1\n"
"v_cvt_pk_bf16_f32 v200, v120, v121\n"
"v_cvt_pk_bf16_f32 v201, v122, v123\n"
"v_cvt_pk_bf16_f32 v202, v124, v125\n"
"v_cvt_pk_bf16_f32 v203, v126, v127\n"
"buffer_store_dwordx4 v[200:203], v235, s[56:59], s83 offen offset:2048 sc1\n"
"v_cvt_pk_bf16_f32 v204, v128, v129\n"
"v_cvt_pk_bf16_f32 v205, v130, v131\n"
"v_cvt_pk_bf16_f32 v206, v132, v133\n"
"v_cvt_pk_bf16_f32 v207, v134, v135\n"
"buffer_store_dwordx4 v[204:207], v235, s[56:59], s83 offen offset:3072 sc1\n"
"v_cvt_pk_bf16_f32 v208, v136, v137\n"
"v_cvt_pk_bf16_f32 v209, v138, v139\n"
"v_cvt_pk_bf16_f32 v210, v140, v141\n"
"v_cvt_pk_bf16_f32 v211, v142, v143\n"
"s_add_u32 s83, s83, 0x1000\n"
"buffer_store_dwordx4 v[208:211], v235, s[56:59], s83 offen sc1\n"
"L_ownx:\n"
"; own quarter written at L_own\n"
"; own quarter written at L_own\n"
"s_waitcnt vmcnt(0) lgkmcnt(0)\n"
"s_barrier\n"
"s_add_u32 s84, s9, 0x28000\n"
"v_mov_b32 v254, s95\n"
"buffer_store_dword v254, v234, s[56:59], s84 offen sc1\n"
"s_and_b32 s84, s9, 0xfffffe00\n"
"v_and_b32 v254, 3, v224\n"
"v_lshlrev_b32 v254, 7, v254\n"
"s_add_u32 s84, s84, 0x28000\n"
"buffer_load_dword v249, v254, s[56:59], s84 offen sc1\n"
"ds_read_b128 v[156:159], v196 offset:20480\n"
"ds_read_b128 v[172:175], v196 offset:24576\n"
"ds_read_b32 v179, v197 offset:20480\n"
"ds_read_b128 v[84:87], v196 offset:30720\n"
"ds_read_b128 v[100:103], v196 offset:34816\n"
"ds_read_b32 v107, v197 offset:30720\n"
"v_lshlrev_b32 v190, 16, v190\n"
"v_lshlrev_b32 v191, 16, v191\n"
"v_mov_b32 v194, 1.0\n"
"s_cmp_lt_u32 s76, 2\n"
"s_cselect_b64 vcc, -1, 0\n"
"v_cndmask_b32 v192, v191, v190, vcc\n"
"s_waitcnt lgkmcnt(0)\n"
"v_lshlrev_b32 v24, 16, v156\n"
"v_and_b32 v25, s14, v156\n"
"v_lshlrev_b32 v26, 16, v157\n"
"v_and_b32 v27, s14, v157\n"
"v_lshlrev_b32 v28, 16, v158\n"
"v_and_b32 v29, s14, v158\n"
"v_lshlrev_b32 v30, 16, v159\n"
"v_and_b32 v31, s14, v159\n"
"v_pk_add_f32 v[48:49], v[24:25], v[190:191] op_sel:[0,0] op_sel_hi:[1,0]\n"
"v_pk_add_f32 v[50:51], v[26:27], v[190:191] op_sel:[0,0] op_sel_hi:[1,0]\n"
"v_pk_add_f32 v[52:53], v[28:29], v[190:191] op_sel:[0,1] op_sel_hi:[1,1]\n"
"v_pk_add_f32 v[54:55], v[30:31], v[190:191] op_sel:[0,1] op_sel_hi:[1,1]\n"
"v_lshlrev_b32 v24, 16, v144\n"
"v_and_b32 v25, s14, v144\n"
"v_lshlrev_b32 v26, 16, v145\n"
"v_and_b32 v27, s14, v145\n"
"v_lshlrev_b32 v28, 16, v146\n"
"v_and_b32 v29, s14, v146\n"
"v_lshlrev_b32 v30, 16, v147\n"
"v_and_b32 v31, s14, v147\n"
"v_pk_add_f32 v[48:49], v[48:49], v[24:25]\n"
"v_pk_add_f32 v[50:51], v[50:51], v[26:27]\n"
"v_pk_add_f32 v[52:53], v[52:53], v[28:29]\n"
"v_pk_add_f32 v[54:55], v[54:55], v[30:31]\n"
"v_lshlrev_b32 v24, 16, v148\n"
"v_and_b32 v25, s14, v148\n"
"v_lshlrev_b32 v26, 16, v149\n"
"v_and_b32 v27, s14, v149\n"
"v_lshlrev_b32 v28, 16, v150\n"
"v_and_b32 v29, s14, v150\n"
"v_lshlrev_b32 v30, 16, v151\n"
"v_and_b32 v31, s14, v151\n"
"v_pk_add_f32 v[48:49], v[48:49], v[24:25]\n"
"v_pk_add_f32 v[50:51], v[50:51], v[26:27]\n"
"v_pk_add_f32 v[52:53], v[52:53], v[28:29]\n"
"v_pk_add_f32 v[54:55], v[54:55], v[30:31]\n"
"v_lshlrev_b32 v24, 16, v152\n"
"v_and_b32 v25, s14, v152\n"
"v_lshlrev_b32 v26, 16, v153\n"
"v_and_b32 v27, s14, v153\n"
"v_lshlrev_b32 v28, 16, v154\n"
"v_and_b32 v29, s14, v154\n"
"v_lshlrev_b32 v30, 16, v155\n"
"v_and_b32 v31, s14, v155\n"
"v_pk_add_f32 v[48:49], v[48:49], v[24:25]\n"
"v_pk_add_f32 v[50:51], v[50:51], v[26:27]\n"
"v_pk_add_f32 v[52:53], v[52:53], v[28:29]\n"
"v_pk_add_f32 v[54:55], v[54:55], v[30:31]\n"
"v_mul_f32 v24, 0xbfb8aa3b, v48\n"
"v_mul_f32 v25, 0xbfb8aa3b, v49\n"
"v_mul_f32 v26, 0xbfb8aa3b, v50\n"
"v_mul_f32 v27, 0xbfb8aa3b, v51\n"
"v_mul_f32 v28, 0xbfb8aa3b, v52\n"
"v_mul_f32 v29, 0xbfb8aa3b, v53\n"
"v_mul_f32 v30, 0xbfb8aa3b, v54\n"
"v_mul_f32 v31, 0xbfb8aa3b, v55\n"
"v_exp_f32 v24, v24\n"
"v_exp_f32 v25, v25\n"
"v_exp_f32 v26, v26\n"
"v_exp_f32 v27, v27\n"
"v_exp_f32 v28, v28\n"
"v_exp_f32 v29, v29\n"
"v_exp_f32 v30, v30\n"
"v_exp_f32 v31, v31\n"
"v_pk_add_f32 v[24:25], v[24:25], v[194:195] op_sel_hi:[1,0]\n"
"v_pk_add_f32 v[26:27], v[26:27], v[194:195] op_sel_hi:[1,0]\n"
"v_pk_add_f32 v[28:29], v[28:29], v[194:195] op_sel_hi:[1,0]\n"
"v_pk_add_f32 v[30:31], v[30:31], v[194:195] op_sel_hi:[1,0]\n"
"v_rcp_f32 v24, v24\n"
"v_rcp_f32 v25, v25\n"
"v_rcp_f32 v26, v26\n"
"v_rcp_f32 v27, v27\n"
"v_rcp_f32 v28, v28\n"
"v_rcp_f32 v29, v29\n"
"v_rcp_f32 v30, v30\n"
"v_rcp_f32 v31, v31\n"
"v_pk_mul_f32 v[48:49], v[48:49], v[24:25]\n"
"v_pk_mul_f32 v[50:51], v[50:51], v[26:27]\n"
"v_pk_mul_f32 v[52:53], v[52:53], v[28:29]\n"
"v_pk_mul_f32 v[54:55], v[54:55], v[30:31]\n"
"v_lshlrev_b32 v0, 16, v180\n"
"v_and_b32 v1, s14, v180\n"
"v_lshlrev_b32 v2, 16, v181\n"
"v_and_b32 v3, s14, v181\n"
"v_lshlrev_b32 v4, 16, v182\n"
"v_and_b32 v5, s14, v182\n"
"v_lshlrev_b32 v6, 16, v183\n"
"v_and_b32 v7, s14, v183\n"
"v_lshlrev_b32 v24, 16, v172\n"
"v_and_b32 v25, s14, v172\n"
"v_lshlrev_b32 v26, 16, v173\n"
"v_and_b32 v27, s14, v173\n"
"v_lshlrev_b32 v28, 16, v174\n"
"v_and_b32 v29, s14, v174\n"
"v_lshlrev_b32 v30, 16, v175\n"
"v_and_b32 v31, s14, v175\n"
"v_pk_add_f32 v[56:57], v[24:25], v[190:191] op_sel:[0,0] op_sel_hi:[1,0]\n"
"v_pk_add_f32 v[58:59], v[26:27], v[190:191] op_sel:[0,0] op_sel_hi:[1,0]\n"
"v_pk_add_f32 v[60:61], v[28:29], v[190:191] op_sel:[0,1] op_sel_hi:[1,1]\n"
"v_pk_add_f32 v[62:63], v[30:31], v[190:191] op_sel:[0,1] op_sel_hi:[1,1]\n"
"v_lshlrev_b32 v24, 16, v160\n"
"v_and_b32 v25, s14, v160\n"
"v_lshlrev_b32 v26, 16, v161\n"
"v_and_b32 v27, s14, v161\n"
"v_lshlrev_b32 v28, 16, v162\n"
"v_and_b32 v29, s14, v162\n"
"v_lshlrev_b32 v30, 16, v163\n"
"v_and_b32 v31, s14, v163\n"
"v_pk_add_f32 v[56:57], v[56:57], v[24:25]\n"
"v_pk_add_f32 v[58:59], v[58:59], v[26:27]\n"
"v_pk_add_f32 v[60:61], v[60:61], v[28:29]\n"
"v_pk_add_f32 v[62:63], v[62:63], v[30:31]\n"
"v_lshlrev_b32 v24, 16, v164\n"
"v_and_b32 v25, s14, v164\n"
"v_lshlrev_b32 v26, 16, v165\n"
"v_and_b32 v27, s14, v165\n"
"v_lshlrev_b32 v28, 16, v166\n"
"v_and_b32 v29, s14, v166\n"
"v_lshlrev_b32 v30, 16, v167\n"
"v_and_b32 v31, s14, v167\n"
"v_pk_add_f32 v[56:57], v[56:57], v[24:25]\n"
"v_pk_add_f32 v[58:59], v[58:59], v[26:27]\n"
"v_pk_add_f32 v[60:61], v[60:61], v[28:29]\n"
"v_pk_add_f32 v[62:63], v[62:63], v[30:31]\n"
"v_lshlrev_b32 v24, 16, v168\n"
"v_and_b32 v25, s14, v168\n"
"v_lshlrev_b32 v26, 16, v169\n"
"v_and_b32 v27, s14, v169\n"
"v_lshlrev_b32 v28, 16, v170\n"
"v_and_b32 v29, s14, v170\n"
"v_lshlrev_b32 v30, 16, v171\n"
"v_and_b32 v31, s14, v171\n"
"v_pk_add_f32 v[56:57], v[56:57], v[24:25]\n"
"v_pk_add_f32 v[58:59], v[58:59], v[26:27]\n"
"v_pk_add_f32 v[60:61], v[60:61], v[28:29]\n"
"v_pk_add_f32 v[62:63], v[62:63], v[30:31]\n"
"v_mul_f32 v24, 0xbfb8aa3b, v56\n"
"v_mul_f32 v25, 0xbfb8aa3b, v57\n"
"v_mul_f32 v26, 0xbfb8aa3b, v58\n"
"v_mul_f32 v27, 0xbfb8aa3b, v59\n"
"v_mul_f32 v28, 0xbfb8aa3b, v60\n"
"v_mul_f32 v29, 0xbfb8aa3b, v61\n"
"v_mul_f32 v30, 0xbfb8aa3b, v62\n"
"v_mul_f32 v31, 0xbfb8aa3b, v63\n"
"v_exp_f32 v24, v24\n"
"v_exp_f32 v25, v25\n"
"v_exp_f32 v26, v26\n"
"v_exp_f32 v27, v27\n"
"v_exp_f32 v28, v28\n"
"v_exp_f32 v29, v29\n"
"v_exp_f32 v30, v30\n"
"v_exp_f32 v31, v31\n"
"v_pk_add_f32 v[24:25], v[24:25], v[194:195] op_sel_hi:[1,0]\n"
"v_pk_add_f32 v[26:27], v[26:27], v[194:195] op_sel_hi:[1,0]\n"
"v_pk_add_f32 v[28:29], v[28:29], v[194:195] op_sel_hi:[1,0]\n"
"v_pk_add_f32 v[30:31], v[30:31], v[194:195] op_sel_hi:[1,0]\n"
"v_rcp_f32 v24, v24\n"
"v_rcp_f32 v25, v25\n"
"v_rcp_f32 v26, v26\n"
"v_rcp_f32 v27, v27\n"
"v_rcp_f32 v28, v28\n"
"v_rcp_f32 v29, v29\n"
"v_rcp_f32 v30, v30\n"
"v_rcp_f32 v31, v31\n"
"v_pk_mul_f32 v[56:57], v[56:57], v[24:25]\n"
"v_pk_mul_f32 v[58:59], v[58:59], v[26:27]\n"
"v_pk_mul_f32 v[60:61], v[60:61], v[28:29]\n"
"v_pk_mul_f32 v[62:63], v[62:63], v[30:31]\n"
"v_lshlrev_b32 v8, 16, v184\n"
"v_and_b32 v9, s14, v184\n"
"v_lshlrev_b32 v10, 16, v185\n"
"v_and_b32 v11, s14, v185\n"
"v_lshlrev_b32 v12, 16, v186\n"
"v_and_b32 v13, s14, v186\n"
"v_lshlrev_b32 v14, 16, v187\n"
"v_and_b32 v15, s14, v187\n"
"v_lshlrev_b32 v24, 16, v179\n"
"v_and_b32 v25, s14, v179\n"
"v_pk_add_f32 v[64:65], v[24:25], v[192:193] op_sel:[0,0] op_sel_hi:[1,0]\n"
"v_lshlrev_b32 v24, 16, v176\n"
"v_and_b32 v25, s14, v176\n"
"v_pk_add_f32 v[64:65], v[64:65], v[24:25]\n"
"v_lshlrev_b32 v24, 16, v177\n"
"v_and_b32 v25, s14, v177\n"
"v_pk_add_f32 v[64:65], v[64:65], v[24:25]\n"
"v_lshlrev_b32 v24, 16, v178\n"
"v_and_b32 v25, s14, v178\n"
"v_pk_add_f32 v[64:65], v[64:65], v[24:25]\n"
"v_mul_f32 v24, 0xbfb8aa3b, v64\n"
"v_mul_f32 v25, 0xbfb8aa3b, v65\n"
"v_exp_f32 v24, v24\n"
"v_exp_f32 v25, v25\n"
"s_nop 0\n"
"v_pk_add_f32 v[24:25], v[24:25], v[194:195] op_sel_hi:[1,0]\n"
"v_rcp_f32 v24, v24\n"
"v_rcp_f32 v25, v25\n"
"s_nop 0\n"
"v_pk_mul_f32 v[64:65], v[64:65], v[24:25]\n"
"v_lshlrev_b32 v16, 16, v188\n"
"v_and_b32 v17, s14, v188\n"
"s_waitcnt vmcnt(0)\n"
"v_cmp_ne_u32 vcc, s95, v249\n"
"s_cbranch_vccnz L_slow6\n"
"L_back6:\n"
"s_add_u32 s84, s3, 0x2528000\n"
"buffer_load_dwordx4 v[72:75], v196, s[56:59], s84 offen sc1\n"
"s_add_u32 s84, s17, 0x2528000\n"
"buffer_load_dwordx4 v[76:79], v196, s[56:59], s84 offen sc1\n"
"s_add_u32 s84, s15, 0x2528000\n"
"buffer_load_dwordx4 v[80:83], v196, s[56:59], s84 offen sc1\n"
"s_add_u32 s84, s3, 0x2529000\n"
"buffer_load_dwordx4 v[88:91], v196, s[56:59], s84 offen sc1\n"
"s_add_u32 s84, s17, 0x2529000\n"
"buffer_load_dwordx4 v[92:95], v196, s[56:59], s84 offen sc1\n"
"s_add_u32 s84, s15, 0x2529000\n"
"buffer_load_dwordx4 v[96:99], v196, s[56:59], s84 offen sc1\n"
"s_add_u32 s84, s3, 0x2528000\n"
"buffer_load_dword v104, v197, s[56:59], s84 offen sc1\n"
"s_add_u32 s84, s17, 0x2528000\n"
"buffer_load_dword v105, v197, s[56:59], s84 offen sc1\n"
"s_add_u32 s84, s15, 0x2528000\n"
"buffer_load_dword v106, v197, s[56:59], s84 offen sc1\n"
"s_waitcnt vmcnt(6)\n"
"v_lshlrev_b32 v32, 16, v84\n"
"v_and_b32 v33, s14, v84\n"
"v_lshlrev_b32 v34, 16, v85\n"
"v_and_b32 v35, s14, v85\n"
"v_lshlrev_b32 v36, 16, v86\n"
"v_and_b32 v37, s14, v86\n"
"v_lshlrev_b32 v38, 16, v87\n"
"v_and_b32 v39, s14, v87\n"
"v_lshlrev_b32 v24, 16, v72\n"
"v_and_b32 v25, s14, v72\n"
"v_lshlrev_b32 v26, 16, v73\n"
"v_and_b32 v27, s14, v73\n"
"v_lshlrev_b32 v28, 16, v74\n"
"v_and_b32 v29, s14, v74\n"
"v_lshlrev_b32 v30, 16, v75\n"
"v_and_b32 v31, s14, v75\n"
"v_pk_add_f32 v[32:33], v[32:33], v[24:25]\n"
"v_pk_add_f32 v[34:35], v[34:35], v[26:27]\n"
"v_pk_add_f32 v[36:37], v[36:37], v[28:29]\n"
"v_pk_add_f32 v[38:39], v[38:39], v[30:31]\n"
"v_lshlrev_b32 v24, 16, v76\n"
"v_and_b32 v25, s14, v76\n"
"v_lshlrev_b32 v26, 16, v77\n"
"v_and_b32 v27, s14, v77\n"
"v_lshlrev_b32 v28, 16, v78\n"
"v_and_b32 v29, s14, v78\n"
"v_lshlrev_b32 v30, 16, v79\n"
"v_and_b32 v31, s14, v79\n"
"v_pk_add_f32 v[32:33], v[32:33], v[24:25]\n"
"v_pk_add_f32 v[34:35], v[34:35], v[26:27]\n"
"v_pk_add_f32 v[36:37], v[36:37], v[28:29]\n"
"v_pk_add_f32 v[38:39], v[38:39], v[30:31]\n"
"v_lshlrev_b32 v24, 16, v80\n"
"v_and_b32 v25, s14, v80\n"
"v_lshlrev_b32 v26, 16, v81\n"
"v_and_b32 v27, s14, v81\n"
"v_lshlrev_b32 v28, 16, v82\n"
"v_and_b32 v29, s14, v82\n"
"v_lshlrev_b32 v30, 16, v83\n"
"v_and_b32 v31, s14, v83\n"
"v_pk_add_f32 v[32:33], v[32:33], v[24:25]\n"
"v_pk_add_f32 v[34:35], v[34:35], v[26:27]\n"
"v_pk_add_f32 v[36:37], v[36:37], v[28:29]\n"
"v_pk_add_f32 v[38:39], v[38:39], v[30:31]\n"
"v_pk_mul_f32 v[32:33], v[32:33], v[48:49]\n"
"v_pk_mul_f32 v[34:35], v[34:35], v[50:51]\n"
"v_pk_mul_f32 v[36:37], v[36:37], v[52:53]\n"
"v_pk_mul_f32 v[38:39], v[38:39], v[54:55]\n"
"s_nop 0\n"
"v_permlane16_swap_b32 v32, v36\n"
"v_permlane16_swap_b32 v33, v37\n"
"v_permlane16_swap_b32 v34, v38\n"
"v_permlane16_swap_b32 v35, v39\n"
"v_pk_add_f32 v[32:33], v[32:33], v[0:1]\n"
"v_pk_add_f32 v[34:35], v[34:35], v[2:3]\n"
"v_pk_add_f32 v[36:37], v[36:37], v[4:5]\n"
"v_pk_add_f32 v[38:39], v[38:39], v[6:7]\n"
"v_cvt_pk_bf16_f32 v200, v32, v33\n"
"v_cvt_pk_bf16_f32 v201, v34, v35\n"
"v_cvt_pk_bf16_f32 v202, v36, v37\n"
"v_cvt_pk_bf16_f32 v203, v38, v39\n"
"buffer_store_dwordx4 v[200:203], v228, s[60:63], 0 offen\n"
"s_waitcnt vmcnt(4)\n"
"v_lshlrev_b32 v32, 16, v100\n"
"v_and_b32 v33, s14, v100\n"
"v_lshlrev_b32 v34, 16, v101\n"
"v_and_b32 v35, s14, v101\n"
"v_lshlrev_b32 v36, 16, v102\n"
"v_and_b32 v37, s14, v102\n"
"v_lshlrev_b32 v38, 16, v103\n"
"v_and_b32 v39, s14, v103\n"
"v_lshlrev_b32 v24, 16, v88\n"
"v_and_b32 v25, s14, v88\n"
"v_lshlrev_b32 v26, 16, v89\n"
"v_and_b32 v27, s14, v89\n"
"v_lshlrev_b32 v28, 16, v90\n"
"v_and_b32 v29, s14, v90\n"
"v_lshlrev_b32 v30, 16, v91\n"
"v_and_b32 v31, s14, v91\n"
"v_pk_add_f32 v[32:33], v[32:33], v[24:25]\n"
"v_pk_add_f32 v[34:35], v[34:35], v[26:27]\n"
"v_pk_add_f32 v[36:37], v[36:37], v[28:29]\n"
"v_pk_add_f32 v[38:39], v[38:39], v[30:31]\n"
"v_lshlrev_b32 v24, 16, v92\n"
"v_and_b32 v25, s14, v92\n"
"v_lshlrev_b32 v26, 16, v93\n"
"v_and_b32 v27, s14, v93\n"
"v_lshlrev_b32 v28, 16, v94\n"
"v_and_b32 v29, s14, v94\n"
"v_lshlrev_b32 v30, 16, v95\n"
"v_and_b32 v31, s14, v95\n"
"v_pk_add_f32 v[32:33], v[32:33], v[24:25]\n"
"v_pk_add_f32 v[34:35], v[34:35], v[26:27]\n"
"v_pk_add_f32 v[36:37], v[36:37], v[28:29]\n"
"v_pk_add_f32 v[38:39], v[38:39], v[30:31]\n"
"v_lshlrev_b32 v24, 16, v96\n"
"v_and_b32 v25, s14, v96\n"
"v_lshlrev_b32 v26, 16, v97\n"
"v_and_b32 v27, s14, v97\n"
"v_lshlrev_b32 v28, 16, v98\n"
"v_and_b32 v29, s14, v98\n"
"v_lshlrev_b32 v30, 16, v99\n"
"v_and_b32 v31, s14, v99\n"
"v_pk_add_f32 v[32:33], v[32:33], v[24:25]\n"
"v_pk_add_f32 v[34:35], v[34:35], v[26:27]\n"
"v_pk_add_f32 v[36:37], v[36:37], v[28:29]\n"
"v_pk_add_f32 v[38:39], v[38:39], v[30:31]\n"
"v_pk_mul_f32 v[32:33], v[32:33], v[56:57]\n"
"v_pk_mul_f32 v[34:35], v[34:35], v[58:59]\n"
"v_pk_mul_f32 v[36:37], v[36:37], v[60:61]\n"
"v_pk_mul_f32 v[38:39], v[38:39], v[62:63]\n"
"s_nop 0\n"
"v_permlane16_swap_b32 v32, v36\n"
"v_permlane16_swap_b32 v33, v37\n"
"v_permlane16_swap_b32 v34, v38\n"
"v_permlane16_swap_b32 v35, v39\n"
"v_pk_add_f32 v[32:33], v[32:33], v[8:9]\n"
"v_pk_add_f32 v[34:35], v[34:35], v[10:11]\n"
"v_pk_add_f32 v[36:37], v[36:37], v[12:13]\n"
"v_pk_add_f32 v[38:39], v[38:39], v[14:15]\n"
"v_cvt_pk_bf16_f32 v204, v32, v33\n"
"v_cvt_pk_bf16_f32 v205, v34, v35\n"
"v_cvt_pk_bf16_f32 v206, v36, v37\n"
"v_cvt_pk_bf16_f32 v207, v38, v39\n"
"buffer_store_dwordx4 v[204:207], v228, s[60:63], 0 offen offset:128\n"
"s_waitcnt vmcnt(2)\n"
"v_lshlrev_b32 v32, 16, v107\n"
"v_and_b32 v33, s14, v107\n"
"v_lshlrev_b32 v24, 16, v104\n"
"v_and_b32 v25, s14, v104\n"
"v_pk_add_f32 v[32:33], v[32:33], v[24:25]\n"
"v_lshlrev_b32 v24, 16, v105\n"
"v_and_b32 v25, s14, v105\n"
"v_pk_add_f32 v[32:33], v[32:33], v[24:25]\n"
"v_lshlrev_b32 v24, 16, v106\n"
"v_and_b32 v25, s14, v106\n"
"v_pk_add_f32 v[32:33], v[32:33], v[24:25]\n"
"v_pk_mul_f32 v[32:33], v[32:33], v[64:65]\n"
"v_pk_add_f32 v[32:33], v[32:33], v[16:17]\n"
"v_cvt_pk_bf16_f32 v208, v32, v33\n"
"buffer_store_dword v208, v229, s[60:63], 0 offen\n"
"s_endpgm\n"
"L_nob1:\n"
"s_waitcnt vmcnt(0) lgkmcnt(0)\n"
"s_barrier\n"
"s_add_u32 s84, s8, 0x10000\n"
"v_mov_b32 v254, s95\n"
"buffer_store_dword v254, v234, s[56:59], s84 offen sc1\n"
"s_sub_u32 s82, s80, 27\n"
"s_lshl_b32 s82, s82, 2\n"
"s_add_u32 s82, s82, s76\n"
"s_lshr_b32 s81, s82, 2\n"
"s_bfe_u32 s83, s80, 0x20001\n"
"s_sub_u32 s81, s81, s83\n"
"s_lshl_b32 s81, s81, 19\n"
"s_bitcmp1_b32 s82, 1\n"
"s_cselect_b64 s[24:25], s[44:45], s[40:41]\n"
"s_bitcmp1_b32 s82, 1\n"
"s_cselect_b32 s83, 0x2000, 0\n"
"s_add_u32 s81, s81, s83\n"
"s_ashr_i32 s83, s81, 31\n"
"s_add_u32 s24, s24, s81\n"
"s_addc_u32 s25, s25, s83\n"
"s_cmp_lt_u32 s82, 16\n"
"s_cselect_b32 s26, 0x20000, 0\n"
"s_and_b32 s81, s82, 1\n"
"s_lshl_b32 s83, s79, 1\n"
"s_add_u32 s81, s81, s83\n"
"s_lshl_b32 s81, s81, 13\n"
"v_lshlrev_b32 v251, 7, v224\n"
"buffer_load_dword v250, v251, s[24:27], s81 offen\n"
"s_add_u32 s81, s81, 0x8000\n"
"buffer_load_dword v250, v251, s[24:27], s81 offen\n"
"s_add_u32 s81, s81, 0x8000\n"
"buffer_load_dword v250, v251, s[24:27], s81 offen\n"
"s_add_u32 s81, s81, 0x8000\n"
"buffer_load_dword v250, v251, s[24:27], s81 offen\n"
"s_add_u32 s81, s81, 0x8000\n"
"s_mov_b32 s90, 0\n"
"s_add_u32 m0, s86, 0x20000\n"
"s_nop 0\n"
"buffer_load_dwordx4 v244, s[40:43], s90 offen lds\n"
"s_addk_i32 m0, 4032\n"
"s_nop 0\n"
"buffer_load_dwordx4 v244, s[40:43], s90 offen offset:64 lds\n"
"s_addk_i32 m0, 4032\n"
"s_nop 0\n"
"buffer_load_dwordx4 v244, s[40:43], s90 offen offset:128 lds\n"
"s_addk_i32 m0, 4032\n"
"s_nop 0\n"
"buffer_load_dwordx4 v244, s[40:43], s90 offen offset:192 lds\n"
"s_addk_i32 m0, 4288\n"
"s_nop 0\n"
"buffer_load_dwordx4 v245, s[44:47], s90 offen lds\n"
"s_addk_i32 m0, 4032\n"
"s_nop 0\n"
"buffer_load_dwordx4 v245, s[44:47], s90 offen offset:64 lds\n"
"s_addk_i32 m0, 4032\n"
"s_nop 0\n"
"buffer_load_dwordx4 v245, s[44:47], s90 offen offset:128 lds\n"
"s_addk_i32 m0, 4032\n"
"s_nop 0\n"
"buffer_load_dwordx4 v245, s[44:47], s90 offen offset:192 lds\n"
"v_lshrrev_b32 v251, 2, v224\n"
"v_mul_u32_u24 v251, 0x480, v251\n"
"v_and_b32 v252, 3, v224\n"
"v_bfe_u32 v253, v224, 4, 1\n"
"v_lshlrev_b32 v253, 1, v253\n"
"v_xor_b32 v252, v252, v253\n"
"v_lshl_add_u32 v251, v252, 4, v251\n"
"s_and_b32 s82, s76, 1\n"
"s_mul_i32 s82, s82, 0x4800\n"
"s_lshr_b32 s83, s76, 1\n"
"s_lshl_b32 s83, s83, 6\n"
"s_add_u32 s82, s82, s83\n"
"v_add_u32 v230, s82, v251\n"
"v_lshrrev_b32 v251, 2, v224\n"
"v_mul_u32_u24 v251, 0x1000, v251\n"
"v_and_b32 v252, 3, v224\n"
"v_bfe_u32 v253, v224, 4, 1\n"
"v_lshlrev_b32 v253, 1, v253\n"
"v_xor_b32 v252, v252, v253\n"
"v_lshl_add_u32 v251, v252, 4, v251\n"
"s_lshr_b32 s82, s76, 1\n"
"s_mul_i32 s82, s82, 0x10000\n"
"s_and_b32 s83, s76, 1\n"
"s_lshl_b32 s83, s83, 7\n"
"s_add_u32 s82, s82, s83\n"
"v_add_u32 v225, s82, v251\n"
"s_lshl_b32 s83, s80, 2\n"
"s_add_u32 s83, s83, s76\n"
"s_mul_i32 s81, s83, 12\n"
"v_add_u32 v251, s81, v224\n"
"v_lshrrev_b32 v252, 4, v251\n"
"v_lshlrev_b32 v252, 12, v252\n"
"v_and_b32 v251, 15, v251\n"
"v_lshl_add_u32 v219, v251, 7, v252\n"
"v_mov_b32 v252, 0x7ffffff0\n"
"s_mul_i32 s81, s83, 0x2880\n"
"v_lshl_add_u32 v222, v224, 7, s81\n"
"v_cmp_gt_u32 vcc, 12, v224\n"
"s_nop 1\n"
"v_cndmask_b32 v219, v252, v219, vcc\n"
"v_cmp_gt_u32 vcc, 41, v224\n"
"s_nop 1\n"
"v_cndmask_b32 v222, v252, v222, vcc\n"
"s_and_b32 s81, s76, 1\n"
"s_lshl_b32 s81, s81, 5\n"
"v_xor_b32 v251, s81, v247\n"
"s_lshr_b32 s82, s76, 1\n"
"s_lshl_b32 s82, s82, 11\n"
"s_add_u32 s82, s82, 0xffff5000\n"
"v_add_u32 v216, s82, v251\n"
"s_cmp_lt_u32 s76, 2\n"
"s_cselect_b32 s82, 0, 0x7ffffff0\n"
"v_or_b32 v217, s82, v235\n"
"s_cmp_eq_u32 s7, 1\n"
"s_cselect_b32 s82, 0, 0x7ffffff0\n"
"v_or_b32 v220, s82, v235\n"
"s_lshl_b32 s11, s79, 3\n"
"s_lshr_b32 s81, s80, 2\n"
"s_add_u32 s11, s11, s81\n"
"s_and_b32 s12, s80, 3\n"
"s_lshl_b32 s81, s11, 3\n"
"s_lshl_b32 s82, s76, 1\n"
"s_add_u32 s81, s81, s82\n"
"s_lshl_b32 s81, s81, 17\n"
"s_lshl_b32 s82, s78, 15\n"
"s_add_u32 s81, s81, s82\n"
"s_add_u32 s81, s81, 0xc28000\n"
"v_add_u32 v226, s81, v235\n"
"v_add_u32 v227, 0x20000, v226\n"
"s_lshl_b32 s81, s11, 2\n"
"s_add_u32 s81, s81, s12\n"
"s_lshl_b32 s81, s81, 2\n"
"s_add_u32 s82, s81, s76\n"
"s_lshl_b32 s82, s82, 2\n"
"s_add_u32 s82, s82, s78\n"
"s_mul_i32 s2, s82, 0x2400\n"
"s_add_u32 s82, s81, s78\n"
"s_mul_i32 s82, s82, 0x9000\n"
"s_add_u32 s83, s78, 1\n"
"s_and_b32 s83, s83, 3\n"
"s_mul_i32 s83, s83, 0x2400\n"
"s_add_u32 s3, s82, s83\n"
"s_add_u32 s83, s78, 2\n"
"s_and_b32 s83, s83, 3\n"
"s_mul_i32 s83, s83, 0x2400\n"
"s_add_u32 s17, s82, s83\n"
"s_add_u32 s83, s78, 3\n"
"s_and_b32 s83, s83, 3\n"
"s_mul_i32 s83, s83, 0x2400\n"
"s_add_u32 s15, s82, s83\n"
"s_cmp_eq_u32 s76, s78\n"
"s_cselect_b32 s82, 0x7ffffff0, 0\n"
"v_or_b32 v218, s82, v235\n"
"s_mul_i32 s82, s12, 0x4800\n"
"s_mul_i32 s81, s78, 0x144000\n"
"s_add_u32 s81, s81, s82\n"
"s_add_u32 s4, s81, 0x718000\n"
"s_mul_i32 s81, s78, 0x120000\n"
"s_add_u32 s81, s81, s82\n"
"s_add_u32 s5, s81, 0x298000\n"
"s_lshl_b32 s81, s11, 7\n"
"s_lshl_b32 s82, s78, 5\n"
"s_add_u32 s81, s81, s82\n"
"s_mul_i32 s81, s81, 0x480\n"
"v_and_b32 v251, 31, v224\n"
"v_mul_u32_u24 v251, 0x480, v251\n"
"v_lshrrev_b32 v252, 5, v224\n"
"v_lshl_add_u32 v251, v252, 4, v251\n"
"s_mul_i32 s82, s12, 0x120\n"
"s_lshl_b32 s83, s76, 5\n"
"s_add_u32 s82, s82, s83\n"
"s_add_u32 s82, s82, s81\n"
"v_add_u32 v228, s82, v251\n"
"v_and_b32 v251, 15, v224\n"
"v_mul_u32_u24 v251, 0x480, v251\n"
"v_lshrrev_b32 v252, 4, v224\n"
"v_lshl_add_u32 v251, v252, 3, v251\n"
"s_mul_i32 s82, s12, 0x120\n"
"s_add_u32 s81, s81, s82\n"
"s_lshr_b32 s82, s76, 1\n"
"s_mul_i32 s82, s82, 0x4800\n"
"s_add_u32 s81, s81, s82\n"
"s_and_b32 s82, s76, 1\n"
"s_lshl_b32 s82, s82, 2\n"
"s_add_u32 s81, s81, s82\n"
"s_add_u32 s81, s81, 0x100\n"
"v_add_u32 v229, s81, v251\n"
"s_waitcnt vmcnt(0)\n"
"s_barrier\n"
"ds_read_b128 a[144:147], v246 offset:32768\n"
"ds_read_b128 a[148:151], v246 offset:36864\n"
"ds_read_b128 a[152:155], v246 offset:40960\n"
"ds_read_b128 a[156:159], v246 offset:45056\n"
"ds_read_b128 a[160:163], v246 offset:33792\n"
"ds_read_b128 a[164:167], v246 offset:37888\n"
"ds_read_b128 a[168:171], v246 offset:41984\n"
"ds_read_b128 a[172:175], v246 offset:46080\n"
"s_waitcnt lgkmcnt(0)\n"
"s_branch L_qk0\n"
"L_redo:\n"
"s_waitcnt vmcnt(0) lgkmcnt(0)\n"
"s_barrier\n"
"s_mov_b32 s97, 1\n"
"s_mov_b32 s94, 8\n"
"s_mov_b32 s91, 0\n"
"v_mov_b32 v220, 0x7ffffff0\n"
"v_mov_b32 v208, 0xff800000\n"
"v_mov_b32 v209, 0xff800000\n"
"v_mov_b32 v210, 0xff800000\n"
"v_mov_b32 v211, 0xff800000\n"
"L_rmax:\n"
"s_add_u32 m0, s86, 0x18000\n"
"s_nop 0\n"
"buffer_load_dwordx4 v244, s[40:43], s91 offen lds\n"
"s_addk_i32 m0, 4032\n"
"s_nop 0\n"
"buffer_load_dwordx4 v244, s[40:43], s91 offen offset:64 lds\n"
"s_addk_i32 m0, 4032\n"
"s_nop 0\n"
"buffer_load_dwordx4 v244, s[40:43], s91 offen offset:128 lds\n"
"s_addk_i32 m0, 4032\n"
"s_nop 0\n"
"buffer_load_dwordx4 v244, s[40:43], s91 offen offset:192 lds\n"
"s_waitcnt vmcnt(0)\n"
"s_barrier\n"
"ds_read_b128 a[144:147], v246\n"
"ds_read_b128 a[148:151], v246 offset:4096\n"
"ds_read_b128 a[152:155], v246 offset:8192\n"
"ds_read_b128 a[156:159], v246 offset:12288\n"
"ds_read_b128 a[160:163], v246 offset:1024\n"
"ds_read_b128 a[164:167], v246 offset:5120\n"
"ds_read_b128 a[168:171], v246 offset:9216\n"
"ds_read_b128 a[172:175], v246 offset:13312\n"
"s_waitcnt lgkmcnt(0)\n"
"v_mfma_f32_16x16x32_bf16 v[0:3], a[144:147], v[128:131], 0\n"
"v_mfma_f32_16x16x32_bf16 v[0:3], a[148:151], v[132:135], v[0:3]\n"
"v_mfma_f32_16x16x32_bf16 v[0:3], a[152:155], v[136:139], v[0:3]\n"
"v_mfma_f32_16x16x32_bf16 v[0:3], a[156:159], v[140:143], v[0:3]\n"
"v_mfma_f32_16x16x32_bf16 v[4:7], a[160:163], v[128:131], 0\n"
"v_mfma_f32_16x16x32_bf16 v[4:7], a[164:167], v[132:135], v[4:7]\n"
"v_mfma_f32_16x16x32_bf16 v[4:7], a[168:171], v[136:139], v[4:7]\n"
"v_mfma_f32_16x16x32_bf16 v[4:7], a[172:175], v[140:143], v[4:7]\n"
"v_mfma_f32_16x16x32_bf16 v[8:11], a[144:147], v[144:147], 0\n"
"v_mfma_f32_16x16x32_bf16 v[8:11], a[148:151], v[148:151], v[8:11]\n"
"v_mfma_f32_16x16x32_bf16 v[8:11], a[152:155], v[152:155], v[8:11]\n"
"v_mfma_f32_16x16x32_bf16 v[8:11], a[156:159], v[156:159], v[8:11]\n"
"v_mfma_f32_16x16x32_bf16 v[12:15], a[160:163], v[144:147], 0\n"
"v_mfma_f32_16x16x32_bf16 v[12:15], a[164:167], v[148:151], v[12:15]\n"
"v_mfma_f32_16x16x32_bf16 v[12:15], a[168:171], v[152:155], v[12:15]\n"
"v_mfma_f32_16x16x32_bf16 v[12:15], a[172:175], v[156:159], v[12:15]\n"
"v_mfma_f32_16x16x32_bf16 v[16:19], a[144:147], v[160:163], 0\n"
"v_mfma_f32_16x16x32_bf16 v[16:19], a[148:151], v[164:167], v[16:19]\n"
"v_mfma_f32_16x16x32_bf16 v[16:19], a[152:155], v[168:171], v[16:19]\n"
"v_mfma_f32_16x16x32_bf16 v[16:19], a[156:159], v[172:175], v[16:19]\n"
"v_mfma_f32_16x16x32_bf16 v[20:23], a[160:163], v[160:163], 0\n"
"v_mfma_f32_16x16x32_bf16 v[20:23], a[164:167], v[164:167], v[20:23]\n"
"v_mfma_f32_16x16x32_bf16 v[20:23], a[168:171], v[168:171], v[20:23]\n"
"v_mfma_f32_16x16x32_bf16 v[20:23], a[172:175], v[172:175], v[20:23]\n"
"v_mfma_f32_16x16x32_bf16 v[24:27], a[144:147], v[176:179], 0\n"
"v_mfma_f32_16x16x32_bf16 v[24:27], a[148:151], v[180:183], v[24:27]\n"
"v_mfma_f32_16x16x32_bf16 v[24:27], a[152:155], v[184:187], v[24:27]\n"
"v_mfma_f32_16x16x32_bf16 v[24:27], a[156:159], v[188:191], v[24:27]\n"
"v_mfma_f32_16x16x32_bf16 v[28:31], a[160:163], v[176:179], 0\n"
"v_mfma_f32_16x16x32_bf16 v[28:31], a[164:167], v[180:183], v[28:31]\n"
"v_mfma_f32_16x16x32_bf16 v[28:31], a[168:171], v[184:187], v[28:31]\n"
"v_mfma_f32_16x16x32_bf16 v[28:31], a[172:175], v[188:191], v[28:31]\n"
"v_max3_f32 v212, v0, v1, v2\n"
"v_max3_f32 v208, v3, v4, v208\n"
"v_max_f32 v208, v208, v5\n"
"v_max3_f32 v212, v212, v6, v7\n"
"v_max3_f32 v213, v8, v9, v10\n"
"v_max3_f32 v209, v11, v12, v209\n"
"v_max_f32 v209, v209, v13\n"
"v_max3_f32 v213, v213, v14, v15\n"
"v_max3_f32 v214, v16, v17, v18\n"
"v_max3_f32 v210, v19, v20, v210\n"
"v_max_f32 v210, v210, v21\n"
"v_max3_f32 v214, v214, v22, v23\n"
"v_max3_f32 v215, v24, v25, v26\n"
"v_max3_f32 v211, v27, v28, v211\n"
"v_max_f32 v211, v211, v29\n"
"v_max3_f32 v215, v215, v30, v31\n"
"v_max_f32 v208, v208, v212\n"
"v_max_f32 v209, v209, v213\n"
"v_max_f32 v210, v210, v214\n"
"v_max_f32 v211, v211, v215\n"
"ds_read_b128 a[144:147], v246 offset:2048\n"
"ds_read_b128 a[148:151], v246 offset:6144\n"
"ds_read_b128 a[152:155], v246 offset:10240\n"
"ds_read_b128 a[156:159], v246 offset:14336\n"
"ds_read_b128 a[160:163], v246 offset:3072\n"
"ds_read_b128 a[164:167], v246 offset:7168\n"
"ds_read_b128 a[168:171], v246 offset:11264\n"
"ds_read_b128 a[172:175], v246 offset:15360\n"
"s_waitcnt lgkmcnt(0)\n"
"v_mfma_f32_16x16x32_bf16 v[32:35], a[144:147], v[128:131], 0\n"
"v_mfma_f32_16x16x32_bf16 v[32:35], a[148:151], v[132:135], v[32:35]\n"
"v_mfma_f32_16x16x32_bf16 v[32:35], a[152:155], v[136:139], v[32:35]\n"
"v_mfma_f32_16x16x32_bf16 v[32:35], a[156:159], v[140:143], v[32:35]\n"
"v_mfma_f32_16x16x32_bf16 v[36:39], a[160:163], v[128:131], 0\n"
"v_mfma_f32_16x16x32_bf16 v[36:39], a[164:167], v[132:135], v[36:39]\n"
"v_mfma_f32_16x16x32_bf16 v[36:39], a[168:171], v[136:139], v[36:39]\n"
"v_mfma_f32_16x16x32_bf16 v[36:39], a[172:175], v[140:143], v[36:39]\n"
"v_mfma_f32_16x16x32_bf16 v[40:43], a[144:147], v[144:147], 0\n"
"v_mfma_f32_16x16x32_bf16 v[40:43], a[148:151], v[148:151], v[40:43]\n"
"v_mfma_f32_16x16x32_bf16 v[40:43], a[152:155], v[152:155], v[40:43]\n"
"v_mfma_f32_16x16x32_bf16 v[40:43], a[156:159], v[156:159], v[40:43]\n"
"v_mfma_f32_16x16x32_bf16 v[44:47], a[160:163], v[144:147], 0\n"
"v_mfma_f32_16x16x32_bf16 v[44:47], a[164:167], v[148:151], v[44:47]\n"
"v_mfma_f32_16x16x32_bf16 v[44:47], a[168:171], v[152:155], v[44:47]\n"
"v_mfma_f32_16x16x32_bf16 v[44:47], a[172:175], v[156:159], v[44:47]\n"
"v_mfma_f32_16x16x32_bf16 v[48:51], a[144:147], v[160:163], 0\n"
"v_mfma_f32_16x16x32_bf16 v[48:51], a[148:151], v[164:167], v[48:51]\n"
"v_mfma_f32_16x16x32_bf16 v[48:51], a[152:155], v[168:171], v[48:51]\n"
"v_mfma_f32_16x16x32_bf16 v[48:51], a[156:159], v[172:175], v[48:51]\n"
"v_mfma_f32_16x16x32_bf16 v[52:55], a[160:163], v[160:163], 0\n"
"v_mfma_f32_16x16x32_bf16 v[52:55], a[164:167], v[164:167], v[52:55]\n"
"v_mfma_f32_16x16x32_bf16 v[52:55], a[168:171], v[168:171], v[52:55]\n"
"v_mfma_f32_16x16x32_bf16 v[52:55], a[172:175], v[172:175], v[52:55]\n"
"v_mfma_f32_16x16x32_bf16 v[56:59], a[144:147], v[176:179], 0\n"
"v_mfma_f32_16x16x32_bf16 v[56:59], a[148:151], v[180:183], v[56:59]\n"
"v_mfma_f32_16x16x32_bf16 v[56:59], a[152:155], v[184:187], v[56:59]\n"
"v_mfma_f32_16x16x32_bf16 v[56:59], a[156:159], v[188:191], v[56:59]\n"
"v_mfma_f32_16x16x32_bf16 v[60:63], a[160:163], v[176:179], 0\n"
"v_mfma_f32_16x16x32_bf16 v[60:63], a[164:167], v[180:183], v[60:63]\n"
"v_mfma_f32_16x16x32_bf16 v[60:63], a[168:171], v[184:187], v[60:63]\n"
"v_mfma_f32_16x16x32_bf16 v[60:63], a[172:175], v[188:191], v[60:63]\n"
"v_max3_f32 v212, v32, v33, v34\n"
"v_max3_f32 v208, v35, v36, v208\n"
"v_max_f32 v208, v208, v37\n"
"v_max3_f32 v212, v212, v38, v39\n"
"v_max3_f32 v213, v40, v41, v42\n"
"v_max3_f32 v209, v43, v44, v209\n"
"v_max_f32 v209, v209, v45\n"
"v_max3_f32 v213, v213, v46, v47\n"
"v_max3_f32 v214, v48, v49, v50\n"
"v_max3_f32 v210, v51, v52, v210\n"
"v_max_f32 v210, v210, v53\n"
"v_max3_f32 v214, v214, v54, v55\n"
"v_max3_f32 v215, v56, v57, v58\n"
"v_max3_f32 v211, v59, v60, v211\n"
"v_max_f32 v211, v211, v61\n"
"v_max3_f32 v215, v215, v62, v63\n"
"v_max_f32 v208, v208, v212\n"
"v_max_f32 v209, v209, v213\n"
"v_max_f32 v210, v210, v214\n"
"v_max_f32 v211, v211, v215\n"
"s_barrier\n"
"s_add_u32 s91, s91, 0x4000\n"
"s_sub_u32 s94, s94, 1\n"
"s_cmp_lg_u32 s94, 0\n"
"s_cbranch_scc1 L_rmax\n"
"v_mov_b32 v212, v208\n"
"v_mov_b32 v213, v209\n"
"v_mov_b32 v214, v210\n"
"v_mov_b32 v215, v211\n"
"v_permlane32_swap_b32 v208, v212\n"
"v_permlane32_swap_b32 v209, v213\n"
"v_permlane32_swap_b32 v210, v214\n"
"v_permlane32_swap_b32 v211, v215\n"
"v_max_f32 v208, v208, v212\n"
"v_max_f32 v209, v209, v213\n"
"v_max_f32 v210, v210, v214\n"
"v_max_f32 v211, v211, v215\n"
"v_mov_b32 v212, v208\n"
"v_mov_b32 v213, v209\n"
"v_mov_b32 v214, v210\n"
"v_mov_b32 v215, v211\n"
"v_permlane16_swap_b32 v208, v212\n"
"v_permlane16_swap_b32 v209, v213\n"
"v_permlane16_swap_b32 v210, v214\n"
"v_permlane16_swap_b32 v211, v215\n"
"v_max_f32 v208, v208, v212\n"
"v_max_f32 v209, v209, v213\n"
"v_max_f32 v210, v210, v214\n"
"v_max_f32 v211, v211, v215\n"
"v_sub_f32 v192, 0, v208\n"
"v_sub_f32 v193, 0, v208\n"
"v_sub_f32 v194, 0, v208\n"
"v_sub_f32 v195, 0, v208\n"
"v_sub_f32 v196, 0, v209\n"
"v_sub_f32 v197, 0, v209\n"
"v_sub_f32 v198, 0, v209\n"
"v_sub_f32 v199, 0, v209\n"
"v_sub_f32 v200, 0, v210\n"
"v_sub_f32 v201, 0, v210\n"
"v_sub_f32 v202, 0, v210\n"
"v_sub_f32 v203, 0, v210\n"
"v_sub_f32 v204, 0, v211\n"
"v_sub_f32 v205, 0, v211\n"
"v_sub_f32 v206, 0, v211\n"
"v_sub_f32 v207, 0, v211\n"
"v_mov_b32 v212, s96\n"
"v_mov_b32 v213, s96\n"
"v_mov_b32 v214, s96\n"
"v_mov_b32 v215, s96\n"
"s_mov_b32 s90, 0\n"
"s_add_u32 m0, s86, 0x20000\n"
"s_nop 0\n"
"buffer_load_dwordx4 v244, s[40:43], s90 offen lds\n"
"s_addk_i32 m0, 4032\n"
"s_nop 0\n"
"buffer_load_dwordx4 v244, s[40:43], s90 offen offset:64 lds\n"
"s_addk_i32 m0, 4032\n"
"s_nop 0\n"
"buffer_load_dwordx4 v244, s[40:43], s90 offen offset:128 lds\n"
"s_addk_i32 m0, 4032\n"
"s_nop 0\n"
"buffer_load_dwordx4 v244, s[40:43], s90 offen offset:192 lds\n"
"s_addk_i32 m0, 4288\n"
"s_nop 0\n"
"buffer_load_dwordx4 v245, s[44:47], s90 offen lds\n"
"s_addk_i32 m0, 4032\n"
"s_nop 0\n"
"buffer_load_dwordx4 v245, s[44:47], s90 offen offset:64 lds\n"
"s_addk_i32 m0, 4032\n"
"s_nop 0\n"
"buffer_load_dwordx4 v245, s[44:47], s90 offen offset:128 lds\n"
"s_addk_i32 m0, 4032\n"
"s_nop 0\n"
"buffer_load_dwordx4 v245, s[44:47], s90 offen offset:192 lds\n"
"s_mov_b32 s90, 0x4000\n"
"s_add_u32 m0, s86, 0x9000\n"
"s_nop 0\n"
"buffer_load_dwordx4 v244, s[40:43], s90 offen lds\n"
"s_addk_i32 m0, 4032\n"
"s_nop 0\n"
"buffer_load_dwordx4 v244, s[40:43], s90 offen offset:64 lds\n"
"s_addk_i32 m0, 4032\n"
"s_nop 0\n"
"buffer_load_dwordx4 v244, s[40:43], s90 offen offset:128 lds\n"
"s_addk_i32 m0, 4032\n"
"s_nop 0\n"
"buffer_load_dwordx4 v244, s[40:43], s90 offen offset:192 lds\n"
"s_addk_i32 m0, 4288\n"
"s_nop 0\n"
"buffer_load_dwordx4 v245, s[44:47], s90 offen lds\n"
"s_addk_i32 m0, 4032\n"
"s_nop 0\n"
"buffer_load_dwordx4 v245, s[44:47], s90 offen offset:64 lds\n"
"s_addk_i32 m0, 4032\n"
"s_nop 0\n"
"buffer_load_dwordx4 v245, s[44:47], s90 offen offset:128 lds\n"
"s_addk_i32 m0, 4032\n"
"s_nop 0\n"
"buffer_load_dwordx4 v245, s[44:47], s90 offen offset:192 lds\n"
"s_waitcnt vmcnt(0)\n"
"s_barrier\n"
"ds_read_b128 a[144:147], v246 offset:32768\n"
"ds_read_b128 a[148:151], v246 offset:36864\n"
"ds_read_b128 a[152:155], v246 offset:40960\n"
"ds_read_b128 a[156:159], v246 offset:45056\n"
"ds_read_b128 a[160:163], v246 offset:33792\n"
"ds_read_b128 a[164:167], v246 offset:37888\n"
"ds_read_b128 a[168:171], v246 offset:41984\n"
"ds_read_b128 a[172:175], v246 offset:46080\n"
"ds_read_b128 a[176:179], v246 offset:34816\n"
"ds_read_b128 a[180:183], v246 offset:38912\n"
"ds_read_b128 a[184:187], v246 offset:43008\n"
"ds_read_b128 a[188:191], v246 offset:47104\n"
"ds_read_b128 a[192:195], v246 offset:35840\n"
"ds_read_b128 a[196:199], v246 offset:39936\n"
"ds_read_b128 a[200:203], v246 offset:44032\n"
"ds_read_b128 a[204:207], v246 offset:48128\n"
"s_waitcnt lgkmcnt(0)\n"
"v_mfma_f32_16x16x32_bf16 v[0:3], a[144:147], v[128:131], v[192:195]\n"
"v_mfma_f32_16x16x32_bf16 v[0:3], a[148:151], v[132:135], v[0:3]\n"
"v_mfma_f32_16x16x32_bf16 v[0:3], a[152:155], v[136:139], v[0:3]\n"
"v_mfma_f32_16x16x32_bf16 v[0:3], a[156:159], v[140:143], v[0:3]\n"
"v_mfma_f32_16x16x32_bf16 v[4:7], a[160:163], v[128:131], v[192:195]\n"
"v_mfma_f32_16x16x32_bf16 v[4:7], a[164:167], v[132:135], v[4:7]\n"
"v_mfma_f32_16x16x32_bf16 v[4:7], a[168:171], v[136:139], v[4:7]\n"
"v_mfma_f32_16x16x32_bf16 v[4:7], a[172:175], v[140:143], v[4:7]\n"
"v_mfma_f32_16x16x32_bf16 v[8:11], a[144:147], v[144:147], v[196:199]\n"
"v_mfma_f32_16x16x32_bf16 v[8:11], a[148:151], v[148:151], v[8:11]\n"
"v_mfma_f32_16x16x32_bf16 v[8:11], a[152:155], v[152:155], v[8:11]\n"
"v_mfma_f32_16x16x32_bf16 v[8:11], a[156:159], v[156:159], v[8:11]\n"
"v_mfma_f32_16x16x32_bf16 v[12:15], a[160:163], v[144:147], v[196:199]\n"
"v_mfma_f32_16x16x32_bf16 v[12:15], a[164:167], v[148:151], v[12:15]\n"
"v_mfma_f32_16x16x32_bf16 v[12:15], a[168:171], v[152:155], v[12:15]\n"
"v_mfma_f32_16x16x32_bf16 v[12:15], a[172:175], v[156:159], v[12:15]\n"
"v_mfma_f32_16x16x32_bf16 v[16:19], a[144:147], v[160:163], v[200:203]\n"
"v_mfma_f32_16x16x32_bf16 v[16:19], a[148:151], v[164:167], v[16:19]\n"
"v_mfma_f32_16x16x32_bf16 v[16:19], a[152:155], v[168:171], v[16:19]\n"
"v_mfma_f32_16x16x32_bf16 v[16:19], a[156:159], v[172:175], v[16:19]\n"
"v_mfma_f32_16x16x32_bf16 v[20:23], a[160:163], v[160:163], v[200:203]\n"
"v_mfma_f32_16x16x32_bf16 v[20:23], a[164:167], v[164:167], v[20:23]\n"
"v_mfma_f32_16x16x32_bf16 v[20:23], a[168:171], v[168:171], v[20:23]\n"
"v_mfma_f32_16x16x32_bf16 v[20:23], a[172:175], v[172:175], v[20:23]\n"
"v_mfma_f32_16x16x32_bf16 v[24:27], a[144:147], v[176:179], v[204:207]\n"
"v_mfma_f32_16x16x32_bf16 v[24:27], a[148:151], v[180:183], v[24:27]\n"
"v_mfma_f32_16x16x32_bf16 v[24:27], a[152:155], v[184:187], v[24:27]\n"
"v_mfma_f32_16x16x32_bf16 v[24:27], a[156:159], v[188:191], v[24:27]\n"
"v_mfma_f32_16x16x32_bf16 v[28:31], a[160:163], v[176:179], v[204:207]\n"
"v_mfma_f32_16x16x32_bf16 v[28:31], a[164:167], v[180:183], v[28:31]\n"
"v_mfma_f32_16x16x32_bf16 v[28:31], a[168:171], v[184:187], v[28:31]\n"
"v_mfma_f32_16x16x32_bf16 v[28:31], a[172:175], v[188:191], v[28:31]\n"
"s_nop 7\n"
"s_branch L_attn\n"
"L_idle:\n"
"s_waitcnt vmcnt(0)\n"
"v_readfirstlane_b32 s95, v140\n"
"v_lshlrev_b32 v142, 16, v128\n"
"v_and_b32 v143, s14, v128\n"
"v_lshlrev_b32 v144, 16, v129\n"
"v_and_b32 v145, s14, v129\n"
"v_lshlrev_b32 v146, 16, v130\n"
"v_and_b32 v147, s14, v130\n"
"v_lshlrev_b32 v148, 16, v132\n"
"v_and_b32 v149, s14, v132\n"
"v_lshlrev_b32 v150, 16, v133\n"
"v_and_b32 v151, s14, v133\n"
"v_lshlrev_b32 v152, 16, v134\n"
"v_and_b32 v153, s14, v134\n"
"v_lshlrev_b32 v154, 16, v136\n"
"v_and_b32 v155, s14, v136\n"
"v_lshlrev_b32 v156, 16, v137\n"
"v_and_b32 v157, s14, v137\n"
"v_lshlrev_b32 v158, 16, v138\n"
"v_and_b32 v159, s14, v138\n"
"s_add_u32 s95, s95, 1\n"
"v_add_f32 v160, v142, v144\n"
"v_add_f32 v161, v143, v145\n"
"v_add_f32 v160, v160, v146\n"
"v_add_f32 v161, v161, v147\n"
"v_add_f32 v160, v160, v148\n"
"v_add_f32 v161, v161, v149\n"
"v_add_f32 v160, v160, v150\n"
"v_add_f32 v161, v161, v151\n"
"v_add_f32 v160, v160, v152\n"
"v_add_f32 v161, v161, v153\n"
"v_add_f32 v160, v160, v154\n"
"v_add_f32 v161, v161, v155\n"
"v_add_f32 v160, v160, v156\n"
"v_add_f32 v161, v161, v157\n"
"v_add_f32 v160, v160, v158\n"
"v_add_f32 v161, v161, v159\n"
"v_add_f32 v160, v160, v161\n"
"s_nop 1\n"
"v_add_f32 v160, v160, v160 row_ror:8 row_mask:0xf bank_mask:0xf\n"
"s_nop 1\n"
"v_add_f32 v160, v160, v160 row_ror:4 row_mask:0xf bank_mask:0xf\n"
"s_nop 1\n"
"v_add_f32 v160, v160, v160 row_ror:2 row_mask:0xf bank_mask:0xf\n"
"s_nop 1\n"
"v_add_f32 v160, v160, v160 row_ror:1 row_mask:0xf bank_mask:0xf\n"
"v_mov_b32 v166, v160\n"
"s_nop 1\n"
"v_permlane16_swap_b32 v160, v166\n"
"v_add_f32 v160, v160, v166\n"
"v_mul_f32 v160, 0xbae38e39, v160\n"
"v_add_f32 v142, v142, v160\n"
"v_add_f32 v143, v143, v160\n"
"v_add_f32 v144, v144, v160\n"
"v_add_f32 v145, v145, v160\n"
"v_add_f32 v146, v146, v160\n"
"v_add_f32 v147, v147, v160\n"
"v_add_f32 v148, v148, v160\n"
"v_add_f32 v149, v149, v160\n"
"v_add_f32 v150, v150, v160\n"
"v_add_f32 v151, v151, v160\n"
"v_add_f32 v152, v152, v160\n"
"v_add_f32 v153, v153, v160\n"
"v_add_f32 v154, v154, v160\n"
"v_add_f32 v155, v155, v160\n"
"v_add_f32 v156, v156, v160\n"
"v_add_f32 v157, v157, v160\n"
"v_add_f32 v158, v158, v160\n"
"v_add_f32 v159, v159, v160\n"
"v_mul_f32 v162, v142, v142\n"
"v_mul_f32 v163, v143, v143\n"
"v_fmac_f32 v162, v144, v144\n"
"v_fmac_f32 v163, v145, v145\n"
"v_fmac_f32 v162, v146, v146\n"
"v_fmac_f32 v163, v147, v147\n"
"v_fmac_f32 v162, v148, v148\n"
"v_fmac_f32 v163, v149, v149\n"
"v_fmac_f32 v162, v150, v150\n"
"v_fmac_f32 v163, v151, v151\n"
"v_fmac_f32 v162, v152, v152\n"
"v_fmac_f32 v163, v153, v153\n"
"v_fmac_f32 v162, v154, v154\n"
"v_fmac_f32 v163, v155, v155\n"
"v_fmac_f32 v162, v156, v156\n"
"v_fmac_f32 v163, v157, v157\n"
"v_fmac_f32 v162, v158, v158\n"
"v_fmac_f32 v163, v159, v159\n"
"v_add_f32 v162, v162, v163\n"
"s_nop 1\n"
"v_add_f32 v162, v162, v162 row_ror:8 row_mask:0xf bank_mask:0xf\n"
"s_nop 1\n"
"v_add_f32 v162, v162, v162 row_ror:4 row_mask:0xf bank_mask:0xf\n"
"s_nop 1\n"
"v_add_f32 v162, v162, v162 row_ror:2 row_mask:0xf bank_mask:0xf\n"
"s_nop 1\n"
"v_add_f32 v162, v162, v162 row_ror:1 row_mask:0xf bank_mask:0xf\n"
"v_mov_b32 v166, v162\n"
"s_nop 1\n"
"v_permlane16_swap_b32 v162, v166\n"
"v_add_f32 v162, v162, v166\n"
"v_mov_b32 v166, 0x3727c5ac\n"
"v_fmac_f32 v166, 0x3ae38e39, v162\n"
"v_rsq_f32 v165, v166\n"
"v_and_b32 v170, 31, v224\n"
"v_mul_f32 v142, v142, v165\n"
"v_mul_f32 v143, v143, v165\n"
"v_mul_f32 v144, v144, v165\n"
"v_mul_f32 v145, v145, v165\n"
"v_mul_f32 v146, v146, v165\n"
"v_mul_f32 v147, v147, v165\n"
"v_mul_f32 v148, v148, v165\n"
"v_mul_f32 v149, v149, v165\n"
"v_mul_f32 v150, v150, v165\n"
"v_mul_f32 v151, v151, v165\n"
"v_mul_f32 v152, v152, v165\n"
"v_mul_f32 v153, v153, v165\n"
"v_mul_f32 v154, v154, v165\n"
"v_mul_f32 v155, v155, v165\n"
"v_mul_f32 v156, v156, v165\n"
"v_mul_f32 v157, v157, v165\n"
"v_mul_f32 v158, v158, v165\n"
"v_mul_f32 v159, v159, v165\n"
"v_cvt_pk_bf16_f32 v142, 0, v142\n"
"v_cvt_pk_bf16_f32 v143, 0, v143\n"
"v_cvt_pk_bf16_f32 v144, 0, v144\n"
"v_cvt_pk_bf16_f32 v145, 0, v145\n"
"v_cvt_pk_bf16_f32 v146, 0, v146\n"
"v_cvt_pk_bf16_f32 v147, 0, v147\n"
"v_cvt_pk_bf16_f32 v148, 0, v148\n"
"v_cvt_pk_bf16_f32 v149, 0, v149\n"
"v_cvt_pk_bf16_f32 v150, 0, v150\n"
"v_cvt_pk_bf16_f32 v151, 0, v151\n"
"v_cvt_pk_bf16_f32 v152, 0, v152\n"
"v_cvt_pk_bf16_f32 v153, 0, v153\n"
"v_cvt_pk_bf16_f32 v154, 0, v154\n"
"v_cvt_pk_bf16_f32 v155, 0, v155\n"
"v_cvt_pk_bf16_f32 v156, 0, v156\n"
"v_cvt_pk_bf16_f32 v157, 0, v157\n"
"v_cvt_pk_bf16_f32 v158, 0, v158\n"
"v_cvt_pk_bf16_f32 v159, 0, v159\n"
"v_mov_b32 v168, v160\n"
"v_mul_f32 v169, 0x3e0293ee, v165\n"
"v_lshrrev_b32 v171, 1, v141\n"
"v_lshlrev_b32 v171, 7, v171\n"
"v_lshrrev_b32 v172, 2, v224\n"
"v_add_u32 v171, v171, v172\n"
"v_mov_b32 v172, 0x7ffffff0\n"
"v_cmp_eq_u32 vcc, 0, v170\n"
"s_nop 1\n"
"v_cndmask_b32 v171, v172, v171, vcc\n"
"s_mov_b32 s11, 0x38000\n"
"buffer_store_dwordx2 v[168:169], v171, s[56:59], s11 offen sc1\n"
"v_add_f32 v160, v142, v144\n"
"v_add_f32 v161, v143, v145\n"
"v_add_f32 v160, v160, v146\n"
"v_add_f32 v161, v161, v147\n"
"v_add_f32 v160, v160, v148\n"
"v_add_f32 v161, v161, v149\n"
"v_add_f32 v160, v160, v150\n"
"v_add_f32 v161, v161, v151\n"
"v_add_f32 v160, v160, v152\n"
"v_add_f32 v161, v161, v153\n"
"v_add_f32 v160, v160, v154\n"
"v_add_f32 v161, v161, v155\n"
"v_add_f32 v160, v160, v156\n"
"v_add_f32 v161, v161, v157\n"
"v_add_f32 v160, v160, v158\n"
"v_add_f32 v161, v161, v159\n"
"v_add_f32 v160, v160, v161\n"
"v_mul_f32 v162, v142, v142\n"
"v_mul_f32 v163, v143, v143\n"
"v_fmac_f32 v162, v144, v144\n"
"v_fmac_f32 v163, v145, v145\n"
"v_fmac_f32 v162, v146, v146\n"
"v_fmac_f32 v163, v147, v147\n"
"v_fmac_f32 v162, v148, v148\n"
"v_fmac_f32 v163, v149, v149\n"
"v_fmac_f32 v162, v150, v150\n"
"v_fmac_f32 v163, v151, v151\n"
"v_fmac_f32 v162, v152, v152\n"
"v_fmac_f32 v163, v153, v153\n"
"v_fmac_f32 v162, v154, v154\n"
"v_fmac_f32 v163, v155, v155\n"
"v_fmac_f32 v162, v156, v156\n"
"v_fmac_f32 v163, v157, v157\n"
"v_fmac_f32 v162, v158, v158\n"
"v_fmac_f32 v163, v159, v159\n"
"v_add_f32 v162, v162, v163\n"
"v_add_f32 v160, v160, v160 row_ror:8 row_mask:0xf bank_mask:0xf\n"
"s_nop 0\n"
"v_add_f32 v162, v162, v162 row_ror:8 row_mask:0xf bank_mask:0xf\n"
"s_nop 0\n"
"v_add_f32 v160, v160, v160 row_ror:4 row_mask:0xf bank_mask:0xf\n"
"s_nop 0\n"
"v_add_f32 v162, v162, v162 row_ror:4 row_mask:0xf bank_mask:0xf\n"
"s_nop 0\n"
"v_add_f32 v160, v160, v160 row_ror:2 row_mask:0xf bank_mask:0xf\n"
"s_nop 0\n"
"v_add_f32 v162, v162, v162 row_ror:2 row_mask:0xf bank_mask:0xf\n"
"s_nop 0\n"
"v_add_f32 v160, v160, v160 row_ror:1 row_mask:0xf bank_mask:0xf\n"
"s_nop 0\n"
"v_add_f32 v162, v162, v162 row_ror:1 row_mask:0xf bank_mask:0xf\n"
"v_mov_b32 v166, v160\n"
"v_mov_b32 v167, v162\n"
"s_nop 0\n"
"v_permlane16_swap_b32 v160, v166\n"
"s_nop 0\n"
"v_permlane16_swap_b32 v162, v167\n"
"v_add_f32 v160, v160, v166\n"
"v_add_f32 v162, v162, v167\n"
"v_mul_f32 v160, 0xbae38e39, v160\n"
"v_mov_b32 v166, 0x3727c5ac\n"
"v_fmac_f32 v166, 0x3ae38e39, v162\n"
"v_fma_f32 v166, -v160, v160, v166\n"
"v_max_f32 v166, 0x3727c5ac, v166\n"
"v_rsq_f32 v165, v166\n"
"v_mul_u32_u24 v182, 24, v170\n"
"v_mul_f32 v164, v160, v165\n"
"v_fma_f32 v142, v142, v165, v164\n"
"v_fma_f32 v143, v143, v165, v164\n"
"v_fma_f32 v144, v144, v165, v164\n"
"v_fma_f32 v145, v145, v165, v164\n"
"v_fma_f32 v146, v146, v165, v164\n"
"v_fma_f32 v147, v147, v165, v164\n"
"v_fma_f32 v148, v148, v165, v164\n"
"v_fma_f32 v149, v149, v165, v164\n"
"v_fma_f32 v150, v150, v165, v164\n"
"v_fma_f32 v151, v151, v165, v164\n"
"v_fma_f32 v152, v152, v165, v164\n"
"v_fma_f32 v153, v153, v165, v164\n"
"v_fma_f32 v154, v154, v165, v164\n"
"v_fma_f32 v155, v155, v165, v164\n"
"v_fma_f32 v156, v156, v165, v164\n"
"v_fma_f32 v157, v157, v165, v164\n"
"v_fma_f32 v158, v158, v165, v164\n"
"v_fma_f32 v159, v159, v165, v164\n"
"s_mul_i32 s11, s76, 0xa00\n"
"s_add_u32 s11, s11, 0x24000\n"
"v_add_u32 v182, s11, v182\n"
"v_lshrrev_b32 v171, 5, v224\n"
"v_mul_u32_u24 v172, 0x5f4, v171\n"
"v_add_u32 v183, v182, v172\n"
"v_mul_u32_u24 v172, 0x30c, v171\n"
"v_add_u32 v182, v182, v172\n"
"v_permlane32_swap_b32 v142, v151\n"
"v_permlane32_swap_b32 v143, v152\n"
"v_permlane32_swap_b32 v144, v153\n"
"v_permlane32_swap_b32 v145, v154\n"
"v_permlane32_swap_b32 v146, v155\n"
"v_permlane32_swap_b32 v147, v156\n"
"v_permlane32_swap_b32 v148, v157\n"
"v_permlane32_swap_b32 v149, v158\n"
"v_permlane32_swap_b32 v150, v159\n"
"v_cvt_pk_bf16_f32 v173, v142, v151\n"
"v_cvt_pk_bf16_f32 v174, v143, v152\n"
"v_cvt_pk_bf16_f32 v175, v144, v153\n"
"v_cvt_pk_bf16_f32 v176, v145, v154\n"
"v_cvt_pk_bf16_f32 v177, v146, v155\n"
"v_cvt_pk_bf16_f32 v178, v147, v156\n"
"v_cvt_pk_bf16_f32 v179, v148, v157\n"
"v_cvt_pk_bf16_f32 v180, v149, v158\n"
"v_cvt_pk_bf16_f32 v181, v150, v159\n"
"ds_write_b32 v182, v173\n"
"ds_write_b32 v182, v174 offset:4\n"
"ds_write_b32 v182, v175 offset:8\n"
"ds_write_b32 v183, v176 offset:12\n"
"ds_write_b32 v183, v177 offset:16\n"
"ds_write_b32 v183, v178 offset:20\n"
"ds_write_b32 v182, v179 offset:768\n"
"ds_write_b32 v182, v180 offset:772\n"
"ds_write_b32 v182, v181 offset:776\n"
"s_waitcnt lgkmcnt(0)\n"
"s_barrier\n"
"s_lshl_b32 s11, s76, 8\n"
"s_add_u32 s11, s11, 0x24000\n"
"v_lshl_add_u32 v184, v224, 2, s11\n"
"ds_read2st64_b32 v[128:129], v184 offset0:0 offset1:10\n"
"ds_read2st64_b32 v[130:131], v184 offset0:20 offset1:30\n"
"ds_read2st64_b32 v[132:133], v184 offset0:4 offset1:14\n"
"ds_read2st64_b32 v[134:135], v184 offset0:24 offset1:34\n"
"ds_read2st64_b32 v[136:137], v184 offset0:8 offset1:18\n"
"ds_read2st64_b32 v[138:139], v184 offset0:28 offset1:38\n"
"v_lshrrev_b32 v182, 4, v224\n"
"s_lshl_b32 s11, s76, 2\n"
"v_add_u32 v182, s11, v182\n"
"v_lshlrev_b32 v182, 11, v182\n"
"v_and_b32 v183, 15, v224\n"
"v_lshl_add_u32 v182, v183, 4, v182\n"
"s_cmp_eq_u32 s76, 0\n"
"s_cselect_b32 s12, 0, 0x7ffffff0\n"
"v_or_b32 v183, s12, v182\n"
"s_lshr_b32 s11, s77, 3\n"
"s_mul_i32 s11, s11, 0x12000\n"
"s_lshl_b32 s12, s79, 10\n"
"s_add_u32 s11, s11, s12\n"
"s_lshl_b32 s12, s78, 8\n"
"s_add_u32 s11, s11, s12\n"
"s_add_u32 s11, s11, 0x58000\n"
"s_waitcnt lgkmcnt(0)\n"
"buffer_store_dwordx4 v[128:131], v182, s[56:59], s11 offen sc1\n"
"s_add_u32 s11, s11, 0x8000\n"
"buffer_store_dwordx4 v[132:135], v182, s[56:59], s11 offen sc1\n"
"s_add_u32 s11, s11, 0x8000\n"
"buffer_store_dwordx4 v[136:139], v183, s[56:59], s11 offen sc1\n"
"v_or_b32 v251, s76, v224\n"
"v_mov_b32 v252, 0x7ffffff0\n"
"v_cmp_eq_u32 vcc, 0, v251\n"
"s_nop 1\n"
"v_cndmask_b32 v234, v252, v251, vcc\n"
"s_lshl_b32 s81, s78, 6\n"
"s_lshl_b32 s82, s79, 5\n"
"s_add_u32 s82, s82, s80\n"
"s_add_u32 s81, s81, s82\n"
"s_lshl_b32 s8, s81, 7\n"
"s_mov_b32 s50, 0x1200000\n"
"s_mul_i32 s81, s79, 0x900000\n"
"s_mul_i32 s82, s78, 0x900\n"
"s_add_u32 s82, s81, s82\n"
"s_add_u32 s24, s48, s82\n"
"s_addc_u32 s25, s49, 0\n"
"s_sub_u32 s26, s50, s82\n"
"s_mov_b32 s27, 0x20000\n"
"s_waitcnt vmcnt(0)\n"
"s_barrier\n"
"s_add_u32 s84, s8, 0x8000\n"
"v_mov_b32 v254, s95\n"
"buffer_store_dword v254, v234, s[56:59], s84 offen sc1\n"
"v_mul_u32_u24 v252, 0x480, v141\n"
"v_and_b32 v253, 31, v224\n"
"v_mad_u32_u24 v252, v253, 12, v252\n"
"buffer_load_dwordx3 v[128:130], v252, s[32:35], 0 offen\n"
"buffer_load_dwordx3 v[132:134], v252, s[32:35], 0 offen offset:384\n"
"buffer_load_dwordx3 v[136:138], v252, s[32:35], 0 offen offset:768\n"
"s_sub_u32 s81, s80, 27\n"
"s_lshl_b32 s81, s81, 2\n"
"s_add_u32 s81, s81, s76\n"
"s_mul_i32 s81, s81, 63\n"
"v_min_u32 v251, 62, v224\n"
"v_add_u32 v251, s81, v251\n"
"v_mul_u32_u24 v252, 0xe39, v251\n"
"v_lshrrev_b32 v252, 16, v252\n"
"v_mul_u32_u24 v253, 18, v252\n"
"v_sub_u32 v253, v251, v253\n"
"v_mul_u32_u24 v252, 0x2400, v252\n"
"v_lshl_add_u32 v251, v253, 7, v252\n"
"s_mov_b32 s81, 0\n"
"buffer_load_dword v250, v251, s[24:27], s81 offen\n"
"s_add_u32 s81, s81, 0x9d800\n"
"buffer_load_dword v250, v251, s[24:27], s81 offen\n"
"s_add_u32 s81, s81, 0x9d800\n"
"buffer_load_dword v250, v251, s[24:27], s81 offen\n"
"s_add_u32 s81, s81, 0x9d800\n"
"buffer_load_dword v250, v251, s[24:27], s81 offen\n"
"s_add_u32 s81, s81, 0x9d800\n"
"buffer_load_dword v250, v251, s[24:27], s81 offen\n"
"s_add_u32 s81, s81, 0x9d800\n"
"buffer_load_dword v250, v251, s[24:27], s81 offen\n"
"s_add_u32 s81, s81, 0x9d800\n"
"s_waitcnt vmcnt(0)\n"
"s_branch L_q\n"
"L_slow0:\n"
"s_mov_b32 s98, 0x20000\n"
"L_spin0:\n"
"s_sleep 2\n"
"s_lshl_b32 s84, s76, 13\n"
"v_lshlrev_b32 v254, 7, v224\n"
"s_add_u32 s84, s84, 0x8000\n"
"buffer_load_dword v249, v254, s[56:59], s84 offen sc1\n"
"s_waitcnt vmcnt(0)\n"
"v_cmp_ne_u32 vcc, s95, v249\n"
"s_cbranch_vccz L_back0\n"
"s_sub_u32 s98, s98, 1\n"
"s_cbranch_scc0 L_spin0\n"
"s_branch L_back0\n"
"L_slow1:\n"
"s_mov_b32 s98, 0x20000\n"
"L_spin1:\n"
"s_sleep 2\n"
"s_xor_b32 s84, s8, 0x1000\n"
"v_mov_b32 v254, 0\n"
"s_add_u32 s84, s84, 0x30000\n"
"buffer_load_dword v249, v254, s[56:59], s84 offen sc1\n"
"s_waitcnt vmcnt(0)\n"
"v_cmp_ne_u32 vcc, s95, v249\n"
"s_cbranch_vccz L_back1\n"
"s_sub_u32 s98, s98, 1\n"
"s_cbranch_scc0 L_spin1\n"
"s_branch L_back1\n"
"L_slow2:\n"
"s_mov_b32 s98, 0x20000\n"
"L_spin2:\n"
"s_sleep 2\n"
"s_lshl_b32 s84, s78, 2\n"
"s_add_u32 s84, s84, s76\n"
"s_lshl_b32 s84, s84, 11\n"
"v_and_b32 v254, 15, v224\n"
"v_lshlrev_b32 v254, 7, v254\n"
"s_add_u32 s84, s84, 0x10000\n"
"buffer_load_dword v249, v254, s[56:59], s84 offen sc1\n"
"s_waitcnt vmcnt(0)\n"
"v_cmp_ne_u32 vcc, s95, v249\n"
"s_cbranch_vccz L_back2\n"
"s_sub_u32 s98, s98, 1\n"
"s_cbranch_scc0 L_spin2\n"
"s_branch L_back2\n"
"L_slow3:\n"
"s_mov_b32 s98, 0x20000\n"
"L_spin3:\n"
"s_sleep 2\n"
"s_lshl_b32 s84, s78, 2\n"
"s_add_u32 s84, s84, s76\n"
"s_lshl_b32 s84, s84, 11\n"
"v_and_b32 v254, 15, v224\n"
"v_lshlrev_b32 v254, 7, v254\n"
"s_add_u32 s84, s84, 0x18000\n"
"buffer_load_dword v249, v254, s[56:59], s84 offen sc1\n"
"s_waitcnt vmcnt(0)\n"
"v_cmp_ne_u32 vcc, s95, v249\n"
"s_cbranch_vccz L_back3\n"
"s_sub_u32 s98, s98, 1\n"
"s_cbranch_scc0 L_spin3\n"
"s_branch L_back3\n"
"L_slow4:\n"
"s_mov_b32 s98, 0x20000\n"
"L_spin4:\n"
"s_sleep 2\n"
"s_and_b32 s84, s9, 0xfffffe00\n"
"v_and_b32 v254, 3, v224\n"
"v_lshlrev_b32 v254, 7, v254\n"
"s_add_u32 s84, s84, 0x20000\n"
"buffer_load_dword v249, v254, s[56:59], s84 offen sc1\n"
"s_waitcnt vmcnt(0)\n"
"v_cmp_ne_u32 vcc, s95, v249\n"
"s_cbranch_vccz L_back4\n"
"s_sub_u32 s98, s98, 1\n"
"s_cbranch_scc0 L_spin4\n"
"s_branch L_back4\n"
"L_own:\n"
"s_nop 1\n"
"v_cvt_pk_bf16_f32 v200, v72, v73\n"
"v_cvt_pk_bf16_f32 v201, v74, v75\n"
"v_cvt_pk_bf16_f32 v202, v76, v77\n"
"v_cvt_pk_bf16_f32 v203, v78, v79\n"
"ds_write_b128 v235, v[200:203] offset:30720\n"
"v_cvt_pk_bf16_f32 v204, v80, v81\n"
"v_cvt_pk_bf16_f32 v205, v82, v83\n"
"v_cvt_pk_bf16_f32 v206, v84, v85\n"
"v_cvt_pk_bf16_f32 v207, v86, v87\n"
"ds_write_b128 v235, v[204:207] offset:31744\n"
"v_cvt_pk_bf16_f32 v208, v88, v89\n"
"v_cvt_pk_bf16_f32 v209, v90, v91\n"
"v_cvt_pk_bf16_f32 v210, v92, v93\n"
"v_cvt_pk_bf16_f32 v211, v94, v95\n"
"ds_write_b128 v235, v[208:211] offset:32768\n"
"v_cvt_pk_bf16_f32 v200, v96, v97\n"
"v_cvt_pk_bf16_f32 v201, v98, v99\n"
"v_cvt_pk_bf16_f32 v202, v100, v101\n"
"v_cvt_pk_bf16_f32 v203, v102, v103\n"
"ds_write_b128 v235, v[200:203] offset:33792\n"
"v_cvt_pk_bf16_f32 v204, v104, v105\n"
"v_cvt_pk_bf16_f32 v205, v106, v107\n"
"v_cvt_pk_bf16_f32 v206, v108, v109\n"
"v_cvt_pk_bf16_f32 v207, v110, v111\n"
"ds_write_b128 v235, v[204:207] offset:34816\n"
"v_cvt_pk_bf16_f32 v208, v112, v113\n"
"v_cvt_pk_bf16_f32 v209, v114, v115\n"
"v_cvt_pk_bf16_f32 v210, v116, v117\n"
"v_cvt_pk_bf16_f32 v211, v118, v119\n"
"ds_write_b128 v235, v[208:211] offset:35840\n"
"v_cvt_pk_bf16_f32 v200, v120, v121\n"
"v_cvt_pk_bf16_f32 v201, v122, v123\n"
"v_cvt_pk_bf16_f32 v202, v124, v125\n"
"v_cvt_pk_bf16_f32 v203, v126, v127\n"
"ds_write_b128 v235, v[200:203] offset:36864\n"
"v_cvt_pk_bf16_f32 v204, v128, v129\n"
"v_cvt_pk_bf16_f32 v205, v130, v131\n"
"v_cvt_pk_bf16_f32 v206, v132, v133\n"
"v_cvt_pk_bf16_f32 v207, v134, v135\n"
"ds_write_b128 v235, v[204:207] offset:37888\n"
"v_cvt_pk_bf16_f32 v208, v136, v137\n"
"v_cvt_pk_bf16_f32 v209, v138, v139\n"
"v_cvt_pk_bf16_f32 v210, v140, v141\n"
"v_cvt_pk_bf16_f32 v211, v142, v143\n"
"ds_write_b128 v235, v[208:211] offset:38912\n"
"v_cvt_pk_bf16_f32 v200, v0, v1\n"
"v_cvt_pk_bf16_f32 v201, v2, v3\n"
"v_cvt_pk_bf16_f32 v202, v4, v5\n"
"v_cvt_pk_bf16_f32 v203, v6, v7\n"
"ds_write_b128 v235, v[200:203] offset:20480\n"
"v_cvt_pk_bf16_f32 v204, v8, v9\n"
"v_cvt_pk_bf16_f32 v205, v10, v11\n"
"v_cvt_pk_bf16_f32 v206, v12, v13\n"
"v_cvt_pk_bf16_f32 v207, v14, v15\n"
"ds_write_b128 v235, v[204:207] offset:21504\n"
"v_cvt_pk_bf16_f32 v208, v16, v17\n"
"v_cvt_pk_bf16_f32 v209, v18, v19\n"
"v_cvt_pk_bf16_f32 v210, v20, v21\n"
"v_cvt_pk_bf16_f32 v211, v22, v23\n"
"ds_write_b128 v235, v[208:211] offset:22528\n"
"v_cvt_pk_bf16_f32 v200, v24, v25\n"
"v_cvt_pk_bf16_f32 v201, v26, v27\n"
"v_cvt_pk_bf16_f32 v202, v28, v29\n"
"v_cvt_pk_bf16_f32 v203, v30, v31\n"
"ds_write_b128 v235, v[200:203] offset:23552\n"
"v_cvt_pk_bf16_f32 v204, v32, v33\n"
"v_cvt_pk_bf16_f32 v205, v34, v35\n"
"v_cvt_pk_bf16_f32 v206, v36, v37\n"
"v_cvt_pk_bf16_f32 v207, v38, v39\n"
"ds_write_b128 v235, v[204:207] offset:24576\n"
"v_cvt_pk_bf16_f32 v208, v40, v41\n"
"v_cvt_pk_bf16_f32 v209, v42, v43\n"
"v_cvt_pk_bf16_f32 v210, v44, v45\n"
"v_cvt_pk_bf16_f32 v211, v46, v47\n"
"ds_write_b128 v235, v[208:211] offset:25600\n"
"v_cvt_pk_bf16_f32 v200, v48, v49\n"
"v_cvt_pk_bf16_f32 v201, v50, v51\n"
"v_cvt_pk_bf16_f32 v202, v52, v53\n"
"v_cvt_pk_bf16_f32 v203, v54, v55\n"
"ds_write_b128 v235, v[200:203] offset:26624\n"
"v_cvt_pk_bf16_f32 v204, v56, v57\n"
"v_cvt_pk_bf16_f32 v205, v58, v59\n"
"v_cvt_pk_bf16_f32 v206, v60, v61\n"
"v_cvt_pk_bf16_f32 v207, v62, v63\n"
"ds_write_b128 v235, v[204:207] offset:27648\n"
"v_cvt_pk_bf16_f32 v208, v64, v65\n"
"v_cvt_pk_bf16_f32 v209, v66, v67\n"
"v_cvt_pk_bf16_f32 v210, v68, v69\n"
"v_cvt_pk_bf16_f32 v211, v70, v71\n"
"ds_write_b128 v235, v[208:211] offset:28672\n"
"s_branch L_ownx\n"
"L_slow6:\n"
"s_mov_b32 s98, 0x20000\n"
"L_spin6:\n"
"s_and_b32 s84, s9, 0xfffffe00\n"
"v_and_b32 v254, 3, v224\n"
"v_lshlrev_b32 v254, 7, v254\n"
"s_add_u32 s84, s84, 0x28000\n"
"buffer_load_dword v249, v254, s[56:59], s84 offen sc1\n"
"s_waitcnt vmcnt(0)\n"
"v_cmp_ne_u32 vcc, s95, v249\n"
"s_cbranch_vccz L_back6\n"
"s_sub_u32 s98, s98, 1\n"
"s_cbranch_scc0 L_spin6\n"
"s_branch L_back6\n"
"L_end:\n"
  :: "s"(ka), "s"(bid), "v"(tid), "s"(ldsb)
  : "v1","v2","v3","v4","v5","v6","v7","v8","v9","v10","v11","v12","v13","v14","v15","v16","v17","v18","v19","v20","v21","v22","v23","v24","v25","v26","v27","v28","v29","v30","v31","v32","v33","v34","v35","v36","v37","v38","v39","v40","v41","v42","v43","v44","v45","v46","v47","v48","v49","v50","v51","v52","v53","v54","v55","v56","v57","v58","v59","v60","v61","v62","v63","v64","v65","v66","v67","v68","v69","v70","v71","v72","v73","v74","v75","v76","v77","v78","v79","v80","v81","v82","v83","v84","v85","v86","v87","v88","v89","v90","v91","v92","v93","v94","v95","v96","v97","v98","v99","v100","v101","v102","v103","v104","v105","v106","v107","v108","v109","v110","v111","v112","v113","v114","v115","v116","v117","v118","v119","v120","v121","v122","v123","v124","v125","v126","v127","v128","v129","v130","v131","v132","v133","v134","v135","v136","v137","v138","v139","v140","v141","v142","v143","v144","v145","v146","v147","v148","v149","v150","v151","v152","v153","v154","v155","v156","v157","v158","v159","v160","v161","v162","v163","v164","v165","v166","v167","v168","v169","v170","v171","v172","v173","v174","v175","v176","v177","v178","v179","v180","v181","v182","v183","v184","v185","v186","v187","v188","v189","v190","v191","v192","v193","v194","v195","v196","v197","v198","v199","v200","v201","v202","v203","v204","v205","v206","v207","v208","v209","v210","v211","v212","v213","v214","v215","v216","v217","v218","v219","v220","v221","v222","v223","v224","v225","v226","v227","v228","v229","v230","v231","v232","v233","v234","v235","v236","v237","v238","v239","v240","v241","v242","v243","v244","v245","v246","v247","v248","v249","v250","v251","v252","v253","v254","v255","a0","a1","a2","a3","a4","a5","a6","a7","a8","a9","a10","a11","a12","a13","a14","a15","a16","a17","a18","a19","a20","a21","a22","a23","a24","a25","a26","a27","a28","a29","a30","a31","a32","a33","a34","a35","a36","a37","a38","a39","a40","a41","a42","a43","a44","a45","a46","a47","a48","a49","a50","a51","a52","a53","a54","a55","a56","a57","a58","a59","a60","a61","a62","a63","a64","a65","a66","a67","a68","a69","a70","a71","a72","a73","a74","a75","a76","a77","a78","a79","a80","a81","a82","a83","a84","a85","a86","a87","a88","a89","a90","a91","a92","a93","a94","a95","a96","a97","a98","a99","a100","a101","a102","a103","a104","a105","a106","a107","a108","a109","a110","a111","a112","a113","a114","a115","a116","a117","a118","a119","a120","a121","a122","a123","a124","a125","a126","a127","a128","a129","a130","a131","a132","a133","a134","a135","a136","a137","a138","a139","a140","a141","a142","a143","a144","a145","a146","a147","a148","a149","a150","a151","a152","a153","a154","a155","a156","a157","a158","a159","a160","a161","a162","a163","a164","a165","a166","a167","a168","a169","a170","a171","a172","a173","a174","a175","a176","a177","a178","a179","a180","a181","a182","a183","a184","a185","a186","a187","a188","a189","a190","a191","a192","a193","a194","a195","a196","a197","a198","a199","a200","a201","a202","a203","a204","a205","a206","a207","a208","a209","a210","a211","a212","a213","a214","a215","a216","a217","a218","a219","a220","a221","a222","a223","a224","a225","a226","a227","a228","a229","a230","a231","a232","a233","a234","a235","a236","a237","a238","a239","a240","a241","a242","a243","a244","a245","a246","a247","a248","a249","a250","a251","a252","a253","a254","a255","s8","s9","s10","s11","s12","s13","s14","s15","s16","s17","s18","s19","s20","s21","s22","s23","s24","s25","s26","s27","s28","s29","s30","s31","s32","s33","s34","s35","s36","s37","s38","s39","s40","s41","s42","s43","s44","s45","s46","s47","s48","s49","s50","s51","s52","s53","s54","s55","s56","s57","s58","s59","s60","s61","s62","s63","s64","s65","s66","s67","s68","s69","s70","s71","s72","s73","s74","s75","s76","s77","s78","s79","s80","s81","s82","s83","s84","s85","s86","s87","s88","s89","s90","s91","s92","s93","s94","s95","s96","s97","s98","s99","vcc","scc","memory");
}
extern "C" void kernel_launch(void* const* d_in, const int* in_sizes, int n_in, void* d_out, int out_size, void* d_ws, size_t ws_size, hipStream_t stream) {
  fused_main<<<256,256,0,stream>>>(d_in[0],d_in[1],d_in[2],d_in[3],d_in[4],d_in[6],d_in[7],d_in[8],d_in[9],d_out,d_ws);
}
```
